```python
import torch

def _softmax(x, axis=-1):
    return torch.softmax(x, dim=axis)
def _gelu(x):
    return torch.nn.functional.gelu(x, approximate="tanh")
def _silu(x):
    return torch.nn.functional.silu(x)
def _sigmoid(x):
    return torch.sigmoid(x)
def _layernorm(x, axis=-1, eps=1e-5):
    m = x.mean(dim=axis, keepdim=True)
    v = x.var(dim=axis, keepdim=True, unbiased=False)
    return (x - m) / torch.sqrt(v + eps)
def _scatter_add(tgt, idx, src, axis=0):
    return tgt.clone().index_add_(axis, idx, src)
def _segment_sum(src, seg, n):
    out = torch.zeros((n,) + src.shape[1:], dtype=src.dtype)
    return out.index_add_(0, seg, src)
def _pad(x, pw):
    flat = tuple(v for pair in reversed(pw) for v in pair)
    return torch.nn.functional.pad(x, flat)

kernel_name = 'standalone__gqa_block_cached__near_ridge'

def setup_inputs():

    _w = (lambda a: (lambda t: t.to(torch.bfloat16) if t.is_floating_point() else t)(torch.as_tensor(a)))
    B, Sq, D = 32, 32, 512
    Hq, Hkv, Sk, Dh = 32, 8, 512, 128
    assert Hq % Hkv == 0, (Hq, Hkv)
    return {
        "x": _w((lambda *s: torch.randn(*s, dtype=torch.bfloat16) * 0.8)(B, Sq, D)),
        "k_cache": _w((lambda *s: torch.randn(*s, dtype=torch.bfloat16) * 0.8)(B, Hkv, Sk, Dh)),
        "v_cache": _w((lambda *s: torch.randn(*s, dtype=torch.bfloat16) * 0.05)(B, Hkv, Sk, Dh)),
        "wq": _w((lambda *s: torch.randn(*s, dtype=torch.bfloat16) * 0.8)(D, Hq * Dh) * (D ** -0.5)),
        "wo": _w((lambda *s: torch.randn(*s, dtype=torch.bfloat16) * 0.05)(Hq * Dh, D) * ((Hq * Dh) ** -0.5)),
    }

def reference(x, k_cache, v_cache, wq, wo):
    B, Sq, D = x.shape
    Hq, Dh = 32, 128
    Hkv = k_cache.shape[1]
    rep = Hq // Hkv
    q = (lambda x, *d: x.permute(*d))((x @ wq).reshape(B, Sq, Hq, Dh), 0, 2, 1, 3)
    k = (lambda x, n, axis: torch.repeat_interleave(x, n, dim=axis))(k_cache, rep, axis=1)
    v = (lambda x, n, axis: torch.repeat_interleave(x, n, dim=axis))(v_cache, rep, axis=1)
    scores = torch.einsum("bhqd,bhkd->bhqk", q, k) * (Dh ** -0.5)
    p = _softmax(scores, axis=-1)
    o = torch.einsum("bhqk,bhkd->bhqd", p, v)
    return (lambda x, *d: x.permute(*d))(o, 0, 2, 1, 3).reshape(B, Sq, Hq * Dh) @ wo
```

```cpp
#include <hip/hip_runtime.h>
#include <hip/hip_bf16.h>

typedef __bf16 bf16x8 __attribute__((ext_vector_type(8)));
typedef __bf16 bf16x2 __attribute__((ext_vector_type(2)));
typedef short s16x4 __attribute__((ext_vector_type(4)));
typedef float f32x2 __attribute__((ext_vector_type(2)));
typedef float f32x4 __attribute__((ext_vector_type(4)));
typedef float f32x16 __attribute__((ext_vector_type(16)));
typedef unsigned u32x2 __attribute__((ext_vector_type(2)));
typedef unsigned u32x4 __attribute__((ext_vector_type(4)));
typedef __attribute__((address_space(3))) s16x4* lds_s16x4_ptr;

#define DI __device__ __forceinline__
#define MFMA32(a, b, c) __builtin_amdgcn_mfma_f32_32x32x16_bf16((a), (b), (c), 0, 0, 0)
#define MFMA16(a, b, c) __builtin_amdgcn_mfma_f32_16x16x32_bf16((a), (b), (c), 0, 0, 0)

constexpr int NB = 32, SQ = 32, DM = 512, HQ = 32, HKV = 8, SK = 512, DH = 128;
constexpr int LDS_BYTES = 163840;
constexpr int X_OFF = 131072;

DI unsigned pk2(float a, float b) {
  f32x2 f = {a, b};
  return __builtin_bit_cast(unsigned, __builtin_convertvector(f, bf16x2));
}
DI float swap_half_max(float v) {
  auto rr = __builtin_amdgcn_permlane32_swap(__float_as_uint(v), __float_as_uint(v), false, false);
  return fmaxf(__uint_as_float(rr[0]), __uint_as_float(rr[1]));
}
DI float swap_half_add(float v) {
  auto rr = __builtin_amdgcn_permlane32_swap(__float_as_uint(v), __float_as_uint(v), false, false);
  return __uint_as_float(rr[0]) + __uint_as_float(rr[1]);
}
DI void wg_barrier() {
  asm volatile("s_waitcnt lgkmcnt(0)" ::: "memory");
  __builtin_amdgcn_s_barrier();
  asm volatile("" ::: "memory");
}
DI void glds16(const void* g, char* l) {
  __builtin_amdgcn_global_load_lds((const __attribute__((address_space(1))) void*)g,
                                   (__attribute__((address_space(3))) void*)l, 16, 0, 0);
}
template <int I>
DI bf16x8 tr_frag(const u32x2* w) {
  u32x4 o;
  constexpr unsigned sel = (I & 1) ? 0x07060302u : 0x05040100u;
#pragma unroll
  for (int m = 0; m < 4; ++m) o[m] = __builtin_amdgcn_perm(w[2 * m + 1][I >> 1], w[2 * m][I >> 1], sel);
  return __builtin_bit_cast(bf16x8, o);
}

constexpr int QD = 4;

__global__ __launch_bounds__(512, 2) void attn_kernel(const __hip_bfloat16* __restrict__ x,
                                                      const __hip_bfloat16* __restrict__ kc,
                                                      const __hip_bfloat16* __restrict__ vc,
                                                      const __hip_bfloat16* __restrict__ wq,
                                                      char* __restrict__ ows) {
  extern __shared__ __attribute__((aligned(16))) char smem[];
  const int tid = threadIdx.x;
  const int wid = __builtin_amdgcn_readfirstlane(tid >> 6);
  const int lane = tid & 63;
  const int kvh = blockIdx.x & 7, b = blockIdx.x >> 3;

  if (wid >= 4) {
    const int lw = wid - 4;
    const __hip_bfloat16* xb = x + (size_t)b * SQ * DM;
#pragma unroll
    for (int i = 0; i < 8; ++i) {
      const int row = 8 * lw + i;
      glds16(xb + row * DM + ((lane ^ (row & 15)) * 8), smem + X_OFF + row * 1024);
    }
    const size_t kvbase = ((size_t)(b * HKV + kvh)) * SK * DH;
    const int sub = lane >> 4, pc = lane & 15;
    auto issue_tile = [&](int t) {
      const int slot = t < 10 ? t : t - 10;
      const __hip_bfloat16* base = ((t & 1) ? vc : kc) + kvbase + (size_t)(t >> 1) * 64 * DH;
#pragma unroll
      for (int u = 0; u < 4; ++u) {
        const int row = 16 * lw + 4 * u + sub;
        const int chunk = (t & 1) ? (pc ^ (sub << 2)) : (pc ^ (4 * u + sub));
        glds16(base + row * DH + chunk * 8, smem + slot * 16384 + (4 * lw + u) * 1024);
      }
    };
#pragma unroll
    for (int t = 0; t < 8; ++t) issue_tile(t);
    asm volatile("s_waitcnt vmcnt(32)" ::: "memory");
    wg_barrier();
    asm volatile("s_waitcnt vmcnt(24)" ::: "memory");
    wg_barrier();
    issue_tile(8);
    issue_tile(9);
    asm volatile("s_waitcnt vmcnt(24)" ::: "memory");
    wg_barrier();
    issue_tile(10);
    issue_tile(11);
    asm volatile("s_waitcnt vmcnt(24)" ::: "memory");
    wg_barrier();
    issue_tile(12);
    issue_tile(13);
    asm volatile("s_waitcnt vmcnt(24)" ::: "memory");
    wg_barrier();
    issue_tile(14);
    issue_tile(15);
    asm volatile("s_waitcnt vmcnt(24)" ::: "memory");
    wg_barrier();
    asm volatile("s_waitcnt vmcnt(16)" ::: "memory");
    wg_barrier();
    asm volatile("s_waitcnt vmcnt(8)" ::: "memory");
    wg_barrier();
    asm volatile("s_waitcnt vmcnt(0)" ::: "memory");
    wg_barrier();
    return;
  }

  const int hh = wid;
  const int r = lane & 31, h = lane >> 5;
  const auto wq_rs = __builtin_amdgcn_make_buffer_rsrc((void*)wq, 0, DM * HQ * DH * 2, 0x00020000);
  const int wq_voff = (8 * h) * 8192 + (kvh * 512 + hh * 128 + 4 * r) * 2;
  u32x2 w[QD][8];
#pragma unroll
  for (int d = 0; d < QD; ++d)
#pragma unroll
    for (int jj = 0; jj < 8; ++jj)
      w[d][jj] = __builtin_amdgcn_raw_buffer_load_b64(wq_rs, wq_voff, (16 * d + jj) * 8192, 0);
  wg_barrier();
  f32x16 acc[4];
#pragma unroll
  for (int i = 0; i < 4; ++i) acc[i] = f32x16{};
  {
    const char* xs = smem + X_OFF + r * 1024;
    const int xr = r & 15;
#pragma unroll
    for (int s = 0; s < 32; ++s) {
      const bf16x8 xf = *reinterpret_cast<const bf16x8*>(xs + (((2 * s + h) ^ xr) << 4));
      const bf16x8 a0 = tr_frag<0>(w[s % QD]);
      const bf16x8 a1 = tr_frag<1>(w[s % QD]);
      const bf16x8 a2 = tr_frag<2>(w[s % QD]);
      const bf16x8 a3 = tr_frag<3>(w[s % QD]);
      if (s + QD < 32) {
#pragma unroll
        for (int jj = 0; jj < 8; ++jj)
          w[s % QD][jj] = __builtin_amdgcn_raw_buffer_load_b64(wq_rs, wq_voff, (16 * (s + QD) + jj) * 8192, 0);
      }
      acc[0] = MFMA32(a0, xf, acc[0]);
      acc[1] = MFMA32(a1, xf, acc[1]);
      acc[2] = MFMA32(a2, xf, acc[2]);
      acc[3] = MFMA32(a3, xf, acc[3]);
    }
  }
  bf16x8 qf[8];
#pragma unroll
  for (int p = 0; p < 8; ++p) {
    u32x4 o;
    o[0] = pk2(acc[0][2 * p], acc[1][2 * p]);
    o[1] = pk2(acc[2][2 * p], acc[3][2 * p]);
    o[2] = pk2(acc[0][2 * p + 1], acc[1][2 * p + 1]);
    o[3] = pk2(acc[2][2 * p + 1], acc[3][2 * p + 1]);
    qf[p] = __builtin_bit_cast(bf16x8, o);
  }
  wg_barrier();

  constexpr float CS = 0.088388347648318447f * 1.4426950408889634f;
  float m = -__builtin_inff(), l = 0.f;
  f32x16 o[4];
#pragma unroll
  for (int i = 0; i < 4; ++i) o[i] = f32x16{};
  const int ky = (r & 15) ^ (2 * h);
  const int koff = r * 256;
  const int vq = (lane >> 2) & 3, vpp = lane & 3, vg = lane >> 4;
  const int voff = (4 * h + vq) * 256 + (2 * (vg & 1) + (vpp >> 1)) * 16 + 8 * (vpp & 1);

#pragma unroll
  for (int j = 0; j < 8; ++j) {
    const int tk = 2 * j, tv = 2 * j + 1;
    const char* Ks = smem + (tk < 10 ? tk : tk - 10) * 16384;
    const char* Vs = smem + (tv < 10 ? tv : tv - 10) * 16384;
    f32x16 s0 = f32x16{}, s1 = f32x16{};
#pragma unroll
    for (int p = 0; p < 8; ++p) {
      const int cp = (p & 1) + 4 * (p >> 1);
      const char* a = Ks + koff + ((cp ^ ky) << 4);
      const bf16x8 a0 = *reinterpret_cast<const bf16x8*>(a);
      const bf16x8 a1 = *reinterpret_cast<const bf16x8*>(a + 8192);
      s0 = MFMA32(a0, qf[p], s0);
      s1 = MFMA32(a1, qf[p], s1);
    }
    float tmax = s0[0];
#pragma unroll
    for (int i = 1; i < 16; ++i) tmax = fmaxf(tmax, s0[i]);
#pragma unroll
    for (int i = 0; i < 16; ++i) tmax = fmaxf(tmax, s1[i]);
    tmax = swap_half_max(tmax);
    const float mnew = fmaxf(m, tmax);
    const float alpha = __builtin_amdgcn_exp2f((m - mnew) * CS);
    m = mnew;
    const float mc = mnew * CS;
    float ps = 0.f;
#pragma unroll
    for (int i = 0; i < 16; ++i) { s0[i] = __builtin_amdgcn_exp2f(fmaf(s0[i], CS, -mc)); ps += s0[i]; }
#pragma unroll
    for (int i = 0; i < 16; ++i) { s1[i] = __builtin_amdgcn_exp2f(fmaf(s1[i], CS, -mc)); ps += s1[i]; }
    l = l * alpha + ps;
#pragma unroll
    for (int dt = 0; dt < 4; ++dt)
#pragma unroll
      for (int i = 0; i < 16; ++i) o[dt][i] *= alpha;
    bf16x8 pf[4];
#pragma unroll
    for (int s = 0; s < 2; ++s) {
      u32x4 u0, u1;
#pragma unroll
      for (int e = 0; e < 4; ++e) {
        u0[e] = pk2(s0[8 * s + 2 * e], s0[8 * s + 2 * e + 1]);
        u1[e] = pk2(s1[8 * s + 2 * e], s1[8 * s + 2 * e + 1]);
      }
      pf[s] = __builtin_bit_cast(bf16x8, u0);
      pf[2 + s] = __builtin_bit_cast(bf16x8, u1);
    }
#pragma unroll
    for (int dt = 0; dt < 4; ++dt) {
      const char* vb = Vs + voff + ((dt ^ vq) << 6);
#pragma unroll
      for (int ks = 0; ks < 4; ++ks) {
        const s16x4 lo = __builtin_amdgcn_ds_read_tr16_b64_v4i16((lds_s16x4_ptr)(vb + ks * 4096));
        const s16x4 hi = __builtin_amdgcn_ds_read_tr16_b64_v4i16((lds_s16x4_ptr)(vb + ks * 4096 + 2048));
        const bf16x8 va = __builtin_bit_cast(bf16x8, __builtin_shufflevector(lo, hi, 0, 1, 2, 3, 4, 5, 6, 7));
        o[dt] = MFMA32(va, pf[ks], o[dt]);
      }
    }
    if (j < 7) wg_barrier();
  }
  l = swap_half_add(l);
  const float inv = 1.0f / l;
  {
    char* ob = ows + ((size_t)(b * 128 + (kvh * 4 + hh) * 4) * 2 + (r >> 4)) * 1024 + (r & 15) * 16 + 8 * h;
#pragma unroll
    for (int dt = 0; dt < 4; ++dt)
#pragma unroll
      for (int g = 0; g < 4; ++g) {
        u32x2 v;
        v[0] = pk2(o[dt][4 * g] * inv, o[dt][4 * g + 1] * inv);
        v[1] = pk2(o[dt][4 * g + 2] * inv, o[dt][4 * g + 3] * inv);
        *reinterpret_cast<u32x2*>(ob + dt * 2048 + g * 256) = v;
      }
  }
}

constexpr int OD = 6;

__global__ __launch_bounds__(512, 2) void oproj_kernel(const char* __restrict__ ows,
                                                       const __hip_bfloat16* __restrict__ wo,
                                                       __hip_bfloat16* __restrict__ out) {
  __shared__ __attribute__((aligned(16))) f32x4 red[8][8][64];
  const int tid = threadIdx.x;
  const int wid = __builtin_amdgcn_readfirstlane(tid >> 6);
  const int lane = tid & 63;
  const int xcd = blockIdx.x & 7, idx = blockIdx.x >> 3;
  const int b = (xcd >> 1) * 8 + (idx >> 2), jc = (xcd & 1) * 4 + (idx & 3);
  const int r = lane & 15, q = lane >> 4;
  const auto wo_rs = __builtin_amdgcn_make_buffer_rsrc((void*)wo, 0, HQ * DH * DM * 2, 0x00020000);
  const auto o_rs = __builtin_amdgcn_make_buffer_rsrc((void*)ows, 0, NB * SQ * HQ * DH * 2, 0x00020000);
  const int w_voff = (wid * 512 + 8 * q) * 1024 + (64 * jc + 4 * r) * 2;
  const int o_voff = (b * 128 + wid * 16) * 2048 + lane * 16;
  u32x2 w[OD][8];
  u32x4 bfr[OD][2];
#pragma unroll
  for (int d = 0; d < OD; ++d) {
    bfr[d][0] = __builtin_amdgcn_raw_buffer_load_b128(o_rs, o_voff, d * 2048, 0);
    bfr[d][1] = __builtin_amdgcn_raw_buffer_load_b128(o_rs, o_voff, d * 2048 + 1024, 0);
#pragma unroll
    for (int jj = 0; jj < 8; ++jj)
      w[d][jj] = __builtin_amdgcn_raw_buffer_load_b64(wo_rs, w_voff, (32 * d + jj) * 1024, 0);
  }
  f32x4 acc[4][2];
#pragma unroll
  for (int i = 0; i < 4; ++i) { acc[i][0] = f32x4{}; acc[i][1] = f32x4{}; }
#pragma unroll
  for (int s = 0; s < 16; ++s) {
    const bf16x8 a0 = tr_frag<0>(w[s % OD]);
    const bf16x8 a1 = tr_frag<1>(w[s % OD]);
    const bf16x8 a2 = tr_frag<2>(w[s % OD]);
    const bf16x8 a3 = tr_frag<3>(w[s % OD]);
    const bf16x8 b0 = __builtin_bit_cast(bf16x8, bfr[s % OD][0]);
    const bf16x8 b1 = __builtin_bit_cast(bf16x8, bfr[s % OD][1]);
    acc[0][0] = MFMA16(a0, b0, acc[0][0]);
    acc[0][1] = MFMA16(a0, b1, acc[0][1]);
    acc[1][0] = MFMA16(a1, b0, acc[1][0]);
    acc[1][1] = MFMA16(a1, b1, acc[1][1]);
    acc[2][0] = MFMA16(a2, b0, acc[2][0]);
    acc[2][1] = MFMA16(a2, b1, acc[2][1]);
    acc[3][0] = MFMA16(a3, b0, acc[3][0]);
    acc[3][1] = MFMA16(a3, b1, acc[3][1]);
    if (s + OD < 16) {
      bfr[s % OD][0] = __builtin_amdgcn_raw_buffer_load_b128(o_rs, o_voff, (s + OD) * 2048, 0);
      bfr[s % OD][1] = __builtin_amdgcn_raw_buffer_load_b128(o_rs, o_voff, (s + OD) * 2048 + 1024, 0);
#pragma unroll
      for (int jj = 0; jj < 8; ++jj)
        w[s % OD][jj] = __builtin_amdgcn_raw_buffer_load_b64(wo_rs, w_voff, (32 * (s + OD) + jj) * 1024, 0);
    }
  }
#pragma unroll
  for (int T = 0; T < 2; ++T)
#pragma unroll
    for (int g = 0; g < 4; ++g) {
      f32x4 v = {acc[0][T][g], acc[1][T][g], acc[2][T][g], acc[3][T][g]};
      red[wid][T * 4 + g][lane] = v;
    }
  __syncthreads();
  {
    const int a = wid;
    f32x4 v = red[0][a][lane];
#pragma unroll
    for (int ww = 1; ww < 8; ++ww) v += red[ww][a][lane];
    const int T = a >> 2, g = a & 3;
    u32x2 pkd = {pk2(v[0], v[1]), pk2(v[2], v[3])};
    *reinterpret_cast<u32x2*>(out + (size_t)(b * SQ + 16 * T + r) * DM + 64 * jc + 16 * q + 4 * g) = pkd;
  }
}

extern "C" void kernel_launch(void* const* d_in, const int* in_sizes, int n_in, void* d_out, int out_size,
                              void* d_ws, size_t ws_size, hipStream_t stream) {
  const __hip_bfloat16* x = (const __hip_bfloat16*)d_in[0];
  const __hip_bfloat16* kc = (const __hip_bfloat16*)d_in[1];
  const __hip_bfloat16* vc = (const __hip_bfloat16*)d_in[2];
  const __hip_bfloat16* wq = (const __hip_bfloat16*)d_in[3];
  const __hip_bfloat16* wo = (const __hip_bfloat16*)d_in[4];
  (void)hipFuncSetAttribute((const void*)attn_kernel, hipFuncAttributeMaxDynamicSharedMemorySize, LDS_BYTES);
  attn_kernel<<<256, 512, LDS_BYTES, stream>>>(x, kc, vc, wq, (char*)d_ws);
  oproj_kernel<<<256, 512, 0, stream>>>((const char*)d_ws, wo, (__hip_bfloat16*)d_out);
}
```

```cpp
#include <hip/hip_runtime.h>
#include <hip/hip_bf16.h>

typedef __bf16 bf16x8 __attribute__((ext_vector_type(8)));
typedef __bf16 bf16x2 __attribute__((ext_vector_type(2)));
typedef short s16x4 __attribute__((ext_vector_type(4)));
typedef float f32x2 __attribute__((ext_vector_type(2)));
typedef float f32x4 __attribute__((ext_vector_type(4)));
typedef float f32x16 __attribute__((ext_vector_type(16)));
typedef unsigned u32x2 __attribute__((ext_vector_type(2)));
typedef unsigned u32x4 __attribute__((ext_vector_type(4)));
typedef __attribute__((address_space(3))) s16x4* lds_s16x4_ptr;

#define DI __device__ __forceinline__
#define MFMA32(a, b, c) __builtin_amdgcn_mfma_f32_32x32x16_bf16((a), (b), (c), 0, 0, 0)
#define MFMA16(a, b, c) __builtin_amdgcn_mfma_f32_16x16x32_bf16((a), (b), (c), 0, 0, 0)
#define SBAR() __builtin_amdgcn_sched_barrier(0)
#define STAMP(i)

constexpr int NB = 32, SQ = 32, DM = 512, HQ = 32, HKV = 8, SK = 512, DH = 128;
constexpr int LDS_BYTES = 163840;
constexpr int QX_OFF = 131072;

DI unsigned pk2(float a, float b) {
  f32x2 f = {a, b};
  return __builtin_bit_cast(unsigned, __builtin_convertvector(f, bf16x2));
}
DI float swap_half_max(float v) {
  auto rr = __builtin_amdgcn_permlane32_swap(__float_as_uint(v), __float_as_uint(v), false, false);
  return fmaxf(__uint_as_float(rr[0]), __uint_as_float(rr[1]));
}
DI float swap_half_add(float v) {
  auto rr = __builtin_amdgcn_permlane32_swap(__float_as_uint(v), __float_as_uint(v), false, false);
  return __uint_as_float(rr[0]) + __uint_as_float(rr[1]);
}
DI void wg_barrier() {
  asm volatile("s_waitcnt lgkmcnt(0)" ::: "memory");
  __builtin_amdgcn_s_barrier();
  asm volatile("" ::: "memory");
}
DI void glds16_nt(const void* sbase, unsigned voff, unsigned lds_dst) {
  unsigned keep;
  asm volatile("s_mov_b32 %0, m0\n\ts_mov_b32 m0, %3\n\ts_nop 0\n\tglobal_load_lds_dwordx4 %1, %2 nt\n\ts_mov_b32 m0, %0"
               : "=&s"(keep) : "v"(voff), "s"(sbase), "s"(lds_dst) : "memory");
}
DI void glds16(const void* sbase, unsigned voff, unsigned lds_dst) {
  unsigned keep;
  asm volatile("s_mov_b32 %0, m0\n\ts_mov_b32 m0, %3\n\ts_nop 0\n\tglobal_load_lds_dwordx4 %1, %2\n\ts_mov_b32 m0, %0"
               : "=&s"(keep) : "v"(voff), "s"(sbase), "s"(lds_dst) : "memory");
}
template <int I>
DI bf16x8 tr_frag(const u32x4* w) {
  u32x4 o;
  constexpr unsigned sel = (I & 1) ? 0x07060302u : 0x05040100u;
#pragma unroll
  for (int m = 0; m < 4; ++m) o[m] = __builtin_amdgcn_perm(w[2 * m + 1][I >> 1], w[2 * m][I >> 1], sel);
  return __builtin_bit_cast(bf16x8, o);
}
#define PROJ_STEP(W, B0, B1, LOADS)                           \
  do {                                                        \
    bf16x8 a_[8];                                             \
    a_[0] = tr_frag<0>(W); a_[1] = tr_frag<1>(W); a_[2] = tr_frag<2>(W); a_[3] = tr_frag<3>(W); \
    a_[4] = tr_frag<4>(W); a_[5] = tr_frag<5>(W); a_[6] = tr_frag<6>(W); a_[7] = tr_frag<7>(W); \
    SBAR();                                                   \
    _Pragma("unroll") for (int i_ = 0; i_ < 8; ++i_) {        \
      acc[i_][0] = MFMA16(a_[i_], B0, acc[i_][0]);            \
      acc[i_][1] = MFMA16(a_[i_], B1, acc[i_][1]);            \
    }                                                         \
    LOADS;                                                    \
    _Pragma("unroll") for (int i_ = 0; i_ < 8; ++i_) {        \
      __builtin_amdgcn_sched_group_barrier(0x8, 2, 0);        \
      __builtin_amdgcn_sched_group_barrier(0x20, 1, 0);       \
    }                                                         \
  } while (0)

constexpr int QD = 3;

__global__ __launch_bounds__(512, 2) void attn_kernel(const __hip_bfloat16* __restrict__ x,
                                                      const __hip_bfloat16* __restrict__ kc,
                                                      const __hip_bfloat16* __restrict__ vc,
                                                      const __hip_bfloat16* __restrict__ wq,
                                                      const __hip_bfloat16* __restrict__ wo,
                                                      char* __restrict__ ows) {
  extern __shared__ __attribute__((aligned(16))) char smem[];
  const int tid = threadIdx.x;
  const int wid = __builtin_amdgcn_readfirstlane(tid >> 6);
  const int lane = tid & 63;
  const int kvh = blockIdx.x & 7, b = blockIdx.x >> 3;
  const int hh = wid & 3, u = wid >> 2;
  const int r = lane & 31, h = lane >> 5;
  const int r16 = lane & 15, q = lane >> 4;
  const int grp = b & 1;
  const int rot = (b >> 1) & 15;
  STAMP(0);

  const unsigned lds0 = (unsigned)(size_t)smem;
  const size_t kvbase = ((size_t)(b * HKV + kvh)) * SK * DH;
  auto tile_isv = [](int t) { return t >= 14 ? 1 : (t < 2 ? 0 : (((t - 2) >> 1) & 1)); };
  auto tile_idx = [](int t) { return t >= 14 ? t - 8 : (t < 2 ? t : ((((t - 2) >> 1) & 1) ? 2 * ((t - 2) >> 2) + (t & 1) : 2 * ((t - 2) >> 2) + 2 + (t & 1))); };
  auto issue_tile = [&](int t) {
    const int slot = t < 10 ? t : t - 10;
    const int isv = tile_isv(t);
    const char* base = (const char*)((isv ? vc : kc) + kvbase + (size_t)tile_idx(t) * 64 * DH) + hh * 4096;
#pragma unroll
    for (int e = 0; e < 4; ++e) {
      const int chunk = isv ? (r16 ^ (q << 2)) : (r16 ^ (4 * e + q));
      glds16_nt(base + e * 1024, q * 256 + chunk * 16, lds0 + slot * 16384 + (4 * hh + e) * 1024);
    }
  };
  auto issue_tile8 = [&](int t) {
    const int slot = t < 10 ? t : t - 10;
    const int isv = tile_isv(t);
    const char* base = (const char*)((isv ? vc : kc) + kvbase + (size_t)tile_idx(t) * 64 * DH) + wid * 2048;
#pragma unroll
    for (int e = 0; e < 2; ++e) {
      const int chunk = isv ? (r16 ^ (q << 2)) : (r16 ^ (8 * (wid & 1) + 4 * e + q));
      glds16_nt(base + e * 1024, q * 256 + chunk * 16, lds0 + slot * 16384 + (2 * wid + e) * 1024);
    }
  };
  {
    const __hip_bfloat16* xb = x + (size_t)b * SQ * DM;
#pragma unroll
    for (int i = 0; i < 4; ++i) {
      const int row = 4 * wid + i;
      glds16(xb + row * DM, (lane ^ (row & 15)) * 16, lds0 + QX_OFF + row * 1024);
    }
  }

  bf16x8 qf[8];
  if (u == 1) {
    if (grp == 0) {
#pragma unroll
      for (int t = 0; t < 8; ++t) issue_tile(t);
      asm volatile("s_waitcnt vmcnt(32)" ::: "memory");
    } else {
      asm volatile("s_waitcnt vmcnt(0)" ::: "memory");
    }
    wg_barrier();
    wg_barrier();
    if (grp == 1) {
#pragma unroll
      for (int t = 0; t < 3; ++t) issue_tile(t);
    }
    wg_barrier();
    wg_barrier();
    if (grp == 1) {
#pragma unroll
      for (int t = 3; t < 8; ++t) issue_tile(t);
    }
#pragma unroll
    for (int p = 0; p < 8; ++p)
      qf[p] = *reinterpret_cast<const bf16x8*>(smem + QX_OFF + (hh * 8 + p) * 1024 + lane * 16);
    asm volatile("s_waitcnt vmcnt(24)" ::: "memory");
  } else {
    const auto wq_rs = __builtin_amdgcn_make_buffer_rsrc((void*)wq, 0, DM * HQ * DH * 2, 0x00020000);
    const int w_voff = (8 * q) * 8192 + (kvh * 512 + hh * 128 + 8 * r16) * 2;
    u32x4 w[QD][8];
#pragma unroll
    for (int d = 0; d < QD; ++d) {
      const int sr = (d + rot) & 15;
#pragma unroll
      for (int jj = 0; jj < 8; ++jj)
        w[d][jj] = __builtin_amdgcn_raw_buffer_load_b128(wq_rs, w_voff, (32 * sr + jj) * 8192, 0);
    }
    asm volatile("s_waitcnt vmcnt(24)" ::: "memory");
    wg_barrier();
    STAMP(14);
    f32x4 acc[8][2];
#pragma unroll
    for (int i = 0; i < 8; ++i) { acc[i][0] = f32x4{}; acc[i][1] = f32x4{}; }
    const char* xs = smem + QX_OFF + r16 * 1024;
#pragma unroll
    for (int s = 0; s < 16; ++s) {
      SBAR();
      const int xo = ((4 * ((s + rot) & 15) + q) ^ r16) << 4;
      const bf16x8 b0 = *reinterpret_cast<const bf16x8*>(xs + xo);
      const bf16x8 b1 = *reinterpret_cast<const bf16x8*>(xs + xo + 16384);
      const int sr = (s + QD + rot) & 15;
      PROJ_STEP(w[s % QD], b0, b1, if (s + QD < 16) {
        _Pragma("unroll") for (int jj = 0; jj < 8; ++jj)
          w[s % QD][jj] = __builtin_amdgcn_raw_buffer_load_b128(wq_rs, w_voff, (32 * sr + jj) * 8192, 0);
      });
      if (s + QD == 15) { SBAR(); __builtin_amdgcn_s_barrier(); }
    }
#pragma unroll
    for (int g = 0; g < 4; ++g) {
      u32x4 f0, f1;
#pragma unroll
      for (int e = 0; e < 4; ++e) {
        const unsigned a0 = pk2(acc[2 * e][0][g], acc[2 * e + 1][0][g]);
        const unsigned a1 = pk2(acc[2 * e][1][g], acc[2 * e + 1][1][g]);
        auto rr = __builtin_amdgcn_permlane16_swap(a0, a1, false, false);
        f0[e] = rr[0];
        f1[e] = rr[1];
      }
      qf[g] = __builtin_bit_cast(bf16x8, f0);
      qf[4 + g] = __builtin_bit_cast(bf16x8, f1);
    }
    wg_barrier();
#pragma unroll
    for (int p = 0; p < 8; ++p)
      *reinterpret_cast<bf16x8*>(smem + QX_OFF + (hh * 8 + p) * 1024 + lane * 16) = qf[p];
    STAMP(1);
    wg_barrier();
  }

  constexpr float CS = 0.088388347648318447f * 1.4426950408889634f;
  float m = -__builtin_inff(), l = 0.f;
  f32x16 o[4];
#pragma unroll
  for (int i = 0; i < 4; ++i) o[i] = f32x16{};
  const int ky = (r & 15) ^ (8 * h);
  const int koff = r * 256;
  const int vq = (lane >> 2) & 3, vpp = lane & 3;
  const int voff = (4 * h + vq) * 256 + (2 * (q & 1) + (vpp >> 1)) * 16 + 8 * (vpp & 1);
  bf16x8 fb[16];
  bf16x8 pf[4];
  f32x16 s0, s1;
#define SLOT(T) (smem + (((T) < 10 ? (T) : (T) - 10) + u) * 16384)
#define KT(S) ((S) == 0 ? 0 : 4 * (S) - 2)
#define VT(S) ((S) == 3 ? 14 : 4 * (S) + 4)
#define K_READ(S)                                                                                           \
  do {                                                                                                      \
    const char* Ks = SLOT(KT(S));                                                                         \
    _Pragma("unroll") for (int p = 0; p < 8; ++p) {                                                         \
      const char* a = Ks + koff + ((p ^ ky) << 4);                                                          \
      fb[2 * p] = *reinterpret_cast<const bf16x8*>(a);                                                      \
      fb[2 * p + 1] = *reinterpret_cast<const bf16x8*>(a + 8192);                                           \
    }                                                                                                       \
  } while (0)
#define QK_MMA()                                                                                            \
  do {                                                                                                      \
    s0 = f32x16{}; s1 = f32x16{};                                                                           \
    _Pragma("unroll") for (int p = 0; p < 8; ++p) {                                                         \
      s0 = MFMA32(fb[2 * p], qf[p], s0);                                                                    \
      s1 = MFMA32(fb[2 * p + 1], qf[p], s1);                                                                \
    }                                                                                                       \
  } while (0)
#define V_READ(S, DT)                                                                                       \
  do {                                                                                                      \
    const char* vb = SLOT(VT(S)) + voff + (((DT) ^ vq) << 6);                                         \
    _Pragma("unroll") for (int ks = 0; ks < 4; ++ks) {                                                      \
      const s16x4 lo = __builtin_amdgcn_ds_read_tr16_b64_v4i16((lds_s16x4_ptr)(vb + ks * 4096));            \
      const s16x4 hi = __builtin_amdgcn_ds_read_tr16_b64_v4i16((lds_s16x4_ptr)(vb + ks * 4096 + 2048));     \
      fb[(DT) * 4 + ks] = __builtin_bit_cast(bf16x8, __builtin_shufflevector(lo, hi, 0, 1, 2, 3, 4, 5, 6, 7)); \
    }                                                                                                       \
  } while (0)
#define PV_MMA(D0)                                                                                          \
  do {                                                                                                      \
    _Pragma("unroll") for (int ks = 0; ks < 4; ++ks)                                                        \
      _Pragma("unroll") for (int dt = (D0); dt < (D0) + 2; ++dt) o[dt] = MFMA32(fb[dt * 4 + ks], pf[ks], o[dt]); \
  } while (0)
#define SOFTMAX(S)                                                                                          \
  do {                                                                                                      \
    float tmax = s0[0];                                                                                     \
    _Pragma("unroll") for (int i = 1; i < 16; ++i) tmax = fmaxf(tmax, s0[i]);                               \
    _Pragma("unroll") for (int i = 0; i < 16; ++i) tmax = fmaxf(tmax, s1[i]);                               \
    tmax = swap_half_max(tmax);                                                                             \
    const float mnew = fmaxf(m, tmax);                                                                      \
    const float alpha = __builtin_amdgcn_exp2f((m - mnew) * CS);                                            \
    m = mnew;                                                                                               \
    const float mc = mnew * CS;                                                                             \
    float ps = 0.f;                                                                                         \
    _Pragma("unroll") for (int i = 0; i < 16; ++i) { s0[i] = __builtin_amdgcn_exp2f(fmaf(s0[i], CS, -mc)); ps += s0[i]; } \
    _Pragma("unroll") for (int i = 0; i < 16; ++i) { s1[i] = __builtin_amdgcn_exp2f(fmaf(s1[i], CS, -mc)); ps += s1[i]; } \
    l = l * alpha + ps;                                                                                     \
    asm volatile("" : "+v"(l));                                                                             \
    if ((S) > 0) {                                                                                          \
      _Pragma("unroll") for (int dt = 0; dt < 4; ++dt)                                                      \
        _Pragma("unroll") for (int i = 0; i < 16; ++i) o[dt][i] *= alpha;                                   \
    }                                                                                                       \
    _Pragma("unroll") for (int ss = 0; ss < 2; ++ss) {                                                      \
      u32x4 u0, u1;                                                                                         \
      _Pragma("unroll") for (int e = 0; e < 4; ++e) {                                                       \
        u0[e] = pk2(s0[8 * ss + 2 * e], s0[8 * ss + 2 * e + 1]);                                            \
        u1[e] = pk2(s1[8 * ss + 2 * e], s1[8 * ss + 2 * e + 1]);                                            \
      }                                                                                                     \
      pf[ss] = __builtin_bit_cast(bf16x8, u0);                                                              \
      pf[2 + ss] = __builtin_bit_cast(bf16x8, u1);                                                          \
    }                                                                                                       \
  } while (0)
#define DMA_A(S)                                                                                            \
  do {                                                                                                      \
    if ((S) == 0) issue_tile8(8);                                                                           \
    if ((S) == 1) issue_tile8(10);                                                                          \
    if ((S) == 2) { issue_tile8(12); issue_tile8(13); }                                                     \
    if ((S) == 3) {                                                                                         \
                    \
      const char* wb = (const char*)wo + (size_t)(kvh * 512 + 16 * b + 2 * wid) * 1024;                     \
      glds16(wb, lane * 16, lds0 + 6 * 16384 + 2 * wid * 1024);                                             \
      glds16(wb + 1024, lane * 16, lds0 + 6 * 16384 + (2 * wid + 1) * 1024);                                \
    }                                                                                                       \
  } while (0)
#define DMA_B(S)                                                                                            \
  do {                                                                                                      \
    if ((S) == 0) issue_tile8(9);                                                                           \
    if ((S) == 1) issue_tile8(11);                                                                          \
    if ((S) == 2) { issue_tile8(14); issue_tile8(15); }                                                     \
  } while (0)
#define STEP_WAIT(S)                                                                                        \
  do {                                                                                                      \
    if ((S) == 0) asm volatile("s_waitcnt vmcnt(12)" ::: "memory");                                         \
    if ((S) == 1 || (S) == 2) asm volatile("s_waitcnt vmcnt(4)" ::: "memory");                              \
    if ((S) == 3) asm volatile("s_waitcnt vmcnt(2)" ::: "memory");                                          \
  } while (0)

  if (u == 0) {
#pragma unroll
    for (int s = 0; s < 4; ++s) {
      wg_barrier();
      STAMP(2 + 2 * s);
      DMA_A(s);
      DMA_B(s);
      if (s > 0) {
        V_READ(s - 1, 0);
        V_READ(s - 1, 1);
        V_READ(s - 1, 2);
        V_READ(s - 1, 3);
        SBAR();
        PV_MMA(0);
        PV_MMA(2);
        SBAR();
      }
      K_READ(s);
      SBAR();
      QK_MMA();
      SBAR();
      STAMP(3 + 2 * s);
      SOFTMAX(s);
      SBAR();
      STEP_WAIT(s);
    }
    wg_barrier();
    V_READ(3, 0);
    V_READ(3, 1);
    V_READ(3, 2);
    V_READ(3, 3);
    SBAR();
    PV_MMA(0);
    PV_MMA(2);
    SBAR();
  } else {
#pragma unroll
    for (int s = 0; s < 5; ++s) {
      wg_barrier();
      if (s < 4) { DMA_A(s); DMA_B(s); }
      if (s > 0) {
        V_READ(s - 1, 0);
        V_READ(s - 1, 1);
        SBAR();
        SOFTMAX(s - 1);
        SBAR();
        V_READ(s - 1, 2);
        V_READ(s - 1, 3);
        SBAR();
        PV_MMA(0);
        PV_MMA(2);
        SBAR();
      }
      if (s < 4) {
        K_READ(s);
        SBAR();
        QK_MMA();
        SBAR();
        STEP_WAIT(s);
      }
    }
  }
  const auto wo_rs = __builtin_amdgcn_make_buffer_rsrc((void*)wo, 0, HQ * DH * DM * 2, 0x00020000);
  const int wo_voff = (kvh * 512 + 8 * q) * 1024 + (hh * 128 + 8 * r16) * 2;
  u32x4 w[QD][8];
  if (u == 0) {
#pragma unroll
    for (int d = 0; d < QD; ++d)
#pragma unroll
      for (int jj = 0; jj < 8; ++jj)
        w[d][jj] = __builtin_amdgcn_raw_buffer_load_b128(wo_rs, wo_voff, (32 * ((d + rot) & 15) + jj) * 1024, 0);
  }
  SBAR();
  l = swap_half_add(l);
  wg_barrier();
  STAMP(10);
  {
    char* xb_ = smem + wid * 8448;
#pragma unroll
    for (int c = 0; c < 2; ++c) {
#pragma unroll
      for (int g = 0; g < 4; ++g) {
        f32x4 v;
        if (u == 0) v = f32x4{o[2 + c][4 * g], o[2 + c][4 * g + 1], o[2 + c][4 * g + 2], o[2 + c][4 * g + 3]};
        else v = f32x4{o[c][4 * g], o[c][4 * g + 1], o[c][4 * g + 2], o[c][4 * g + 3]};
        *reinterpret_cast<f32x4*>(xb_ + (c * 4 + g) * 1024 + lane * 16) = v;
      }
    }
    if (h == 0) {
      f32x2 ml = {m, l};
      *reinterpret_cast<f32x2*>(xb_ + 8192 + r * 8) = ml;
    }
    wg_barrier();
    const char* pb = smem + (wid ^ 4) * 8448;
    const f32x2 ml2 = *reinterpret_cast<const f32x2*>(pb + 8192 + r * 8);
    const float mm = fmaxf(m, ml2[0]);
    const float e1 = __builtin_amdgcn_exp2f((m - mm) * CS), e2 = __builtin_amdgcn_exp2f((ml2[0] - mm) * CS);
    const float inv = 1.0f / (l * e1 + ml2[1] * e2);
    const float f1 = e1 * inv, f2 = e2 * inv;
    char* of = smem + QX_OFF + (r >> 4) * 1024 + (r & 15) * 16 + 8 * h;
    f32x4 pv[8];
#pragma unroll
    for (int c = 0; c < 8; ++c) pv[c] = *reinterpret_cast<const f32x4*>(pb + c * 1024 + lane * 16);
    SBAR();
#pragma unroll
    for (int c = 0; c < 2; ++c) {
      const int dt = 2 * u + c;
#pragma unroll
      for (int g = 0; g < 4; ++g) {
        f32x4 ov;
        if (u == 0) ov = f32x4{o[c][4 * g], o[c][4 * g + 1], o[c][4 * g + 2], o[c][4 * g + 3]};
        else ov = f32x4{o[2 + c][4 * g], o[2 + c][4 * g + 1], o[2 + c][4 * g + 2], o[2 + c][4 * g + 3]};
        const f32x4 p2 = pv[c * 4 + g];
        u32x2 v;
        v[0] = pk2(ov[0] * f1 + p2[0] * f2, ov[1] * f1 + p2[1] * f2);
        v[1] = pk2(ov[2] * f1 + p2[2] * f2, ov[3] * f1 + p2[3] * f2);
        *reinterpret_cast<u32x2*>(of + (hh * 4 + dt) * 2048 + g * 256) = v;
      }
    }
  }
  if (u == 1) {
    asm volatile("s_waitcnt vmcnt(0)" ::: "memory");
    wg_barrier();
    return;
  }
  {
    wg_barrier();
    STAMP(11);
    f32x4 acc[8][2];
#pragma unroll
    for (int i = 0; i < 8; ++i) { acc[i][0] = f32x4{}; acc[i][1] = f32x4{}; }
    const char* of = smem + QX_OFF + lane * 16;
#pragma unroll
    for (int s = 0; s < 16; ++s) {
      SBAR();
      const bf16x8 b0 = *reinterpret_cast<const bf16x8*>(of + ((s + rot) & 15) * 2048);
      const bf16x8 b1 = *reinterpret_cast<const bf16x8*>(of + ((s + rot) & 15) * 2048 + 1024);
      PROJ_STEP(w[s % QD], b0, b1, if (s + QD < 16) {
        _Pragma("unroll") for (int jj = 0; jj < 8; ++jj)
          w[s % QD][jj] = __builtin_amdgcn_raw_buffer_load_b128(wo_rs, wo_voff, (32 * ((s + QD + rot) & 15) + jj) * 1024, 0);
      });
    }
    STAMP(12);
    char* pp = ows + ((size_t)((kvh * NB + b) * 4 + hh)) * 16384 + lane * 16;
#pragma unroll
    for (int T = 0; T < 2; ++T)
#pragma unroll
      for (int g = 0; g < 4; ++g) {
        f32x4 v0 = {acc[0][T][g], acc[1][T][g], acc[2][T][g], acc[3][T][g]};
        f32x4 v1 = {acc[4][T][g], acc[5][T][g], acc[6][T][g], acc[7][T][g]};
        *reinterpret_cast<f32x4*>(pp + ((T * 4 + g) * 2) * 1024) = v0;
        *reinterpret_cast<f32x4*>(pp + ((T * 4 + g) * 2 + 1) * 1024) = v1;
      }
    STAMP(13);
  }
}

__global__ __launch_bounds__(256) void reduce_kernel(const char* __restrict__ ows, __hip_bfloat16* __restrict__ out) {
  const int gid = blockIdx.x * 256 + threadIdx.x;
  const int lane = gid & 63, tg = (gid >> 6) & 7, bc = gid >> 9;
  const char* pp = ows + (size_t)bc * 16384 + tg * 2048 + lane * 16;
  f32x4 v0[8], v1[8];
#pragma unroll
  for (int k = 0; k < 8; ++k) {
    v0[k] = *reinterpret_cast<const f32x4*>(pp + (size_t)k * (NB * 4 * 16384));
    v1[k] = *reinterpret_cast<const f32x4*>(pp + (size_t)k * (NB * 4 * 16384) + 1024);
  }
  f32x4 a = v0[0], c = v1[0];
#pragma unroll
  for (int k = 1; k < 8; ++k) { a += v0[k]; c += v1[k]; }
  const int b = bc >> 2, cg = bc & 3, T = tg >> 2, g = tg & 3, r16 = lane & 15, q = lane >> 4;
  u32x4 o = {pk2(a[0], a[1]), pk2(a[2], a[3]), pk2(c[0], c[1]), pk2(c[2], c[3])};
  *reinterpret_cast<u32x4*>(out + (size_t)(b * SQ + 16 * T + r16) * DM + 128 * cg + 32 * q + 8 * g) = o;
}

extern "C" void kernel_launch(void* const* d_in, const int* in_sizes, int n_in, void* d_out, int out_size,
                              void* d_ws, size_t ws_size, hipStream_t stream) {
  const __hip_bfloat16* x = (const __hip_bfloat16*)d_in[0];
  const __hip_bfloat16* kc = (const __hip_bfloat16*)d_in[1];
  const __hip_bfloat16* vc = (const __hip_bfloat16*)d_in[2];
  const __hip_bfloat16* wq = (const __hip_bfloat16*)d_in[3];
  const __hip_bfloat16* wo = (const __hip_bfloat16*)d_in[4];
  (void)hipFuncSetAttribute((const void*)attn_kernel, hipFuncAttributeMaxDynamicSharedMemorySize, LDS_BYTES);
  attn_kernel<<<256, 512, LDS_BYTES, stream>>>(x, kc, vc, wq, wo, (char*)d_ws);
  reduce_kernel<<<256, 256, 0, stream>>>((const char*)d_ws, (__hip_bfloat16*)d_out);
}
```

```cpp
#include <hip/hip_runtime.h>
#include <hip/hip_bf16.h>

typedef __bf16 bf16x8 __attribute__((ext_vector_type(8)));
typedef __bf16 bf16x2 __attribute__((ext_vector_type(2)));
typedef short s16x4 __attribute__((ext_vector_type(4)));
typedef float f32x2 __attribute__((ext_vector_type(2)));
typedef float f32x4 __attribute__((ext_vector_type(4)));
typedef float f32x16 __attribute__((ext_vector_type(16)));
typedef unsigned u32x2 __attribute__((ext_vector_type(2)));
typedef unsigned u32x4 __attribute__((ext_vector_type(4)));
typedef __attribute__((address_space(3))) s16x4* lds_s16x4_ptr;

#define DI __device__ __forceinline__
#define MFMA32(a, b, c) __builtin_amdgcn_mfma_f32_32x32x16_bf16((a), (b), (c), 0, 0, 0)
#define MFMA16(a, b, c) __builtin_amdgcn_mfma_f32_16x16x32_bf16((a), (b), (c), 0, 0, 0)
#define SBAR() __builtin_amdgcn_sched_barrier(0)
#define STAMP(i)

constexpr int NB = 32, SQ = 32, DM = 512, HQ = 32, HKV = 8, SK = 512, DH = 128;
constexpr int LDS_BYTES = 163840;
constexpr int QX_OFF = 131072;

DI unsigned pk2(float a, float b) {
  f32x2 f = {a, b};
  return __builtin_bit_cast(unsigned, __builtin_convertvector(f, bf16x2));
}
DI float swap_half_max(float v) {
  auto rr = __builtin_amdgcn_permlane32_swap(__float_as_uint(v), __float_as_uint(v), false, false);
  return fmaxf(__uint_as_float(rr[0]), __uint_as_float(rr[1]));
}
DI float swap_half_add(float v) {
  auto rr = __builtin_amdgcn_permlane32_swap(__float_as_uint(v), __float_as_uint(v), false, false);
  return __uint_as_float(rr[0]) + __uint_as_float(rr[1]);
}
DI void wg_barrier() {
  asm volatile("s_waitcnt lgkmcnt(0)" ::: "memory");
  __builtin_amdgcn_s_barrier();
  asm volatile("" ::: "memory");
}
DI void glds16_nt(const void* sbase, unsigned voff, unsigned lds_dst) {
  unsigned keep;
  asm volatile("s_mov_b32 %0, m0\n\ts_mov_b32 m0, %3\n\ts_nop 0\n\tglobal_load_lds_dwordx4 %1, %2 nt\n\ts_mov_b32 m0, %0"
               : "=&s"(keep) : "v"(voff), "s"(sbase), "s"(lds_dst) : "memory");
}
DI void glds16(const void* sbase, unsigned voff, unsigned lds_dst) {
  unsigned keep;
  asm volatile("s_mov_b32 %0, m0\n\ts_mov_b32 m0, %3\n\ts_nop 0\n\tglobal_load_lds_dwordx4 %1, %2\n\ts_mov_b32 m0, %0"
               : "=&s"(keep) : "v"(voff), "s"(sbase), "s"(lds_dst) : "memory");
}
template <int I>
DI bf16x8 tr_frag(const u32x4* w) {
  u32x4 o;
  constexpr unsigned sel = (I & 1) ? 0x07060302u : 0x05040100u;
#pragma unroll
  for (int m = 0; m < 4; ++m) o[m] = __builtin_amdgcn_perm(w[2 * m + 1][I >> 1], w[2 * m][I >> 1], sel);
  return __builtin_bit_cast(bf16x8, o);
}
#define PROJ_STEP(W, B0, B1, LOADS)                           \
  do {                                                        \
    bf16x8 a_[8];                                             \
    a_[0] = tr_frag<0>(W); a_[1] = tr_frag<1>(W); a_[2] = tr_frag<2>(W); a_[3] = tr_frag<3>(W); \
    a_[4] = tr_frag<4>(W); a_[5] = tr_frag<5>(W); a_[6] = tr_frag<6>(W); a_[7] = tr_frag<7>(W); \
    SBAR();                                                   \
    _Pragma("unroll") for (int i_ = 0; i_ < 8; ++i_) {        \
      acc[i_][0] = MFMA16(a_[i_], B0, acc[i_][0]);            \
      acc[i_][1] = MFMA16(a_[i_], B1, acc[i_][1]);            \
    }                                                         \
    LOADS;                                                    \
    _Pragma("unroll") for (int i_ = 0; i_ < 8; ++i_) {        \
      __builtin_amdgcn_sched_group_barrier(0x8, 2, 0);        \
      __builtin_amdgcn_sched_group_barrier(0x20, 1, 0);       \
    }                                                         \
  } while (0)

constexpr int QD = 3;

__global__ __launch_bounds__(512, 2) void attn_kernel(const __hip_bfloat16* __restrict__ x,
                                                      const __hip_bfloat16* __restrict__ kc,
                                                      const __hip_bfloat16* __restrict__ vc,
                                                      const __hip_bfloat16* __restrict__ wq,
                                                      const __hip_bfloat16* __restrict__ wo,
                                                      char* __restrict__ ows) {
  extern __shared__ __attribute__((aligned(16))) char smem[];
  const int tid = threadIdx.x;
  const int wid = __builtin_amdgcn_readfirstlane(tid >> 6);
  const int lane = tid & 63;
  const int kvh = blockIdx.x & 7, b = blockIdx.x >> 3;
  const int hh = wid & 3, u = wid >> 2;
  const int r = lane & 31, h = lane >> 5;
  const int r16 = lane & 15, q = lane >> 4;
  const int grp = b & 1;
  const int rot = (b >> 1) & 15;
  STAMP(0);

  const unsigned lds0 = (unsigned)(size_t)smem;
  const size_t kvbase = ((size_t)(b * HKV + kvh)) * SK * DH;
  auto tile_isv = [](int t) { return t >= 14 ? 1 : (t < 2 ? 0 : (((t - 2) >> 1) & 1)); };
  auto tile_idx = [](int t) { return t >= 14 ? t - 8 : (t < 2 ? t : ((((t - 2) >> 1) & 1) ? 2 * ((t - 2) >> 2) + (t & 1) : 2 * ((t - 2) >> 2) + 2 + (t & 1))); };
  auto issue_tile = [&](int t) {
    const int slot = t < 10 ? t : t - 10;
    const int isv = tile_isv(t);
    const char* base = (const char*)((isv ? vc : kc) + kvbase + (size_t)tile_idx(t) * 64 * DH) + hh * 4096;
#pragma unroll
    for (int e = 0; e < 4; ++e) {
      const int chunk = isv ? (r16 ^ (q << 2)) : (r16 ^ (4 * e + q));
      glds16_nt(base + e * 1024, q * 256 + chunk * 16, lds0 + slot * 16384 + (4 * hh + e) * 1024);
    }
  };
  auto issue_tile8 = [&](int t) {
    const int slot = t < 10 ? t : t - 10;
    const int isv = tile_isv(t);
    const char* base = (const char*)((isv ? vc : kc) + kvbase + (size_t)tile_idx(t) * 64 * DH) + wid * 2048;
#pragma unroll
    for (int e = 0; e < 2; ++e) {
      const int chunk = isv ? (r16 ^ (q << 2)) : (r16 ^ (8 * (wid & 1) + 4 * e + q));
      glds16_nt(base + e * 1024, q * 256 + chunk * 16, lds0 + slot * 16384 + (2 * wid + e) * 1024);
    }
  };
  {
    const __hip_bfloat16* xb = x + (size_t)b * SQ * DM;
#pragma unroll
    for (int i = 0; i < 4; ++i) {
      const int row = 4 * wid + i;
      glds16(xb + row * DM, (lane ^ (row & 15)) * 16, lds0 + QX_OFF + row * 1024);
    }
  }

  bf16x8 qf[8];
  if (u == 1) {
    if (grp == 0) {
#pragma unroll
      for (int t = 0; t < 8; ++t) issue_tile(t);
      asm volatile("s_waitcnt vmcnt(32)" ::: "memory");
    } else {
      asm volatile("s_waitcnt vmcnt(0)" ::: "memory");
    }
    wg_barrier();
    wg_barrier();
    if (grp == 1) {
#pragma unroll
      for (int t = 0; t < 3; ++t) issue_tile(t);
    }
    wg_barrier();
    wg_barrier();
    if (grp == 1) {
#pragma unroll
      for (int t = 3; t < 8; ++t) issue_tile(t);
    }
#pragma unroll
    for (int p = 0; p < 8; ++p)
      qf[p] = *reinterpret_cast<const bf16x8*>(smem + QX_OFF + (hh * 8 + p) * 1024 + lane * 16);
    asm volatile("s_waitcnt vmcnt(24)" ::: "memory");
  } else {
    const auto wq_rs = __builtin_amdgcn_make_buffer_rsrc((void*)wq, 0, DM * HQ * DH * 2, 0x00020000);
    const int w_voff = (8 * q) * 8192 + (kvh * 512 + hh * 128 + 8 * r16) * 2;
    u32x4 w[QD][8];
#pragma unroll
    for (int d = 0; d < QD; ++d) {
      const int sr = (d + rot) & 15;
#pragma unroll
      for (int jj = 0; jj < 8; ++jj)
        w[d][jj] = __builtin_amdgcn_raw_buffer_load_b128(wq_rs, w_voff, (32 * sr + jj) * 8192, 0);
    }
    asm volatile("s_waitcnt vmcnt(24)" ::: "memory");
    wg_barrier();
    STAMP(14);
    f32x4 acc[8][2];
#pragma unroll
    for (int i = 0; i < 8; ++i) { acc[i][0] = f32x4{}; acc[i][1] = f32x4{}; }
    const char* xs = smem + QX_OFF + r16 * 1024;
#pragma unroll
    for (int s = 0; s < 16; ++s) {
      SBAR();
      const int xo = ((4 * ((s + rot) & 15) + q) ^ r16) << 4;
      const bf16x8 b0 = *reinterpret_cast<const bf16x8*>(xs + xo);
      const bf16x8 b1 = *reinterpret_cast<const bf16x8*>(xs + xo + 16384);
      const int sr = (s + QD + rot) & 15;
      PROJ_STEP(w[s % QD], b0, b1, if (s + QD < 16) {
        _Pragma("unroll") for (int jj = 0; jj < 8; ++jj)
          w[s % QD][jj] = __builtin_amdgcn_raw_buffer_load_b128(wq_rs, w_voff, (32 * sr + jj) * 8192, 0);
      });
      if (s + QD == 15) { SBAR(); __builtin_amdgcn_s_barrier(); }
    }
#pragma unroll
    for (int g = 0; g < 4; ++g) {
      u32x4 f0, f1;
#pragma unroll
      for (int e = 0; e < 4; ++e) {
        const unsigned a0 = pk2(acc[2 * e][0][g], acc[2 * e + 1][0][g]);
        const unsigned a1 = pk2(acc[2 * e][1][g], acc[2 * e + 1][1][g]);
        auto rr = __builtin_amdgcn_permlane16_swap(a0, a1, false, false);
        f0[e] = rr[0];
        f1[e] = rr[1];
      }
      qf[g] = __builtin_bit_cast(bf16x8, f0);
      qf[4 + g] = __builtin_bit_cast(bf16x8, f1);
    }
    wg_barrier();
#pragma unroll
    for (int p = 0; p < 8; ++p)
      *reinterpret_cast<bf16x8*>(smem + QX_OFF + (hh * 8 + p) * 1024 + lane * 16) = qf[p];
    STAMP(1);
    wg_barrier();
  }

  constexpr float CS = 0.088388347648318447f * 1.4426950408889634f;
  float m = -__builtin_inff(), l = 0.f;
  f32x16 o[4];
#pragma unroll
  for (int i = 0; i < 4; ++i) o[i] = f32x16{};
  const int ky = (r & 15) ^ (8 * h);
  const int koff = r * 256;
  const int vq = (lane >> 2) & 3, vpp = lane & 3;
  const int voff = (4 * h + vq) * 256 + (2 * (q & 1) + (vpp >> 1)) * 16 + 8 * (vpp & 1);
  bf16x8 fb[16];
  bf16x8 pf[4];
  f32x16 s0, s1;
#define SLOT(T) (smem + (((T) < 10 ? (T) : (T) - 10) + u) * 16384)
#define KT(S) ((S) == 0 ? 0 : 4 * (S) - 2)
#define VT(S) ((S) == 3 ? 14 : 4 * (S) + 4)
#define K_READ(S)                                                                                           \
  do {                                                                                                      \
    const char* Ks = SLOT(KT(S));                                                                         \
    _Pragma("unroll") for (int p = 0; p < 8; ++p) {                                                         \
      const char* a = Ks + koff + ((p ^ ky) << 4);                                                          \
      fb[2 * p] = *reinterpret_cast<const bf16x8*>(a);                                                      \
      fb[2 * p + 1] = *reinterpret_cast<const bf16x8*>(a + 8192);                                           \
    }                                                                                                       \
  } while (0)
#define QK_MMA()                                                                                            \
  do {                                                                                                      \
    s0 = f32x16{}; s1 = f32x16{};                                                                           \
    _Pragma("unroll") for (int p = 0; p < 8; ++p) {                                                         \
      s0 = MFMA32(fb[2 * p], qf[p], s0);                                                                    \
      s1 = MFMA32(fb[2 * p + 1], qf[p], s1);                                                                \
    }                                                                                                       \
  } while (0)
#define V_READ(S, DT)                                                                                       \
  do {                                                                                                      \
    const char* vb = SLOT(VT(S)) + voff + (((DT) ^ vq) << 6);                                         \
    _Pragma("unroll") for (int ks = 0; ks < 4; ++ks) {                                                      \
      const s16x4 lo = __builtin_amdgcn_ds_read_tr16_b64_v4i16((lds_s16x4_ptr)(vb + ks * 4096));            \
      const s16x4 hi = __builtin_amdgcn_ds_read_tr16_b64_v4i16((lds_s16x4_ptr)(vb + ks * 4096 + 2048));     \
      fb[(DT) * 4 + ks] = __builtin_bit_cast(bf16x8, __builtin_shufflevector(lo, hi, 0, 1, 2, 3, 4, 5, 6, 7)); \
    }                                                                                                       \
  } while (0)
#define PV_MMA(D0)                                                                                          \
  do {                                                                                                      \
    _Pragma("unroll") for (int ks = 0; ks < 4; ++ks)                                                        \
      _Pragma("unroll") for (int dt = (D0); dt < (D0) + 2; ++dt) o[dt] = MFMA32(fb[dt * 4 + ks], pf[ks], o[dt]); \
  } while (0)
#define SOFTMAX(S)                                                                                          \
  do {                                                                                                      \
    float tmax = s0[0];                                                                                     \
    _Pragma("unroll") for (int i = 1; i < 16; ++i) tmax = fmaxf(tmax, s0[i]);                               \
    _Pragma("unroll") for (int i = 0; i < 16; ++i) tmax = fmaxf(tmax, s1[i]);                               \
    tmax = swap_half_max(tmax);                                                                             \
    const float mnew = fmaxf(m, tmax);                                                                      \
    const float alpha = __builtin_amdgcn_exp2f((m - mnew) * CS);                                            \
    m = mnew;                                                                                               \
    const float mc = mnew * CS;                                                                             \
    float ps = 0.f;                                                                                         \
    _Pragma("unroll") for (int i = 0; i < 16; ++i) { s0[i] = __builtin_amdgcn_exp2f(fmaf(s0[i], CS, -mc)); ps += s0[i]; } \
    _Pragma("unroll") for (int i = 0; i < 16; ++i) { s1[i] = __builtin_amdgcn_exp2f(fmaf(s1[i], CS, -mc)); ps += s1[i]; } \
    l = l * alpha + ps;                                                                                     \
    asm volatile("" : "+v"(l));                                                                             \
    if ((S) > 0) {                                                                                          \
      _Pragma("unroll") for (int dt = 0; dt < 4; ++dt)                                                      \
        _Pragma("unroll") for (int i = 0; i < 16; ++i) o[dt][i] *= alpha;                                   \
    }                                                                                                       \
    _Pragma("unroll") for (int ss = 0; ss < 2; ++ss) {                                                      \
      u32x4 u0, u1;                                                                                         \
      _Pragma("unroll") for (int e = 0; e < 4; ++e) {                                                       \
        u0[e] = pk2(s0[8 * ss + 2 * e], s0[8 * ss + 2 * e + 1]);                                            \
        u1[e] = pk2(s1[8 * ss + 2 * e], s1[8 * ss + 2 * e + 1]);                                            \
      }                                                                                                     \
      pf[ss] = __builtin_bit_cast(bf16x8, u0);                                                              \
      pf[2 + ss] = __builtin_bit_cast(bf16x8, u1);                                                          \
    }                                                                                                       \
  } while (0)
#define DMA_A(S)                                                                                            \
  do {                                                                                                      \
    if ((S) == 0) issue_tile8(8);                                                                           \
    if ((S) == 1) issue_tile8(10);                                                                          \
    if ((S) == 2) { issue_tile8(12); issue_tile8(13); }                                                     \
    if ((S) == 3) {                                                                                         \
                    \
      const char* wb = (const char*)wo + (size_t)(kvh * 512 + 16 * b + 2 * wid) * 1024;                     \
      glds16(wb, lane * 16, lds0 + 6 * 16384 + 2 * wid * 1024);                                             \
      glds16(wb + 1024, lane * 16, lds0 + 6 * 16384 + (2 * wid + 1) * 1024);                                \
    }                                                                                                       \
  } while (0)
#define DMA_B(S)                                                                                            \
  do {                                                                                                      \
    if ((S) == 0) issue_tile8(9);                                                                           \
    if ((S) == 1) issue_tile8(11);                                                                          \
    if ((S) == 2) { issue_tile8(14); issue_tile8(15); }                                                     \
  } while (0)
#define STEP_WAIT(S)                                                                                        \
  do {                                                                                                      \
    if ((S) == 0) asm volatile("s_waitcnt vmcnt(12)" ::: "memory");                                         \
    if ((S) == 1 || (S) == 2) asm volatile("s_waitcnt vmcnt(4)" ::: "memory");                              \
    if ((S) == 3) asm volatile("s_waitcnt vmcnt(2)" ::: "memory");                                          \
  } while (0)

  if (u == 0) {
#pragma unroll
    for (int s = 0; s < 4; ++s) {
      wg_barrier();
      STAMP(2 + 2 * s);
      DMA_A(s);
      DMA_B(s);
      if (s > 0) {
        V_READ(s - 1, 0);
        V_READ(s - 1, 1);
        V_READ(s - 1, 2);
        V_READ(s - 1, 3);
        SBAR();
        PV_MMA(0);
        PV_MMA(2);
        SBAR();
      }
      K_READ(s);
      SBAR();
      QK_MMA();
      SBAR();
      STAMP(3 + 2 * s);
      SOFTMAX(s);
      SBAR();
      STEP_WAIT(s);
    }
    wg_barrier();
    V_READ(3, 0);
    V_READ(3, 1);
    V_READ(3, 2);
    V_READ(3, 3);
    SBAR();
    PV_MMA(0);
    PV_MMA(2);
    SBAR();
  } else {
#pragma unroll
    for (int s = 0; s < 5; ++s) {
      wg_barrier();
      if (s < 4) { DMA_A(s); DMA_B(s); }
      if (s > 0) {
        V_READ(s - 1, 0);
        V_READ(s - 1, 1);
        SBAR();
        SOFTMAX(s - 1);
        SBAR();
        V_READ(s - 1, 2);
        V_READ(s - 1, 3);
        SBAR();
        PV_MMA(0);
        PV_MMA(2);
        SBAR();
      }
      if (s < 4) {
        K_READ(s);
        SBAR();
        QK_MMA();
        SBAR();
        STEP_WAIT(s);
      }
    }
  }
  const auto wo_rs = __builtin_amdgcn_make_buffer_rsrc((void*)wo, 0, HQ * DH * DM * 2, 0x00020000);
  const int wo_voff = (kvh * 512 + 8 * q) * 1024 + (hh * 128 + 8 * r16) * 2;
  u32x4 w[QD][8];
  if (u == 0) {
#pragma unroll
    for (int d = 0; d < QD; ++d)
#pragma unroll
      for (int jj = 0; jj < 8; ++jj)
        w[d][jj] = __builtin_amdgcn_raw_buffer_load_b128(wo_rs, wo_voff, (32 * ((d + rot) & 15) + jj) * 1024, 0);
  }
  SBAR();
  l = swap_half_add(l);
  wg_barrier();
  STAMP(10);
  {
    char* xb_ = smem + wid * 8448;
#pragma unroll
    for (int c = 0; c < 2; ++c) {
#pragma unroll
      for (int g = 0; g < 4; ++g) {
        f32x4 v;
        if (u == 0) v = f32x4{o[2 + c][4 * g], o[2 + c][4 * g + 1], o[2 + c][4 * g + 2], o[2 + c][4 * g + 3]};
        else v = f32x4{o[c][4 * g], o[c][4 * g + 1], o[c][4 * g + 2], o[c][4 * g + 3]};
        *reinterpret_cast<f32x4*>(xb_ + (c * 4 + g) * 1024 + lane * 16) = v;
      }
    }
    if (h == 0) {
      f32x2 ml = {m, l};
      *reinterpret_cast<f32x2*>(xb_ + 8192 + r * 8) = ml;
    }
    wg_barrier();
    const char* pb = smem + (wid ^ 4) * 8448;
    const f32x2 ml2 = *reinterpret_cast<const f32x2*>(pb + 8192 + r * 8);
    const float mm = fmaxf(m, ml2[0]);
    const float e1 = __builtin_amdgcn_exp2f((m - mm) * CS), e2 = __builtin_amdgcn_exp2f((ml2[0] - mm) * CS);
    const float inv = 1.0f / (l * e1 + ml2[1] * e2);
    const float f1 = e1 * inv, f2 = e2 * inv;
    char* of = smem + QX_OFF + (r >> 4) * 1024 + (r & 15) * 16 + 8 * h;
    f32x4 pv[8];
#pragma unroll
    for (int c = 0; c < 8; ++c) pv[c] = *reinterpret_cast<const f32x4*>(pb + c * 1024 + lane * 16);
    SBAR();
#pragma unroll
    for (int c = 0; c < 2; ++c) {
      const int dt = 2 * u + c;
#pragma unroll
      for (int g = 0; g < 4; ++g) {
        f32x4 ov;
        if (u == 0) ov = f32x4{o[c][4 * g], o[c][4 * g + 1], o[c][4 * g + 2], o[c][4 * g + 3]};
        else ov = f32x4{o[2 + c][4 * g], o[2 + c][4 * g + 1], o[2 + c][4 * g + 2], o[2 + c][4 * g + 3]};
        const f32x4 p2 = pv[c * 4 + g];
        u32x2 v;
        v[0] = pk2(ov[0] * f1 + p2[0] * f2, ov[1] * f1 + p2[1] * f2);
        v[1] = pk2(ov[2] * f1 + p2[2] * f2, ov[3] * f1 + p2[3] * f2);
        *reinterpret_cast<u32x2*>(of + (hh * 4 + dt) * 2048 + g * 256) = v;
      }
    }
  }
  if (u == 1) {
    asm volatile("s_waitcnt vmcnt(0)" ::: "memory");
    wg_barrier();
    return;
  }
  {
    wg_barrier();
    STAMP(11);
    f32x4 acc[8][2];
#pragma unroll
    for (int i = 0; i < 8; ++i) { acc[i][0] = f32x4{}; acc[i][1] = f32x4{}; }
    const char* of = smem + QX_OFF + lane * 16;
#pragma unroll
    for (int s = 0; s < 16; ++s) {
      SBAR();
      const bf16x8 b0 = *reinterpret_cast<const bf16x8*>(of + ((s + rot) & 15) * 2048);
      const bf16x8 b1 = *reinterpret_cast<const bf16x8*>(of + ((s + rot) & 15) * 2048 + 1024);
      PROJ_STEP(w[s % QD], b0, b1, if (s + QD < 16) {
        _Pragma("unroll") for (int jj = 0; jj < 8; ++jj)
          w[s % QD][jj] = __builtin_amdgcn_raw_buffer_load_b128(wo_rs, wo_voff, (32 * ((s + QD + rot) & 15) + jj) * 1024, 0);
      });
    }
    STAMP(12);
    char* pp = ows + ((size_t)((kvh * NB + b) * 4 + hh)) * 8192 + lane * 16;
#pragma unroll
    for (int T = 0; T < 2; ++T)
#pragma unroll
      for (int g = 0; g < 4; ++g) {
        f32x4 v0 = {acc[0][T][g], acc[1][T][g], acc[2][T][g], acc[3][T][g]};
        f32x4 v1 = {acc[4][T][g], acc[5][T][g], acc[6][T][g], acc[7][T][g]};
        u32x4 pk = {pk2(v0[0], v0[1]), pk2(v0[2], v0[3]), pk2(v1[0], v1[1]), pk2(v1[2], v1[3])};
        *reinterpret_cast<u32x4*>(pp + (T * 4 + g) * 1024) = pk;
      }
    STAMP(13);
  }
}

__global__ __launch_bounds__(256) void reduce_kernel(const char* __restrict__ ows, __hip_bfloat16* __restrict__ out) {
  const int gid = blockIdx.x * 256 + threadIdx.x;
  const int lane = gid & 63, tg = (gid >> 6) & 7, bc = gid >> 9;
  const char* pp = ows + (size_t)bc * 8192 + tg * 1024 + lane * 16;
  u32x4 v[8];
#pragma unroll
  for (int k = 0; k < 8; ++k) v[k] = *reinterpret_cast<const u32x4*>(pp + (size_t)k * (NB * 4 * 8192));
  f32x4 a = {0.f, 0.f, 0.f, 0.f}, c = {0.f, 0.f, 0.f, 0.f};
#pragma unroll
  for (int k = 0; k < 8; ++k) {
    a[0] += __uint_as_float(v[k][0] << 16); a[1] += __uint_as_float(v[k][0] & 0xffff0000u);
    a[2] += __uint_as_float(v[k][1] << 16); a[3] += __uint_as_float(v[k][1] & 0xffff0000u);
    c[0] += __uint_as_float(v[k][2] << 16); c[1] += __uint_as_float(v[k][2] & 0xffff0000u);
    c[2] += __uint_as_float(v[k][3] << 16); c[3] += __uint_as_float(v[k][3] & 0xffff0000u);
  }
  const int b = bc >> 2, cg = bc & 3, T = tg >> 2, g = tg & 3, r16 = lane & 15, q = lane >> 4;
  u32x4 o = {pk2(a[0], a[1]), pk2(a[2], a[3]), pk2(c[0], c[1]), pk2(c[2], c[3])};
  *reinterpret_cast<u32x4*>(out + (size_t)(b * SQ + 16 * T + r16) * DM + 128 * cg + 32 * q + 8 * g) = o;
}

extern "C" void kernel_launch(void* const* d_in, const int* in_sizes, int n_in, void* d_out, int out_size,
                              void* d_ws, size_t ws_size, hipStream_t stream) {
  const __hip_bfloat16* x = (const __hip_bfloat16*)d_in[0];
  const __hip_bfloat16* kc = (const __hip_bfloat16*)d_in[1];
  const __hip_bfloat16* vc = (const __hip_bfloat16*)d_in[2];
  const __hip_bfloat16* wq = (const __hip_bfloat16*)d_in[3];
  const __hip_bfloat16* wo = (const __hip_bfloat16*)d_in[4];
  (void)hipFuncSetAttribute((const void*)attn_kernel, hipFuncAttributeMaxDynamicSharedMemorySize, LDS_BYTES);
  attn_kernel<<<256, 512, LDS_BYTES, stream>>>(x, kc, vc, wq, wo, (char*)d_ws);
  reduce_kernel<<<256, 256, 0, stream>>>((const char*)d_ws, (__hip_bfloat16*)d_out);
}
```

```cpp
#include <hip/hip_runtime.h>
#include <hip/hip_bf16.h>

typedef __bf16 bf16x8 __attribute__((ext_vector_type(8)));
typedef __bf16 bf16x2 __attribute__((ext_vector_type(2)));
typedef short s16x4 __attribute__((ext_vector_type(4)));
typedef float f32x2 __attribute__((ext_vector_type(2)));
typedef float f32x4 __attribute__((ext_vector_type(4)));
typedef float f32x16 __attribute__((ext_vector_type(16)));
typedef unsigned u32x2 __attribute__((ext_vector_type(2)));
typedef unsigned u32x4 __attribute__((ext_vector_type(4)));
typedef __attribute__((address_space(3))) s16x4* lds_s16x4_ptr;

#define DI __device__ __forceinline__
#define MFMA32(a, b, c) __builtin_amdgcn_mfma_f32_32x32x16_bf16((a), (b), (c), 0, 0, 0)
#define MFMA16(a, b, c) __builtin_amdgcn_mfma_f32_16x16x32_bf16((a), (b), (c), 0, 0, 0)
#define SBAR() __builtin_amdgcn_sched_barrier(0)
#define STAMP(i)

constexpr int NB = 32, SQ = 32, DM = 512, HQ = 32, HKV = 8, SK = 512, DH = 128;
constexpr int LDS_BYTES = 163840;
constexpr int QX_OFF = 131072;

DI unsigned pk2(float a, float b) {
  f32x2 f = {a, b};
  return __builtin_bit_cast(unsigned, __builtin_convertvector(f, bf16x2));
}
DI float swap_half_max(float v) {
  auto rr = __builtin_amdgcn_permlane32_swap(__float_as_uint(v), __float_as_uint(v), false, false);
  return fmaxf(__uint_as_float(rr[0]), __uint_as_float(rr[1]));
}
DI float swap_half_add(float v) {
  auto rr = __builtin_amdgcn_permlane32_swap(__float_as_uint(v), __float_as_uint(v), false, false);
  return __uint_as_float(rr[0]) + __uint_as_float(rr[1]);
}
DI void wg_barrier() {
  asm volatile("s_waitcnt lgkmcnt(0)" ::: "memory");
  __builtin_amdgcn_s_barrier();
  asm volatile("" ::: "memory");
}
DI void glds16_nt(const void* sbase, unsigned voff, unsigned lds_dst) {
  unsigned keep;
  asm volatile("s_mov_b32 %0, m0\n\ts_mov_b32 m0, %3\n\ts_nop 0\n\tglobal_load_lds_dwordx4 %1, %2 nt\n\ts_mov_b32 m0, %0"
               : "=&s"(keep) : "v"(voff), "s"(sbase), "s"(lds_dst) : "memory");
}
DI void glds16(const void* sbase, unsigned voff, unsigned lds_dst) {
  unsigned keep;
  asm volatile("s_mov_b32 %0, m0\n\ts_mov_b32 m0, %3\n\ts_nop 0\n\tglobal_load_lds_dwordx4 %1, %2\n\ts_mov_b32 m0, %0"
               : "=&s"(keep) : "v"(voff), "s"(sbase), "s"(lds_dst) : "memory");
}
template <int I>
DI bf16x8 tr_frag(const u32x4* w) {
  u32x4 o;
  constexpr unsigned sel = (I & 1) ? 0x07060302u : 0x05040100u;
#pragma unroll
  for (int m = 0; m < 4; ++m) o[m] = __builtin_amdgcn_perm(w[2 * m + 1][I >> 1], w[2 * m][I >> 1], sel);
  return __builtin_bit_cast(bf16x8, o);
}
#define PROJ_STEP(W, B0, B1, LOADS)                           \
  do {                                                        \
    bf16x8 a_[8];                                             \
    a_[0] = tr_frag<0>(W); a_[1] = tr_frag<1>(W); a_[2] = tr_frag<2>(W); a_[3] = tr_frag<3>(W); \
    a_[4] = tr_frag<4>(W); a_[5] = tr_frag<5>(W); a_[6] = tr_frag<6>(W); a_[7] = tr_frag<7>(W); \
    SBAR();                                                   \
    _Pragma("unroll") for (int i_ = 0; i_ < 8; ++i_) {        \
      acc[i_][0] = MFMA16(a_[i_], B0, acc[i_][0]);            \
      acc[i_][1] = MFMA16(a_[i_], B1, acc[i_][1]);            \
    }                                                         \
    LOADS;                                                    \
    _Pragma("unroll") for (int i_ = 0; i_ < 8; ++i_) {        \
      __builtin_amdgcn_sched_group_barrier(0x8, 2, 0);        \
      __builtin_amdgcn_sched_group_barrier(0x20, 1, 0);       \
    }                                                         \
  } while (0)

constexpr int QD = 3;

__global__ __launch_bounds__(512, 2) void attn_kernel(const __hip_bfloat16* __restrict__ x,
                                                      const __hip_bfloat16* __restrict__ kc,
                                                      const __hip_bfloat16* __restrict__ vc,
                                                      const __hip_bfloat16* __restrict__ wq,
                                                      const __hip_bfloat16* __restrict__ wo,
                                                      char* __restrict__ ows) {
  extern __shared__ __attribute__((aligned(16))) char smem[];
  const int tid = threadIdx.x;
  const int wid = __builtin_amdgcn_readfirstlane(tid >> 6);
  const int lane = tid & 63;
  const int kvh = blockIdx.x & 7, b = blockIdx.x >> 3;
  const int hh = wid & 3, u = wid >> 2;
  const int r = lane & 31, h = lane >> 5;
  const int r16 = lane & 15, q = lane >> 4;
  const int grp = 1;
  const int rot = b & 15;
  STAMP(0);

  const unsigned lds0 = (unsigned)(size_t)smem;
  const size_t kvbase = ((size_t)(b * HKV + kvh)) * SK * DH;
  auto tile_isv = [](int t) { return t >= 14 ? 1 : (t < 2 ? 0 : (((t - 2) >> 1) & 1)); };
  auto tile_idx = [](int t) { return t >= 14 ? t - 8 : (t < 2 ? t : ((((t - 2) >> 1) & 1) ? 2 * ((t - 2) >> 2) + (t & 1) : 2 * ((t - 2) >> 2) + 2 + (t & 1))); };
  auto issue_tile = [&](int t) {
    const int slot = t < 10 ? t : t - 10;
    const int isv = tile_isv(t);
    const char* base = (const char*)((isv ? vc : kc) + kvbase + (size_t)tile_idx(t) * 64 * DH) + hh * 4096;
#pragma unroll
    for (int e = 0; e < 4; ++e) {
      const int chunk = isv ? (r16 ^ (q << 2)) : (r16 ^ (4 * e + q));
      glds16_nt(base + e * 1024, q * 256 + chunk * 16, lds0 + slot * 16384 + (4 * hh + e) * 1024);
    }
  };
  auto issue_tile8 = [&](int t) {
    const int slot = t < 10 ? t : t - 10;
    const int isv = tile_isv(t);
    const char* base = (const char*)((isv ? vc : kc) + kvbase + (size_t)tile_idx(t) * 64 * DH) + wid * 2048;
#pragma unroll
    for (int e = 0; e < 2; ++e) {
      const int chunk = isv ? (r16 ^ (q << 2)) : (r16 ^ (8 * (wid & 1) + 4 * e + q));
      glds16_nt(base + e * 1024, q * 256 + chunk * 16, lds0 + slot * 16384 + (2 * wid + e) * 1024);
    }
  };
  {
    const __hip_bfloat16* xb = x + (size_t)b * SQ * DM;
#pragma unroll
    for (int i = 0; i < 4; ++i) {
      const int row = 4 * wid + i;
      glds16(xb + row * DM, (lane ^ (row & 15)) * 16, lds0 + QX_OFF + row * 1024);
    }
  }

  bf16x8 qf[8];
  if (u == 1) {
    if (grp == 0) {
#pragma unroll
      for (int t = 0; t < 8; ++t) issue_tile(t);
      asm volatile("s_waitcnt vmcnt(32)" ::: "memory");
    } else {
      asm volatile("s_waitcnt vmcnt(0)" ::: "memory");
    }
    wg_barrier();
    wg_barrier();
    if (grp == 1) {
#pragma unroll
      for (int t = 0; t < 3; ++t) issue_tile(t);
    }
    wg_barrier();
    wg_barrier();
    if (grp == 1) {
#pragma unroll
      for (int t = 3; t < 8; ++t) issue_tile(t);
    }
#pragma unroll
    for (int p = 0; p < 8; ++p)
      qf[p] = *reinterpret_cast<const bf16x8*>(smem + QX_OFF + (hh * 8 + p) * 1024 + lane * 16);
    asm volatile("s_waitcnt vmcnt(24)" ::: "memory");
  } else {
    const auto wq_rs = __builtin_amdgcn_make_buffer_rsrc((void*)wq, 0, DM * HQ * DH * 2, 0x00020000);
    const int w_voff = (8 * q) * 8192 + (kvh * 512 + hh * 128 + 8 * r16) * 2;
    u32x4 w[QD][8];
#pragma unroll
    for (int d = 0; d < QD; ++d) {
      const int sr = (d + rot) & 15;
#pragma unroll
      for (int jj = 0; jj < 8; ++jj)
        w[d][jj] = __builtin_amdgcn_raw_buffer_load_b128(wq_rs, w_voff, (32 * sr + jj) * 8192, 0);
    }
    asm volatile("s_waitcnt vmcnt(24)" ::: "memory");
    wg_barrier();
    STAMP(14);
    f32x4 acc[8][2];
#pragma unroll
    for (int i = 0; i < 8; ++i) { acc[i][0] = f32x4{}; acc[i][1] = f32x4{}; }
    const char* xs = smem + QX_OFF + r16 * 1024;
#pragma unroll
    for (int s = 0; s < 16; ++s) {
      SBAR();
      const int xo = ((4 * ((s + rot) & 15) + q) ^ r16) << 4;
      const bf16x8 b0 = *reinterpret_cast<const bf16x8*>(xs + xo);
      const bf16x8 b1 = *reinterpret_cast<const bf16x8*>(xs + xo + 16384);
      const int sr = (s + QD + rot) & 15;
      PROJ_STEP(w[s % QD], b0, b1, if (s + QD < 16) {
        _Pragma("unroll") for (int jj = 0; jj < 8; ++jj)
          w[s % QD][jj] = __builtin_amdgcn_raw_buffer_load_b128(wq_rs, w_voff, (32 * sr + jj) * 8192, 0);
      });
      if (s + QD == 15) { SBAR(); __builtin_amdgcn_s_barrier(); }
    }
#pragma unroll
    for (int g = 0; g < 4; ++g) {
      u32x4 f0, f1;
#pragma unroll
      for (int e = 0; e < 4; ++e) {
        const unsigned a0 = pk2(acc[2 * e][0][g], acc[2 * e + 1][0][g]);
        const unsigned a1 = pk2(acc[2 * e][1][g], acc[2 * e + 1][1][g]);
        auto rr = __builtin_amdgcn_permlane16_swap(a0, a1, false, false);
        f0[e] = rr[0];
        f1[e] = rr[1];
      }
      qf[g] = __builtin_bit_cast(bf16x8, f0);
      qf[4 + g] = __builtin_bit_cast(bf16x8, f1);
    }
    wg_barrier();
#pragma unroll
    for (int p = 0; p < 8; ++p)
      *reinterpret_cast<bf16x8*>(smem + QX_OFF + (hh * 8 + p) * 1024 + lane * 16) = qf[p];
    STAMP(1);
    wg_barrier();
  }

  constexpr float CS = 0.088388347648318447f * 1.4426950408889634f;
  float m = -__builtin_inff(), l = 0.f;
  f32x16 o[4];
#pragma unroll
  for (int i = 0; i < 4; ++i) o[i] = f32x16{};
  const int ky = (r & 15) ^ (8 * h);
  const int koff = r * 256;
  const int vq = (lane >> 2) & 3, vpp = lane & 3;
  const int voff = (4 * h + vq) * 256 + (2 * (q & 1) + (vpp >> 1)) * 16 + 8 * (vpp & 1);
  bf16x8 fb[16];
  bf16x8 pf[4];
  f32x16 s0, s1;
#define SLOT(T) (smem + (((T) < 10 ? (T) : (T) - 10) + u) * 16384)
#define KT(S) ((S) == 0 ? 0 : 4 * (S) - 2)
#define VT(S) ((S) == 3 ? 14 : 4 * (S) + 4)
#define K_READ(S)                                                                                           \
  do {                                                                                                      \
    const char* Ks = SLOT(KT(S));                                                                         \
    _Pragma("unroll") for (int p = 0; p < 8; ++p) {                                                         \
      const char* a = Ks + koff + ((p ^ ky) << 4);                                                          \
      fb[2 * p] = *reinterpret_cast<const bf16x8*>(a);                                                      \
      fb[2 * p + 1] = *reinterpret_cast<const bf16x8*>(a + 8192);                                           \
    }                                                                                                       \
  } while (0)
#define QK_MMA()                                                                                            \
  do {                                                                                                      \
    s0 = f32x16{}; s1 = f32x16{};                                                                           \
    _Pragma("unroll") for (int p = 0; p < 8; ++p) {                                                         \
      s0 = MFMA32(fb[2 * p], qf[p], s0);                                                                    \
      s1 = MFMA32(fb[2 * p + 1], qf[p], s1);                                                                \
    }                                                                                                       \
  } while (0)
#define V_READ(S, DT)                                                                                       \
  do {                                                                                                      \
    const char* vb = SLOT(VT(S)) + voff + (((DT) ^ vq) << 6);                                         \
    _Pragma("unroll") for (int ks = 0; ks < 4; ++ks) {                                                      \
      const s16x4 lo = __builtin_amdgcn_ds_read_tr16_b64_v4i16((lds_s16x4_ptr)(vb + ks * 4096));            \
      const s16x4 hi = __builtin_amdgcn_ds_read_tr16_b64_v4i16((lds_s16x4_ptr)(vb + ks * 4096 + 2048));     \
      fb[(DT) * 4 + ks] = __builtin_bit_cast(bf16x8, __builtin_shufflevector(lo, hi, 0, 1, 2, 3, 4, 5, 6, 7)); \
    }                                                                                                       \
  } while (0)
#define PV_MMA(D0)                                                                                          \
  do {                                                                                                      \
    _Pragma("unroll") for (int ks = 0; ks < 4; ++ks)                                                        \
      _Pragma("unroll") for (int dt = (D0); dt < (D0) + 2; ++dt) o[dt] = MFMA32(fb[dt * 4 + ks], pf[ks], o[dt]); \
  } while (0)
#define SOFTMAX(S)                                                                                          \
  do {                                                                                                      \
    float tmax = s0[0];                                                                                     \
    _Pragma("unroll") for (int i = 1; i < 16; ++i) tmax = fmaxf(tmax, s0[i]);                               \
    _Pragma("unroll") for (int i = 0; i < 16; ++i) tmax = fmaxf(tmax, s1[i]);                               \
    tmax = swap_half_max(tmax);                                                                             \
    const float mnew = fmaxf(m, tmax);                                                                      \
    const float alpha = __builtin_amdgcn_exp2f((m - mnew) * CS);                                            \
    m = mnew;                                                                                               \
    const float mc = mnew * CS;                                                                             \
    float ps = 0.f;                                                                                         \
    _Pragma("unroll") for (int i = 0; i < 16; ++i) { s0[i] = __builtin_amdgcn_exp2f(fmaf(s0[i], CS, -mc)); ps += s0[i]; } \
    _Pragma("unroll") for (int i = 0; i < 16; ++i) { s1[i] = __builtin_amdgcn_exp2f(fmaf(s1[i], CS, -mc)); ps += s1[i]; } \
    l = l * alpha + ps;                                                                                     \
    asm volatile("" : "+v"(l));                                                                             \
    if ((S) > 0) {                                                                                          \
      _Pragma("unroll") for (int dt = 0; dt < 4; ++dt)                                                      \
        _Pragma("unroll") for (int i = 0; i < 16; ++i) o[dt][i] *= alpha;                                   \
    }                                                                                                       \
    _Pragma("unroll") for (int ss = 0; ss < 2; ++ss) {                                                      \
      u32x4 u0, u1;                                                                                         \
      _Pragma("unroll") for (int e = 0; e < 4; ++e) {                                                       \
        u0[e] = pk2(s0[8 * ss + 2 * e], s0[8 * ss + 2 * e + 1]);                                            \
        u1[e] = pk2(s1[8 * ss + 2 * e], s1[8 * ss + 2 * e + 1]);                                            \
      }                                                                                                     \
      pf[ss] = __builtin_bit_cast(bf16x8, u0);                                                              \
      pf[2 + ss] = __builtin_bit_cast(bf16x8, u1);                                                          \
    }                                                                                                       \
  } while (0)
#define DMA_A(S)                                                                                            \
  do {                                                                                                      \
    if ((S) == 0) issue_tile8(8);                                                                           \
    if ((S) == 1) issue_tile8(10);                                                                          \
    if ((S) == 2) { issue_tile8(12); issue_tile8(13); }                                                     \
    if ((S) == 3) {                                                                                         \
                    \
      const char* wb = (const char*)wo + (size_t)(kvh * 512 + 16 * b + 2 * wid) * 1024;                     \
      glds16(wb, lane * 16, lds0 + 6 * 16384 + 2 * wid * 1024);                                             \
      glds16(wb + 1024, lane * 16, lds0 + 6 * 16384 + (2 * wid + 1) * 1024);                                \
    }                                                                                                       \
  } while (0)
#define DMA_B(S)                                                                                            \
  do {                                                                                                      \
    if ((S) == 0) issue_tile8(9);                                                                           \
    if ((S) == 1) issue_tile8(11);                                                                          \
    if ((S) == 2) { issue_tile8(14); issue_tile8(15); }                                                     \
  } while (0)
#define STEP_WAIT(S)                                                                                        \
  do {                                                                                                      \
    if ((S) == 0) asm volatile("s_waitcnt vmcnt(12)" ::: "memory");                                         \
    if ((S) == 1 || (S) == 2) asm volatile("s_waitcnt vmcnt(4)" ::: "memory");                              \
    if ((S) == 3) asm volatile("s_waitcnt vmcnt(2)" ::: "memory");                                          \
  } while (0)

  if (u == 0) {
#pragma unroll
    for (int s = 0; s < 4; ++s) {
      wg_barrier();
      STAMP(2 + 2 * s);
      DMA_A(s);
      DMA_B(s);
      if (s > 0) {
        V_READ(s - 1, 0);
        V_READ(s - 1, 1);
        V_READ(s - 1, 2);
        V_READ(s - 1, 3);
        SBAR();
        PV_MMA(0);
        PV_MMA(2);
        SBAR();
      }
      K_READ(s);
      SBAR();
      QK_MMA();
      SBAR();
      STAMP(3 + 2 * s);
      SOFTMAX(s);
      SBAR();
      STEP_WAIT(s);
    }
    wg_barrier();
    V_READ(3, 0);
    V_READ(3, 1);
    V_READ(3, 2);
    V_READ(3, 3);
    SBAR();
    PV_MMA(0);
    PV_MMA(2);
    SBAR();
  } else {
#pragma unroll
    for (int s = 0; s < 5; ++s) {
      wg_barrier();
      if (s < 4) { DMA_A(s); DMA_B(s); }
      if (s > 0) {
        V_READ(s - 1, 0);
        V_READ(s - 1, 1);
        SBAR();
        SOFTMAX(s - 1);
        SBAR();
        V_READ(s - 1, 2);
        V_READ(s - 1, 3);
        SBAR();
        PV_MMA(0);
        PV_MMA(2);
        SBAR();
      }
      if (s < 4) {
        K_READ(s);
        SBAR();
        QK_MMA();
        SBAR();
        STEP_WAIT(s);
      }
    }
  }
  const auto wo_rs = __builtin_amdgcn_make_buffer_rsrc((void*)wo, 0, HQ * DH * DM * 2, 0x00020000);
  const int wo_voff = (kvh * 512 + 8 * q) * 1024 + (hh * 128 + 8 * r16) * 2;
  u32x4 w[QD][8];
  if (u == 0) {
#pragma unroll
    for (int d = 0; d < QD; ++d)
#pragma unroll
      for (int jj = 0; jj < 8; ++jj)
        w[d][jj] = __builtin_amdgcn_raw_buffer_load_b128(wo_rs, wo_voff, (32 * ((d + rot) & 15) + jj) * 1024, 0);
  }
  SBAR();
  l = swap_half_add(l);
  wg_barrier();
  STAMP(10);
  {
    char* xb_ = smem + wid * 8448;
#pragma unroll
    for (int c = 0; c < 2; ++c) {
#pragma unroll
      for (int g = 0; g < 4; ++g) {
        f32x4 v;
        if (u == 0) v = f32x4{o[2 + c][4 * g], o[2 + c][4 * g + 1], o[2 + c][4 * g + 2], o[2 + c][4 * g + 3]};
        else v = f32x4{o[c][4 * g], o[c][4 * g + 1], o[c][4 * g + 2], o[c][4 * g + 3]};
        *reinterpret_cast<f32x4*>(xb_ + (c * 4 + g) * 1024 + lane * 16) = v;
      }
    }
    if (h == 0) {
      f32x2 ml = {m, l};
      *reinterpret_cast<f32x2*>(xb_ + 8192 + r * 8) = ml;
    }
    wg_barrier();
    const char* pb = smem + (wid ^ 4) * 8448;
    const f32x2 ml2 = *reinterpret_cast<const f32x2*>(pb + 8192 + r * 8);
    const float mm = fmaxf(m, ml2[0]);
    const float e1 = __builtin_amdgcn_exp2f((m - mm) * CS), e2 = __builtin_amdgcn_exp2f((ml2[0] - mm) * CS);
    const float inv = 1.0f / (l * e1 + ml2[1] * e2);
    const float f1 = e1 * inv, f2 = e2 * inv;
    char* of = smem + QX_OFF + (r >> 4) * 1024 + (r & 15) * 16 + 8 * h;
    f32x4 pv[8];
#pragma unroll
    for (int c = 0; c < 8; ++c) pv[c] = *reinterpret_cast<const f32x4*>(pb + c * 1024 + lane * 16);
    SBAR();
#pragma unroll
    for (int c = 0; c < 2; ++c) {
      const int dt = 2 * u + c;
#pragma unroll
      for (int g = 0; g < 4; ++g) {
        f32x4 ov;
        if (u == 0) ov = f32x4{o[c][4 * g], o[c][4 * g + 1], o[c][4 * g + 2], o[c][4 * g + 3]};
        else ov = f32x4{o[2 + c][4 * g], o[2 + c][4 * g + 1], o[2 + c][4 * g + 2], o[2 + c][4 * g + 3]};
        const f32x4 p2 = pv[c * 4 + g];
        u32x2 v;
        v[0] = pk2(ov[0] * f1 + p2[0] * f2, ov[1] * f1 + p2[1] * f2);
        v[1] = pk2(ov[2] * f1 + p2[2] * f2, ov[3] * f1 + p2[3] * f2);
        *reinterpret_cast<u32x2*>(of + (hh * 4 + dt) * 2048 + g * 256) = v;
      }
    }
  }
  if (u == 1) {
    asm volatile("s_waitcnt vmcnt(0)" ::: "memory");
    wg_barrier();
    return;
  }
  {
    wg_barrier();
    STAMP(11);
    f32x4 acc[8][2];
#pragma unroll
    for (int i = 0; i < 8; ++i) { acc[i][0] = f32x4{}; acc[i][1] = f32x4{}; }
    const char* of = smem + QX_OFF + lane * 16;
#pragma unroll
    for (int s = 0; s < 16; ++s) {
      SBAR();
      const bf16x8 b0 = *reinterpret_cast<const bf16x8*>(of + ((s + rot) & 15) * 2048);
      const bf16x8 b1 = *reinterpret_cast<const bf16x8*>(of + ((s + rot) & 15) * 2048 + 1024);
      PROJ_STEP(w[s % QD], b0, b1, if (s + QD < 16) {
        _Pragma("unroll") for (int jj = 0; jj < 8; ++jj)
          w[s % QD][jj] = __builtin_amdgcn_raw_buffer_load_b128(wo_rs, wo_voff, (32 * ((s + QD + rot) & 15) + jj) * 1024, 0);
      });
    }
    STAMP(12);
    char* pp = ows + ((size_t)((kvh * NB + b) * 4 + hh)) * 16384 + lane * 16;
#pragma unroll
    for (int T = 0; T < 2; ++T)
#pragma unroll
      for (int g = 0; g < 4; ++g) {
        f32x4 v0 = {acc[0][T][g], acc[1][T][g], acc[2][T][g], acc[3][T][g]};
        f32x4 v1 = {acc[4][T][g], acc[5][T][g], acc[6][T][g], acc[7][T][g]};
        *reinterpret_cast<f32x4*>(pp + ((T * 4 + g) * 2) * 1024) = v0;
        *reinterpret_cast<f32x4*>(pp + ((T * 4 + g) * 2 + 1) * 1024) = v1;
      }
    STAMP(13);
  }
}

__global__ __launch_bounds__(256) void reduce_kernel(const char* __restrict__ ows, __hip_bfloat16* __restrict__ out) {
  const int gid = blockIdx.x * 256 + threadIdx.x;
  const int lane = gid & 63, tg = (gid >> 6) & 7, bc = gid >> 9;
  const char* pp = ows + (size_t)bc * 16384 + tg * 2048 + lane * 16;
  f32x4 v0[8], v1[8];
#pragma unroll
  for (int k = 0; k < 8; ++k) {
    v0[k] = *reinterpret_cast<const f32x4*>(pp + (size_t)k * (NB * 4 * 16384));
    v1[k] = *reinterpret_cast<const f32x4*>(pp + (size_t)k * (NB * 4 * 16384) + 1024);
  }
  f32x4 a = v0[0], c = v1[0];
#pragma unroll
  for (int k = 1; k < 8; ++k) { a += v0[k]; c += v1[k]; }
  const int b = bc >> 2, cg = bc & 3, T = tg >> 2, g = tg & 3, r16 = lane & 15, q = lane >> 4;
  u32x4 o = {pk2(a[0], a[1]), pk2(a[2], a[3]), pk2(c[0], c[1]), pk2(c[2], c[3])};
  *reinterpret_cast<u32x4*>(out + (size_t)(b * SQ + 16 * T + r16) * DM + 128 * cg + 32 * q + 8 * g) = o;
}

extern "C" void kernel_launch(void* const* d_in, const int* in_sizes, int n_in, void* d_out, int out_size,
                              void* d_ws, size_t ws_size, hipStream_t stream) {
  const __hip_bfloat16* x = (const __hip_bfloat16*)d_in[0];
  const __hip_bfloat16* kc = (const __hip_bfloat16*)d_in[1];
  const __hip_bfloat16* vc = (const __hip_bfloat16*)d_in[2];
  const __hip_bfloat16* wq = (const __hip_bfloat16*)d_in[3];
  const __hip_bfloat16* wo = (const __hip_bfloat16*)d_in[4];
  (void)hipFuncSetAttribute((const void*)attn_kernel, hipFuncAttributeMaxDynamicSharedMemorySize, LDS_BYTES);
  attn_kernel<<<256, 512, LDS_BYTES, stream>>>(x, kc, vc, wq, wo, (char*)d_ws);
  reduce_kernel<<<256, 256, 0, stream>>>((const char*)d_ws, (__hip_bfloat16*)d_out);
}
```

```cpp
#include <hip/hip_runtime.h>
#include <hip/hip_bf16.h>

typedef __bf16 bf16x8 __attribute__((ext_vector_type(8)));
typedef __bf16 bf16x2 __attribute__((ext_vector_type(2)));
typedef short s16x4 __attribute__((ext_vector_type(4)));
typedef float f32x2 __attribute__((ext_vector_type(2)));
typedef float f32x4 __attribute__((ext_vector_type(4)));
typedef float f32x16 __attribute__((ext_vector_type(16)));
typedef unsigned u32x2 __attribute__((ext_vector_type(2)));
typedef unsigned u32x4 __attribute__((ext_vector_type(4)));
typedef __attribute__((address_space(3))) s16x4* lds_s16x4_ptr;

#define DI __device__ __forceinline__
#define MFMA32(a, b, c) __builtin_amdgcn_mfma_f32_32x32x16_bf16((a), (b), (c), 0, 0, 0)
#define MFMA16(a, b, c) __builtin_amdgcn_mfma_f32_16x16x32_bf16((a), (b), (c), 0, 0, 0)
#define SBAR() __builtin_amdgcn_sched_barrier(0)
#define STAMP(i)

constexpr int NB = 32, SQ = 32, DM = 512, HQ = 32, HKV = 8, SK = 512, DH = 128;
constexpr int LDS_BYTES = 163840;
constexpr int QX_OFF = 131072;

DI unsigned pk2(float a, float b) {
  f32x2 f = {a, b};
  return __builtin_bit_cast(unsigned, __builtin_convertvector(f, bf16x2));
}
DI float swap_half_max(float v) {
  auto rr = __builtin_amdgcn_permlane32_swap(__float_as_uint(v), __float_as_uint(v), false, false);
  return fmaxf(__uint_as_float(rr[0]), __uint_as_float(rr[1]));
}
DI float swap_half_add(float v) {
  auto rr = __builtin_amdgcn_permlane32_swap(__float_as_uint(v), __float_as_uint(v), false, false);
  return __uint_as_float(rr[0]) + __uint_as_float(rr[1]);
}
DI void wg_barrier() {
  asm volatile("s_waitcnt lgkmcnt(0)" ::: "memory");
  __builtin_amdgcn_s_barrier();
  asm volatile("" ::: "memory");
}
DI void glds16_nt(const void* sbase, unsigned voff, unsigned lds_dst) {
  unsigned keep;
  asm volatile("s_mov_b32 %0, m0\n\ts_mov_b32 m0, %3\n\ts_nop 0\n\tglobal_load_lds_dwordx4 %1, %2 nt\n\ts_mov_b32 m0, %0"
               : "=&s"(keep) : "v"(voff), "s"(sbase), "s"(lds_dst) : "memory");
}
DI void glds16(const void* sbase, unsigned voff, unsigned lds_dst) {
  unsigned keep;
  asm volatile("s_mov_b32 %0, m0\n\ts_mov_b32 m0, %3\n\ts_nop 0\n\tglobal_load_lds_dwordx4 %1, %2\n\ts_mov_b32 m0, %0"
               : "=&s"(keep) : "v"(voff), "s"(sbase), "s"(lds_dst) : "memory");
}
template <int I>
DI bf16x8 tr_frag(const u32x4* w) {
  u32x4 o;
  constexpr unsigned sel = (I & 1) ? 0x07060302u : 0x05040100u;
#pragma unroll
  for (int m = 0; m < 4; ++m) o[m] = __builtin_amdgcn_perm(w[2 * m + 1][I >> 1], w[2 * m][I >> 1], sel);
  return __builtin_bit_cast(bf16x8, o);
}
#define PROJ_STEP(W, B0, B1, LOADS)                           \
  do {                                                        \
    bf16x8 a_[8];                                             \
    a_[0] = tr_frag<0>(W); a_[1] = tr_frag<1>(W); a_[2] = tr_frag<2>(W); a_[3] = tr_frag<3>(W); \
    a_[4] = tr_frag<4>(W); a_[5] = tr_frag<5>(W); a_[6] = tr_frag<6>(W); a_[7] = tr_frag<7>(W); \
    SBAR();                                                   \
    _Pragma("unroll") for (int i_ = 0; i_ < 8; ++i_) {        \
      acc[i_][0] = MFMA16(a_[i_], B0, acc[i_][0]);            \
      acc[i_][1] = MFMA16(a_[i_], B1, acc[i_][1]);            \
    }                                                         \
    LOADS;                                                    \
    _Pragma("unroll") for (int i_ = 0; i_ < 8; ++i_) {        \
      __builtin_amdgcn_sched_group_barrier(0x8, 2, 0);        \
      __builtin_amdgcn_sched_group_barrier(0x20, 1, 0);       \
    }                                                         \
  } while (0)

constexpr int QD = 3;

__global__ __launch_bounds__(512, 2) void attn_kernel(const __hip_bfloat16* __restrict__ x,
                                                      const __hip_bfloat16* __restrict__ kc,
                                                      const __hip_bfloat16* __restrict__ vc,
                                                      const __hip_bfloat16* __restrict__ wq,
                                                      const __hip_bfloat16* __restrict__ wo,
                                                      char* __restrict__ ows) {
  extern __shared__ __attribute__((aligned(16))) char smem[];
  const int tid = threadIdx.x;
  const int wid = __builtin_amdgcn_readfirstlane(tid >> 6);
  const int lane = tid & 63;
  const int kvh = blockIdx.x & 7, b = blockIdx.x >> 3;
  const int hh = wid & 3, u = wid >> 2;
  const int r = lane & 31, h = lane >> 5;
  const int r16 = lane & 15, q = lane >> 4;
  const int grp = b & 1;
  const int rot = (b >> 1) & 15;
  STAMP(0);

  const unsigned lds0 = (unsigned)(size_t)smem;
  const size_t kvbase = ((size_t)(b * HKV + kvh)) * SK * DH;
  auto tile_isv = [](int t) { return t >= 14 ? 1 : (t < 2 ? 0 : (((t - 2) >> 1) & 1)); };
  auto tile_idx = [](int t) { return t >= 14 ? t - 8 : (t < 2 ? t : ((((t - 2) >> 1) & 1) ? 2 * ((t - 2) >> 2) + (t & 1) : 2 * ((t - 2) >> 2) + 2 + (t & 1))); };
  auto issue_tile = [&](int t) {
    const int slot = t < 10 ? t : t - 10;
    const int isv = tile_isv(t);
    const char* base = (const char*)((isv ? vc : kc) + kvbase + (size_t)tile_idx(t) * 64 * DH) + hh * 4096;
#pragma unroll
    for (int e = 0; e < 4; ++e) {
      const int chunk = isv ? (r16 ^ (q << 2)) : (r16 ^ (4 * e + q));
      glds16_nt(base + e * 1024, q * 256 + chunk * 16, lds0 + slot * 16384 + (4 * hh + e) * 1024);
    }
  };
  auto issue_tile8 = [&](int t) {
    const int slot = t < 10 ? t : t - 10;
    const int isv = tile_isv(t);
    const char* base = (const char*)((isv ? vc : kc) + kvbase + (size_t)tile_idx(t) * 64 * DH) + wid * 2048;
#pragma unroll
    for (int e = 0; e < 2; ++e) {
      const int chunk = isv ? (r16 ^ (q << 2)) : (r16 ^ (8 * (wid & 1) + 4 * e + q));
      glds16_nt(base + e * 1024, q * 256 + chunk * 16, lds0 + slot * 16384 + (2 * wid + e) * 1024);
    }
  };
  {
    const __hip_bfloat16* xb = x + (size_t)b * SQ * DM;
#pragma unroll
    for (int i = 0; i < 4; ++i) {
      const int row = 4 * wid + i;
      glds16(xb + row * DM, (lane ^ (row & 15)) * 16, lds0 + QX_OFF + row * 1024);
    }
  }

  bf16x8 qf[8];
  if (u == 1) {
    if (grp == 0) {
#pragma unroll
      for (int t = 0; t < 8; ++t) issue_tile(t);
      asm volatile("s_waitcnt vmcnt(32)" ::: "memory");
    } else {
      asm volatile("s_waitcnt vmcnt(0)" ::: "memory");
    }
    wg_barrier();
    wg_barrier();
    if (grp == 1) {
#pragma unroll
      for (int t = 0; t < 3; ++t) issue_tile(t);
    }
    wg_barrier();
    wg_barrier();
    if (grp == 1) {
#pragma unroll
      for (int t = 3; t < 8; ++t) issue_tile(t);
    }
#pragma unroll
    for (int p = 0; p < 8; ++p)
      qf[p] = *reinterpret_cast<const bf16x8*>(smem + QX_OFF + (hh * 8 + p) * 1024 + lane * 16);
    asm volatile("s_waitcnt vmcnt(24)" ::: "memory");
  } else {
    const auto wq_rs = __builtin_amdgcn_make_buffer_rsrc((void*)wq, 0, DM * HQ * DH * 2, 0x00020000);
    const int w_voff = (8 * q) * 8192 + (kvh * 512 + hh * 128 + 8 * r16) * 2;
    u32x4 w[QD][8];
#pragma unroll
    for (int d = 0; d < QD; ++d) {
      const int sr = (d + rot) & 15;
#pragma unroll
      for (int jj = 0; jj < 8; ++jj)
        w[d][jj] = __builtin_amdgcn_raw_buffer_load_b128(wq_rs, w_voff, (32 * sr + jj) * 8192, 0);
    }
    asm volatile("s_waitcnt vmcnt(24)" ::: "memory");
    wg_barrier();
    STAMP(14);
    f32x4 acc[8][2];
#pragma unroll
    for (int i = 0; i < 8; ++i) { acc[i][0] = f32x4{}; acc[i][1] = f32x4{}; }
    const char* xs = smem + QX_OFF + r16 * 1024;
#pragma unroll
    for (int s = 0; s < 16; ++s) {
      SBAR();
      const int xo = ((4 * ((s + rot) & 15) + q) ^ r16) << 4;
      const bf16x8 b0 = *reinterpret_cast<const bf16x8*>(xs + xo);
      const bf16x8 b1 = *reinterpret_cast<const bf16x8*>(xs + xo + 16384);
      const int sr = (s + QD + rot) & 15;
      PROJ_STEP(w[s % QD], b0, b1, if (s + QD < 16) {
        _Pragma("unroll") for (int jj = 0; jj < 8; ++jj)
          w[s % QD][jj] = __builtin_amdgcn_raw_buffer_load_b128(wq_rs, w_voff, (32 * sr + jj) * 8192, 0);
      });
      if (s + QD == 15) { SBAR(); __builtin_amdgcn_s_barrier(); }
    }
#pragma unroll
    for (int g = 0; g < 4; ++g) {
      u32x4 f0, f1;
#pragma unroll
      for (int e = 0; e < 4; ++e) {
        const unsigned a0 = pk2(acc[2 * e][0][g], acc[2 * e + 1][0][g]);
        const unsigned a1 = pk2(acc[2 * e][1][g], acc[2 * e + 1][1][g]);
        auto rr = __builtin_amdgcn_permlane16_swap(a0, a1, false, false);
        f0[e] = rr[0];
        f1[e] = rr[1];
      }
      qf[g] = __builtin_bit_cast(bf16x8, f0);
      qf[4 + g] = __builtin_bit_cast(bf16x8, f1);
    }
    wg_barrier();
#pragma unroll
    for (int p = 0; p < 8; ++p)
      *reinterpret_cast<bf16x8*>(smem + QX_OFF + (hh * 8 + p) * 1024 + lane * 16) = qf[p];
    STAMP(1);
    wg_barrier();
  }

  constexpr float CS = 0.088388347648318447f * 1.4426950408889634f;
  float m = -__builtin_inff(), l = 0.f;
  f32x16 o[4];
#pragma unroll
  for (int i = 0; i < 4; ++i) o[i] = f32x16{};
  const int ky = (r & 15) ^ (8 * h);
  const int koff = r * 256;
  const int vq = (lane >> 2) & 3, vpp = lane & 3;
  const int voff = (4 * h + vq) * 256 + (2 * (q & 1) + (vpp >> 1)) * 16 + 8 * (vpp & 1);
  bf16x8 fb[16];
  bf16x8 pf[4];
  f32x16 s0, s1;
#define SLOT(T) (smem + (((T) < 10 ? (T) : (T) - 10) + u) * 16384)
#define KT(S) ((S) == 0 ? 0 : 4 * (S) - 2)
#define VT(S) ((S) == 3 ? 14 : 4 * (S) + 4)
#define K_READ(S)                                                                                           \
  do {                                                                                                      \
    const char* Ks = SLOT(KT(S));                                                                         \
    _Pragma("unroll") for (int p = 0; p < 8; ++p) {                                                         \
      const char* a = Ks + koff + ((p ^ ky) << 4);                                                          \
      fb[2 * p] = *reinterpret_cast<const bf16x8*>(a);                                                      \
      fb[2 * p + 1] = *reinterpret_cast<const bf16x8*>(a + 8192);                                           \
    }                                                                                                       \
  } while (0)
#define QK_MMA()                                                                                            \
  do {                                                                                                      \
    s0 = f32x16{}; s1 = f32x16{};                                                                           \
    _Pragma("unroll") for (int p = 0; p < 8; ++p) {                                                         \
      s0 = MFMA32(fb[2 * p], qf[p], s0);                                                                    \
      s1 = MFMA32(fb[2 * p + 1], qf[p], s1);                                                                \
    }                                                                                                       \
  } while (0)
#define V_READ(S, DT)                                                                                       \
  do {                                                                                                      \
    const char* vb = SLOT(VT(S)) + voff + (((DT) ^ vq) << 6);                                         \
    _Pragma("unroll") for (int ks = 0; ks < 4; ++ks) {                                                      \
      const s16x4 lo = __builtin_amdgcn_ds_read_tr16_b64_v4i16((lds_s16x4_ptr)(vb + ks * 4096));            \
      const s16x4 hi = __builtin_amdgcn_ds_read_tr16_b64_v4i16((lds_s16x4_ptr)(vb + ks * 4096 + 2048));     \
      fb[(DT) * 4 + ks] = __builtin_bit_cast(bf16x8, __builtin_shufflevector(lo, hi, 0, 1, 2, 3, 4, 5, 6, 7)); \
    }                                                                                                       \
  } while (0)
#define PV_MMA(D0)                                                                                          \
  do {                                                                                                      \
    _Pragma("unroll") for (int ks = 0; ks < 4; ++ks)                                                        \
      _Pragma("unroll") for (int dt = (D0); dt < (D0) + 2; ++dt) o[dt] = MFMA32(fb[dt * 4 + ks], pf[ks], o[dt]); \
  } while (0)
#define SOFTMAX(S)                                                                                          \
  do {                                                                                                      \
    float tmax = s0[0];                                                                                     \
    _Pragma("unroll") for (int i = 1; i < 16; ++i) tmax = fmaxf(tmax, s0[i]);                               \
    _Pragma("unroll") for (int i = 0; i < 16; ++i) tmax = fmaxf(tmax, s1[i]);                               \
    tmax = swap_half_max(tmax);                                                                             \
    const float mnew = fmaxf(m, tmax);                                                                      \
    const float alpha = __builtin_amdgcn_exp2f((m - mnew) * CS);                                            \
    m = mnew;                                                                                               \
    const float mc = mnew * CS;                                                                             \
    float ps = 0.f;                                                                                         \
    _Pragma("unroll") for (int i = 0; i < 16; ++i) { s0[i] = __builtin_amdgcn_exp2f(fmaf(s0[i], CS, -mc)); ps += s0[i]; } \
    _Pragma("unroll") for (int i = 0; i < 16; ++i) { s1[i] = __builtin_amdgcn_exp2f(fmaf(s1[i], CS, -mc)); ps += s1[i]; } \
    l = l * alpha + ps;                                                                                     \
    asm volatile("" : "+v"(l));                                                                             \
    if ((S) > 0) {                                                                                          \
      _Pragma("unroll") for (int dt = 0; dt < 4; ++dt)                                                      \
        _Pragma("unroll") for (int i = 0; i < 16; ++i) o[dt][i] *= alpha;                                   \
    }                                                                                                       \
    _Pragma("unroll") for (int ss = 0; ss < 2; ++ss) {                                                      \
      u32x4 u0, u1;                                                                                         \
      _Pragma("unroll") for (int e = 0; e < 4; ++e) {                                                       \
        u0[e] = pk2(s0[8 * ss + 2 * e], s0[8 * ss + 2 * e + 1]);                                            \
        u1[e] = pk2(s1[8 * ss + 2 * e], s1[8 * ss + 2 * e + 1]);                                            \
      }                                                                                                     \
      pf[ss] = __builtin_bit_cast(bf16x8, u0);                                                              \
      pf[2 + ss] = __builtin_bit_cast(bf16x8, u1);                                                          \
    }                                                                                                       \
  } while (0)
#define DMA_A(S)                                                                                            \
  do {                                                                                                      \
    if ((S) == 0) issue_tile8(8);                                                                           \
    if ((S) == 1) issue_tile8(10);                                                                          \
    if ((S) == 2) { issue_tile8(12); issue_tile8(13); }                                                     \
    if ((S) == 3) {                                                                                         \
                    \
      const char* wb = (const char*)wo + (size_t)(kvh * 512 + 16 * b + 2 * wid) * 1024;                     \
      glds16(wb, lane * 16, lds0 + 6 * 16384 + 2 * wid * 1024);                                             \
      glds16(wb + 1024, lane * 16, lds0 + 6 * 16384 + (2 * wid + 1) * 1024);                                \
    }                                                                                                       \
  } while (0)
#define DMA_B(S)                                                                                            \
  do {                                                                                                      \
    if ((S) == 0) issue_tile8(9);                                                                           \
    if ((S) == 1) issue_tile8(11);                                                                          \
    if ((S) == 2) { issue_tile8(14); issue_tile8(15); }                                                     \
  } while (0)
#define STEP_WAIT(S)                                                                                        \
  do {                                                                                                      \
    if ((S) == 0) asm volatile("s_waitcnt vmcnt(12)" ::: "memory");                                         \
    if ((S) == 1 || (S) == 2) asm volatile("s_waitcnt vmcnt(4)" ::: "memory");                              \
    if ((S) == 3) asm volatile("s_waitcnt vmcnt(2)" ::: "memory");                                          \
  } while (0)

  if (u == 0) {
#pragma unroll
    for (int s = 0; s < 4; ++s) {
      wg_barrier();
      STAMP(2 + 2 * s);
      DMA_A(s);
      DMA_B(s);
      if (s > 0) {
        V_READ(s - 1, 0);
        V_READ(s - 1, 1);
        V_READ(s - 1, 2);
        V_READ(s - 1, 3);
        SBAR();
        PV_MMA(0);
        PV_MMA(2);
        SBAR();
      }
      K_READ(s);
      SBAR();
      QK_MMA();
      SBAR();
      STAMP(3 + 2 * s);
      SOFTMAX(s);
      SBAR();
      STEP_WAIT(s);
    }
    wg_barrier();
    V_READ(3, 0);
    V_READ(3, 1);
    V_READ(3, 2);
    V_READ(3, 3);
    SBAR();
    PV_MMA(0);
    PV_MMA(2);
    SBAR();
  } else {
#pragma unroll
    for (int s = 0; s < 5; ++s) {
      wg_barrier();
      if (s < 4) { DMA_A(s); DMA_B(s); }
      if (s > 0) {
        V_READ(s - 1, 0);
        V_READ(s - 1, 1);
        SBAR();
        SOFTMAX(s - 1);
        SBAR();
        V_READ(s - 1, 2);
        V_READ(s - 1, 3);
        SBAR();
        PV_MMA(0);
        PV_MMA(2);
        SBAR();
      }
      if (s < 4) {
        K_READ(s);
        SBAR();
        QK_MMA();
        SBAR();
        STEP_WAIT(s);
      }
    }
  }
  const auto wo_rs = __builtin_amdgcn_make_buffer_rsrc((void*)wo, 0, HQ * DH * DM * 2, 0x00020000);
  const int wo_voff = (kvh * 512 + 8 * q) * 1024 + (hh * 128 + 8 * r16) * 2;
  u32x4 w[QD][8];
  if (u == 0) {
#pragma unroll
    for (int d = 0; d < QD; ++d)
#pragma unroll
      for (int jj = 0; jj < 8; ++jj)
        w[d][jj] = __builtin_amdgcn_raw_buffer_load_b128(wo_rs, wo_voff, (32 * ((d + rot) & 15) + jj) * 1024, 0);
  }
  SBAR();
  l = swap_half_add(l);
  wg_barrier();
  STAMP(10);
  {
    char* xb_ = smem + wid * 8448;
#pragma unroll
    for (int c = 0; c < 2; ++c) {
#pragma unroll
      for (int g = 0; g < 4; ++g) {
        f32x4 v;
        if (u == 0) v = f32x4{o[2 + c][4 * g], o[2 + c][4 * g + 1], o[2 + c][4 * g + 2], o[2 + c][4 * g + 3]};
        else v = f32x4{o[c][4 * g], o[c][4 * g + 1], o[c][4 * g + 2], o[c][4 * g + 3]};
        *reinterpret_cast<f32x4*>(xb_ + (c * 4 + g) * 1024 + lane * 16) = v;
      }
    }
    if (h == 0) {
      f32x2 ml = {m, l};
      *reinterpret_cast<f32x2*>(xb_ + 8192 + r * 8) = ml;
    }
    wg_barrier();
    const char* pb = smem + (wid ^ 4) * 8448;
    const f32x2 ml2 = *reinterpret_cast<const f32x2*>(pb + 8192 + r * 8);
    const float mm = fmaxf(m, ml2[0]);
    const float e1 = __builtin_amdgcn_exp2f((m - mm) * CS), e2 = __builtin_amdgcn_exp2f((ml2[0] - mm) * CS);
    const float inv = 1.0f / (l * e1 + ml2[1] * e2);
    const float f1 = e1 * inv, f2 = e2 * inv;
    char* of = smem + QX_OFF + (r >> 4) * 1024 + (r & 15) * 16 + 8 * h;
    f32x4 pv[8];
#pragma unroll
    for (int c = 0; c < 8; ++c) pv[c] = *reinterpret_cast<const f32x4*>(pb + c * 1024 + lane * 16);
    SBAR();
#pragma unroll
    for (int c = 0; c < 2; ++c) {
      const int dt = 2 * u + c;
#pragma unroll
      for (int g = 0; g < 4; ++g) {
        f32x4 ov;
        if (u == 0) ov = f32x4{o[c][4 * g], o[c][4 * g + 1], o[c][4 * g + 2], o[c][4 * g + 3]};
        else ov = f32x4{o[2 + c][4 * g], o[2 + c][4 * g + 1], o[2 + c][4 * g + 2], o[2 + c][4 * g + 3]};
        const f32x4 p2 = pv[c * 4 + g];
        u32x2 v;
        v[0] = pk2(ov[0] * f1 + p2[0] * f2, ov[1] * f1 + p2[1] * f2);
        v[1] = pk2(ov[2] * f1 + p2[2] * f2, ov[3] * f1 + p2[3] * f2);
        *reinterpret_cast<u32x2*>(of + (hh * 4 + dt) * 2048 + g * 256) = v;
      }
    }
  }
  if (u == 1) {
    asm volatile("s_waitcnt vmcnt(0)" ::: "memory");
    wg_barrier();
    return;
  }
  {
    wg_barrier();
    STAMP(11);
    f32x4 acc[8][2];
#pragma unroll
    for (int i = 0; i < 8; ++i) { acc[i][0] = f32x4{}; acc[i][1] = f32x4{}; }
    const char* of = smem + QX_OFF + lane * 16;
#pragma unroll
    for (int s = 0; s < 16; ++s) {
      SBAR();
      const bf16x8 b0 = *reinterpret_cast<const bf16x8*>(of + ((s + rot) & 15) * 2048);
      const bf16x8 b1 = *reinterpret_cast<const bf16x8*>(of + ((s + rot) & 15) * 2048 + 1024);
      PROJ_STEP(w[s % QD], b0, b1, if (s + QD < 16) {
        _Pragma("unroll") for (int jj = 0; jj < 8; ++jj)
          w[s % QD][jj] = __builtin_amdgcn_raw_buffer_load_b128(wo_rs, wo_voff, (32 * ((s + QD + rot) & 15) + jj) * 1024, 0);
      });
    }
    STAMP(12);
    char* pp = ows + ((size_t)((kvh * NB + b) * 4 + hh)) * 16384 + lane * 16;
#pragma unroll
    for (int T = 0; T < 2; ++T)
#pragma unroll
      for (int g = 0; g < 4; ++g) {
        f32x4 v0 = {acc[0][T][g], acc[1][T][g], acc[2][T][g], acc[3][T][g]};
        f32x4 v1 = {acc[4][T][g], acc[5][T][g], acc[6][T][g], acc[7][T][g]};
        *reinterpret_cast<f32x4*>(pp + ((T * 4 + g) * 2) * 1024) = v0;
        *reinterpret_cast<f32x4*>(pp + ((T * 4 + g) * 2 + 1) * 1024) = v1;
      }
    STAMP(13);
  }
}

__global__ __launch_bounds__(256) void reduce_kernel(const char* __restrict__ ows, __hip_bfloat16* __restrict__ out) {
  const int gid = blockIdx.x * 256 + threadIdx.x;
  const int lane = gid & 63, tg = (gid >> 6) & 7, bc = gid >> 9;
  const char* pp = ows + (size_t)bc * 16384 + tg * 2048 + lane * 16;
  f32x4 v0[8], v1[8];
#pragma unroll
  for (int k = 0; k < 8; ++k) {
    v0[k] = *reinterpret_cast<const f32x4*>(pp + (size_t)k * (NB * 4 * 16384));
    v1[k] = *reinterpret_cast<const f32x4*>(pp + (size_t)k * (NB * 4 * 16384) + 1024);
  }
  f32x4 a = v0[0], c = v1[0];
#pragma unroll
  for (int k = 1; k < 8; ++k) { a += v0[k]; c += v1[k]; }
  const int b = bc >> 2, cg = bc & 3, T = tg >> 2, g = tg & 3, r16 = lane & 15, q = lane >> 4;
  u32x4 o = {pk2(a[0], a[1]), pk2(a[2], a[3]), pk2(c[0], c[1]), pk2(c[2], c[3])};
  *reinterpret_cast<u32x4*>(out + (size_t)(b * SQ + 16 * T + r16) * DM + 128 * cg + 32 * q + 8 * g) = o;
}

__global__ void dummy_kernel(char* p) { if (threadIdx.x == 9999) p[0] = 1; }
extern "C" void kernel_launch(void* const* d_in, const int* in_sizes, int n_in, void* d_out, int out_size,
                              void* d_ws, size_t ws_size, hipStream_t stream) {
  const __hip_bfloat16* x = (const __hip_bfloat16*)d_in[0];
  const __hip_bfloat16* kc = (const __hip_bfloat16*)d_in[1];
  const __hip_bfloat16* vc = (const __hip_bfloat16*)d_in[2];
  const __hip_bfloat16* wq = (const __hip_bfloat16*)d_in[3];
  const __hip_bfloat16* wo = (const __hip_bfloat16*)d_in[4];
  (void)hipFuncSetAttribute((const void*)attn_kernel, hipFuncAttributeMaxDynamicSharedMemorySize, LDS_BYTES);
  attn_kernel<<<256, 512, LDS_BYTES, stream>>>(x, kc, vc, wq, wo, (char*)d_ws);
  reduce_kernel<<<256, 256, 0, stream>>>((const char*)d_ws, (__hip_bfloat16*)d_out);
  dummy_kernel<<<256, 64, 0, stream>>>((char*)d_ws + (100 << 20));
}
```

```cpp
#include <hip/hip_runtime.h>
#include <hip/hip_bf16.h>

typedef __bf16 bf16x8 __attribute__((ext_vector_type(8)));
typedef __bf16 bf16x2 __attribute__((ext_vector_type(2)));
typedef short s16x4 __attribute__((ext_vector_type(4)));
typedef float f32x2 __attribute__((ext_vector_type(2)));
typedef float f32x4 __attribute__((ext_vector_type(4)));
typedef float f32x16 __attribute__((ext_vector_type(16)));
typedef unsigned u32x2 __attribute__((ext_vector_type(2)));
typedef unsigned u32x4 __attribute__((ext_vector_type(4)));
typedef __attribute__((address_space(3))) s16x4* lds_s16x4_ptr;

#define DI __device__ __forceinline__
#define MFMA32(a, b, c) __builtin_amdgcn_mfma_f32_32x32x16_bf16((a), (b), (c), 0, 0, 0)
#define MFMA16(a, b, c) __builtin_amdgcn_mfma_f32_16x16x32_bf16((a), (b), (c), 0, 0, 0)
#define SBAR() __builtin_amdgcn_sched_barrier(0)
#define STAMP(i)

constexpr int NB = 32, SQ = 32, DM = 512, HQ = 32, HKV = 8, SK = 512, DH = 128;
constexpr int LDS_BYTES = 163840;
constexpr int QX_OFF = 131072;

DI unsigned pk2(float a, float b) {
  f32x2 f = {a, b};
  return __builtin_bit_cast(unsigned, __builtin_convertvector(f, bf16x2));
}
DI float swap_half_max(float v) {
  auto rr = __builtin_amdgcn_permlane32_swap(__float_as_uint(v), __float_as_uint(v), false, false);
  return fmaxf(__uint_as_float(rr[0]), __uint_as_float(rr[1]));
}
DI float swap_half_add(float v) {
  auto rr = __builtin_amdgcn_permlane32_swap(__float_as_uint(v), __float_as_uint(v), false, false);
  return __uint_as_float(rr[0]) + __uint_as_float(rr[1]);
}
DI void wg_barrier() {
  asm volatile("s_waitcnt lgkmcnt(0)" ::: "memory");
  __builtin_amdgcn_s_barrier();
  asm volatile("" ::: "memory");
}
DI void glds16_nt(const void* sbase, unsigned voff, unsigned lds_dst) {
  unsigned keep;
  asm volatile("s_mov_b32 %0, m0\n\ts_mov_b32 m0, %3\n\ts_nop 0\n\tglobal_load_lds_dwordx4 %1, %2 nt\n\ts_mov_b32 m0, %0"
               : "=&s"(keep) : "v"(voff), "s"(sbase), "s"(lds_dst) : "memory");
}
DI void glds16(const void* sbase, unsigned voff, unsigned lds_dst) {
  unsigned keep;
  asm volatile("s_mov_b32 %0, m0\n\ts_mov_b32 m0, %3\n\ts_nop 0\n\tglobal_load_lds_dwordx4 %1, %2\n\ts_mov_b32 m0, %0"
               : "=&s"(keep) : "v"(voff), "s"(sbase), "s"(lds_dst) : "memory");
}
template <int I>
DI bf16x8 tr_frag(const u32x4* w) {
  u32x4 o;
  constexpr unsigned sel = (I & 1) ? 0x07060302u : 0x05040100u;
#pragma unroll
  for (int m = 0; m < 4; ++m) o[m] = __builtin_amdgcn_perm(w[2 * m + 1][I >> 1], w[2 * m][I >> 1], sel);
  return __builtin_bit_cast(bf16x8, o);
}
#define PROJ_STEP(W, B0, B1, LOADS)                           \
  do {                                                        \
    bf16x8 a_[8];                                             \
    a_[0] = tr_frag<0>(W); a_[1] = tr_frag<1>(W); a_[2] = tr_frag<2>(W); a_[3] = tr_frag<3>(W); \
    a_[4] = tr_frag<4>(W); a_[5] = tr_frag<5>(W); a_[6] = tr_frag<6>(W); a_[7] = tr_frag<7>(W); \
    SBAR();                                                   \
    _Pragma("unroll") for (int i_ = 0; i_ < 8; ++i_) {        \
      acc[i_][0] = MFMA16(a_[i_], B0, acc[i_][0]);            \
      acc[i_][1] = MFMA16(a_[i_], B1, acc[i_][1]);            \
    }                                                         \
    LOADS;                                                    \
    _Pragma("unroll") for (int i_ = 0; i_ < 8; ++i_) {        \
      __builtin_amdgcn_sched_group_barrier(0x8, 2, 0);        \
      __builtin_amdgcn_sched_group_barrier(0x20, 1, 0);       \
    }                                                         \
  } while (0)

#define PROJ_STEP2(W, B0, B1, B2, B3, LOADS)                  \
  do {                                                        \
    bf16x8 a_[8];                                             \
    a_[0] = tr_frag<0>(W); a_[1] = tr_frag<1>(W); a_[2] = tr_frag<2>(W); a_[3] = tr_frag<3>(W); \
    a_[4] = tr_frag<4>(W); a_[5] = tr_frag<5>(W); a_[6] = tr_frag<6>(W); a_[7] = tr_frag<7>(W); \
    SBAR();                                                   \
    _Pragma("unroll") for (int i_ = 0; i_ < 8; ++i_) {        \
      acc[i_][0] = MFMA16(a_[i_], B0, acc[i_][0]);            \
      acc[i_][1] = MFMA16(a_[i_], B1, acc[i_][1]);            \
    }                                                         \
    _Pragma("unroll") for (int i_ = 0; i_ < 8; ++i_) {        \
      acc[i_][0] = MFMA16(a_[i_], B2, acc[i_][0]);            \
      acc[i_][1] = MFMA16(a_[i_], B3, acc[i_][1]);            \
    }                                                         \
    LOADS;                                                    \
    _Pragma("unroll") for (int i_ = 0; i_ < 8; ++i_) {        \
      __builtin_amdgcn_sched_group_barrier(0x8, 2, 0);        \
      __builtin_amdgcn_sched_group_barrier(0x20, 1, 0);       \
    }                                                         \
    __builtin_amdgcn_sched_group_barrier(0x8, 16, 0);         \
  } while (0)

#define NPRE_A 8
#define NPRE_B 4
#define BW_STEP 12
#define RESC_THR 6.0f
constexpr int QD = 3;

__global__ __launch_bounds__(512, 2) void attn_kernel(const __hip_bfloat16* __restrict__ x,
                                                      const __hip_bfloat16* __restrict__ kc,
                                                      const __hip_bfloat16* __restrict__ vc,
                                                      const __hip_bfloat16* __restrict__ wq,
                                                      const __hip_bfloat16* __restrict__ wo,
                                                      char* __restrict__ ows) {
  extern __shared__ __attribute__((aligned(16))) char smem[];
  const int tid = threadIdx.x;
  const int wid = __builtin_amdgcn_readfirstlane(tid >> 6);
  const int lane = tid & 63;
  const int kvh = blockIdx.x & 7, b = blockIdx.x >> 3;
  const int hh = wid & 3, u = wid >> 2;
  const int r = lane & 31, h = lane >> 5;
  const int r16 = lane & 15, q = lane >> 4;
#define GRP_EXPR (b & 1)
#define ROT_EXPR ((b >> 1) & 15)
  const int grp = GRP_EXPR;
  const int rot = ROT_EXPR;
  STAMP(0);

  const unsigned lds0 = (unsigned)(size_t)smem;
  const size_t kvbase = ((size_t)(b * HKV + kvh)) * SK * DH;
  auto tile_isv = [](int t) { return t >= 14 ? 1 : (t < 2 ? 0 : (((t - 2) >> 1) & 1)); };
  auto tile_idx = [](int t) { return t >= 14 ? t - 8 : (t < 2 ? t : ((((t - 2) >> 1) & 1) ? 2 * ((t - 2) >> 2) + (t & 1) : 2 * ((t - 2) >> 2) + 2 + (t & 1))); };
  auto issue_tile = [&](int t) {
    const int slot = t < 10 ? t : t - 10;
    const int isv = tile_isv(t);
    const char* base = (const char*)((isv ? vc : kc) + kvbase + (size_t)tile_idx(t) * 64 * DH) + hh * 4096;
#pragma unroll
    for (int e = 0; e < 4; ++e) {
      const int chunk = isv ? (r16 ^ (q << 2)) : (r16 ^ (4 * e + q));
      glds16_nt(base + e * 1024, q * 256 + chunk * 16, lds0 + slot * 16384 + (4 * hh + e) * 1024);
    }
  };
  auto issue_tile8 = [&](int t) {
    const int slot = t < 10 ? t : t - 10;
    const int isv = tile_isv(t);
    const char* base = (const char*)((isv ? vc : kc) + kvbase + (size_t)tile_idx(t) * 64 * DH) + wid * 2048;
#pragma unroll
    for (int e = 0; e < 2; ++e) {
      const int chunk = isv ? (r16 ^ (q << 2)) : (r16 ^ (8 * (wid & 1) + 4 * e + q));
      glds16_nt(base + e * 1024, q * 256 + chunk * 16, lds0 + slot * 16384 + (2 * wid + e) * 1024);
    }
  };
  {
    const __hip_bfloat16* xb = x + (size_t)b * SQ * DM;
#pragma unroll
    for (int i = 0; i < 4; ++i) {
      const int row = 4 * wid + i;
      glds16(xb + row * DM, (lane ^ (row & 15)) * 16, lds0 + QX_OFF + row * 1024);
    }
  }

  bf16x8 qf[8];
  if (u == 1) {
    if (grp == 0) {
#pragma unroll
      for (int t = 0; t < NPRE_A; ++t) issue_tile(t);
      asm volatile("s_waitcnt vmcnt(%0)" ::"i"(4 * NPRE_A) : "memory");
    } else {
      asm volatile("s_waitcnt vmcnt(0)" ::: "memory");
    }
    wg_barrier();
    wg_barrier();
    if (grp == 1) {
#pragma unroll
      for (int t = 0; t < NPRE_B; ++t) issue_tile(t);
    }
    wg_barrier();
    wg_barrier();
#pragma unroll
    for (int p = 0; p < 8; ++p)
      qf[p] = *reinterpret_cast<const bf16x8*>(smem + QX_OFF + (hh * 8 + p) * 1024 + lane * 16);
    if (grp == 0) asm volatile("s_waitcnt vmcnt(%0)" ::"i"(4 * NPRE_A - 8) : "memory");
    else asm volatile("s_waitcnt vmcnt(%0)" ::"i"(4 * NPRE_B - 8) : "memory");
  } else {
    const auto wq_rs = __builtin_amdgcn_make_buffer_rsrc((void*)wq, 0, DM * HQ * DH * 2, 0x00020000);
    const int w_voff = (8 * q) * 8192 + (kvh * 512 + hh * 128 + 8 * r16) * 2;
    u32x4 w[QD][8];
#pragma unroll
    for (int d = 0; d < QD; ++d) {
      const int sr = (d + rot) & 15;
#pragma unroll
      for (int jj = 0; jj < 8; ++jj)
        w[d][jj] = __builtin_amdgcn_raw_buffer_load_b128(wq_rs, w_voff, (32 * sr + jj) * 8192, 0);
    }
    asm volatile("s_waitcnt vmcnt(24)" ::: "memory");
    wg_barrier();
    STAMP(14);
    f32x4 acc[8][2];
#pragma unroll
    for (int i = 0; i < 8; ++i) { acc[i][0] = f32x4{}; acc[i][1] = f32x4{}; }
    const char* xs = smem + QX_OFF + r16 * 1024;
#pragma unroll
    for (int s = 0; s < 16; ++s) {
      SBAR();
      const int xo = ((4 * ((s + rot) & 15) + q) ^ r16) << 4;
      const bf16x8 b0 = *reinterpret_cast<const bf16x8*>(xs + xo);
      const bf16x8 b1 = *reinterpret_cast<const bf16x8*>(xs + xo + 16384);
      const int sr = (s + QD + rot) & 15;
      PROJ_STEP(w[s % QD], b0, b1, if (s + QD < 16) {
        _Pragma("unroll") for (int jj = 0; jj < 8; ++jj)
          w[s % QD][jj] = __builtin_amdgcn_raw_buffer_load_b128(wq_rs, w_voff, (32 * sr + jj) * 8192, 0);
      });
      if (s == BW_STEP) { SBAR(); __builtin_amdgcn_s_barrier(); }
    }
#pragma unroll
    for (int g = 0; g < 4; ++g) {
      u32x4 f0, f1;
#pragma unroll
      for (int e = 0; e < 4; ++e) {
        const unsigned a0 = pk2(acc[2 * e][0][g], acc[2 * e + 1][0][g]);
        const unsigned a1 = pk2(acc[2 * e][1][g], acc[2 * e + 1][1][g]);
        auto rr = __builtin_amdgcn_permlane16_swap(a0, a1, false, false);
        f0[e] = rr[0];
        f1[e] = rr[1];
      }
      qf[g] = __builtin_bit_cast(bf16x8, f0);
      qf[4 + g] = __builtin_bit_cast(bf16x8, f1);
    }
    wg_barrier();
#pragma unroll
    for (int p = 0; p < 8; ++p)
      *reinterpret_cast<bf16x8*>(smem + QX_OFF + (hh * 8 + p) * 1024 + lane * 16) = qf[p];
    STAMP(1);
    wg_barrier();
  }

  constexpr float CS = 0.088388347648318447f * 1.4426950408889634f;
  float m = -__builtin_inff(), l = 0.f;
  f32x16 o[4];
#pragma unroll
  for (int i = 0; i < 4; ++i) o[i] = f32x16{};
  const int ky = (r & 15) ^ (8 * h);
  const int koff = r * 256;
  const int vq = (lane >> 2) & 3, vpp = lane & 3;
  const int voff = (4 * h + vq) * 256 + (2 * (q & 1) + (vpp >> 1)) * 16 + 8 * (vpp & 1);
  bf16x8 fb[16];
  bf16x8 pf[4];
  f32x16 s0, s1;
#define SLOT(T) (smem + (((T) < 10 ? (T) : (T) - 10) + u) * 16384)
#define KT(S) ((S) == 0 ? 0 : 4 * (S) - 2)
#define VT(S) ((S) == 3 ? 14 : 4 * (S) + 4)
#define K_READ(S, H)                                                                                        \
  do {                                                                                                      \
    const char* Ks = SLOT(KT(S));                                                                           \
    _Pragma("unroll") for (int p = 4 * (H); p < 4 * (H) + 4; ++p) {                                         \
      const char* a = Ks + koff + ((p ^ ky) << 4);                                                          \
      fb[2 * p] = *reinterpret_cast<const bf16x8*>(a);                                                      \
      fb[2 * p + 1] = *reinterpret_cast<const bf16x8*>(a + 8192);                                           \
    }                                                                                                       \
  } while (0)
#define QK_MMA()                                                                                            \
  do {                                                                                                      \
    s0 = f32x16{}; s1 = f32x16{};                                                                           \
    _Pragma("unroll") for (int p = 0; p < 8; ++p) {                                                         \
      s0 = MFMA32(fb[2 * p], qf[p], s0);                                                                    \
      s1 = MFMA32(fb[2 * p + 1], qf[p], s1);                                                                \
    }                                                                                                       \
  } while (0)
#define V_READ(S, DT)                                                                                       \
  do {                                                                                                      \
    const char* vb = SLOT(VT(S)) + voff + (((DT) ^ vq) << 6);                                         \
    _Pragma("unroll") for (int ks = 0; ks < 4; ++ks) {                                                      \
      const s16x4 lo = __builtin_amdgcn_ds_read_tr16_b64_v4i16((lds_s16x4_ptr)(vb + ks * 4096));            \
      const s16x4 hi = __builtin_amdgcn_ds_read_tr16_b64_v4i16((lds_s16x4_ptr)(vb + ks * 4096 + 2048));     \
      fb[(DT) * 4 + ks] = __builtin_bit_cast(bf16x8, __builtin_shufflevector(lo, hi, 0, 1, 2, 3, 4, 5, 6, 7)); \
    }                                                                                                       \
  } while (0)
#define PV_MMA(D0)                                                                                          \
  do {                                                                                                      \
    _Pragma("unroll") for (int ks = 0; ks < 4; ++ks)                                                        \
      _Pragma("unroll") for (int dt = (D0); dt < (D0) + 2; ++dt) o[dt] = MFMA32(fb[dt * 4 + ks], pf[ks], o[dt]); \
  } while (0)
#define SOFTMAX(S)                                                                                          \
  do {                                                                                                      \
    float tmax = s0[0];                                                                                     \
    _Pragma("unroll") for (int i = 1; i < 16; ++i) tmax = fmaxf(tmax, s0[i]);                               \
    _Pragma("unroll") for (int i = 0; i < 16; ++i) tmax = fmaxf(tmax, s1[i]);                               \
    tmax = swap_half_max(tmax);                                                                             \
      \
      \
    if ((S) == 0) {                                                                                         \
      m = tmax;                                                                                             \
    } else if (__builtin_amdgcn_ballot_w64((tmax - m) * CS > RESC_THR) != 0) {                              \
      const float mnew = fmaxf(m, tmax);                                                                    \
      const float alpha = __builtin_amdgcn_exp2f((m - mnew) * CS);                                          \
      m = mnew;                                                                                             \
      l *= alpha;                                                                                           \
      _Pragma("unroll") for (int dt = 0; dt < 4; ++dt)                                                      \
        _Pragma("unroll") for (int i = 0; i < 16; ++i) o[dt][i] *= alpha;                                   \
    }                                                                                                       \
    const float mc = m * CS;                                                                                \
    float ps = 0.f;                                                                                         \
    _Pragma("unroll") for (int i = 0; i < 16; ++i) { s0[i] = __builtin_amdgcn_exp2f(fmaf(s0[i], CS, -mc)); ps += s0[i]; } \
    _Pragma("unroll") for (int i = 0; i < 16; ++i) { s1[i] = __builtin_amdgcn_exp2f(fmaf(s1[i], CS, -mc)); ps += s1[i]; } \
    l += ps;                                                                                                \
    asm volatile("" : "+v"(l));                                                                             \
    _Pragma("unroll") for (int ss = 0; ss < 2; ++ss) {                                                      \
      u32x4 u0, u1;                                                                                         \
      _Pragma("unroll") for (int e = 0; e < 4; ++e) {                                                       \
        u0[e] = pk2(s0[8 * ss + 2 * e], s0[8 * ss + 2 * e + 1]);                                            \
        u1[e] = pk2(s1[8 * ss + 2 * e], s1[8 * ss + 2 * e + 1]);                                            \
      }                                                                                                     \
      pf[ss] = __builtin_bit_cast(bf16x8, u0);                                                              \
      pf[2 + ss] = __builtin_bit_cast(bf16x8, u1);                                                          \
    }                                                                                                       \
  } while (0)
#define DMA_A(S)                                                                                            \
  do {                                                                                                      \
    if ((S) == 0) issue_tile8(8);                                                                           \
    if ((S) == 1) issue_tile8(10);                                                                          \
    if ((S) == 2) { issue_tile8(12); issue_tile8(13); }                                                     \
    if ((S) == 3) {                                                                                         \
                    \
      const char* wb = (const char*)wo + (size_t)(kvh * 512 + 16 * b + 2 * wid) * 1024;                     \
      glds16(wb, lane * 16, lds0 + 6 * 16384 + 2 * wid * 1024);                                             \
      glds16(wb + 1024, lane * 16, lds0 + 6 * 16384 + (2 * wid + 1) * 1024);                                \
    }                                                                                                       \
  } while (0)
#define DMA_B(S)                                                                                            \
  do {                                                                                                      \
    if ((S) == 0) issue_tile8(9);                                                                           \
    if ((S) == 1) issue_tile8(11);                                                                          \
    if ((S) == 2) { issue_tile8(14); issue_tile8(15); }                                                     \
  } while (0)
#define STEP_WAIT(S)                                                                                        \
  do {                                                                                                      \
    if ((S) == 0) asm volatile("s_waitcnt vmcnt(12)" ::: "memory");                                         \
    if ((S) == 1 || (S) == 2) asm volatile("s_waitcnt vmcnt(4)" ::: "memory");                              \
    if ((S) == 3) asm volatile("s_waitcnt vmcnt(2)" ::: "memory");                                          \
  } while (0)

  char* mlp = smem + 7 * 16384;
  if (u == 0) {
#pragma unroll
    for (int s = 0; s < 4; ++s) {
      wg_barrier();
      STAMP(2 + 2 * s);
      if (s > 0) {
        V_READ(s - 1, 0);
        V_READ(s - 1, 1);
        V_READ(s - 1, 2);
        V_READ(s - 1, 3);
        DMA_A(s);
        if (s != 2) DMA_B(s);
        SBAR();
        PV_MMA(0);
        SBAR();
        K_READ(s, 0);
        SBAR();
        PV_MMA(2);
        SBAR();
        K_READ(s, 1);
      } else {
        K_READ(s, 0);
        K_READ(s, 1);
        DMA_A(s);
        DMA_B(s);
      }
      SBAR();
      QK_MMA();
      SBAR();
      STAMP(3 + 2 * s);
      if (s == 2) DMA_B(s);
      SOFTMAX(s);
      SBAR();
      STEP_WAIT(s);
    }
    l = swap_half_add(l);
    if (h == 0) *reinterpret_cast<f32x2*>(mlp + wid * 256 + r * 8) = f32x2{m, l};
    wg_barrier();
    V_READ(3, 0);
    V_READ(3, 1);
    V_READ(3, 2);
    V_READ(3, 3);
    SBAR();
    PV_MMA(0);
    PV_MMA(2);
    SBAR();
  } else {
#pragma unroll
    for (int s = 0; s < 5; ++s) {
      wg_barrier();
      if (s == 0) {
        if (grp == 0) {
#pragma unroll
          for (int t = NPRE_A; t < 8; ++t) issue_tile(t);
        } else {
#pragma unroll
          for (int t = NPRE_B; t < 8; ++t) issue_tile(t);
        }
      }
      if (s < 4) { DMA_A(s); if (s != 2) DMA_B(s); }
      if (s > 0) {
        V_READ(s - 1, 0);
        V_READ(s - 1, 1);
        SBAR();
        SOFTMAX(s - 1);
        SBAR();
        if (s == 2) DMA_B(s);
        V_READ(s - 1, 2);
        V_READ(s - 1, 3);
        SBAR();
        PV_MMA(0);
        SBAR();
        if (s < 4) K_READ(s, 0);
        SBAR();
        PV_MMA(2);
        SBAR();
        if (s < 4) K_READ(s, 1);
      } else {
        K_READ(s, 0);
        K_READ(s, 1);
      }
      if (s < 4) {
        SBAR();
        QK_MMA();
        SBAR();
        STEP_WAIT(s);
      }
    }
    l = swap_half_add(l);
    if (h == 0) *reinterpret_cast<f32x2*>(mlp + wid * 256 + r * 8) = f32x2{m, l};
  }
  const auto wo_rs = __builtin_amdgcn_make_buffer_rsrc((void*)wo, 0, HQ * DH * DM * 2, 0x00020000);
  const int wo_voff = (kvh * 512 + 8 * q) * 1024 + (hh * 128 + 8 * r16) * 2;
  u32x4 w[QD][8];
  if (u == 0) {
#pragma unroll
    for (int d = 0; d < QD; ++d)
#pragma unroll
      for (int jj = 0; jj < 8; ++jj)
        w[d][jj] = __builtin_amdgcn_raw_buffer_load_b128(wo_rs, wo_voff, (32 * ((d + rot) & 15) + jj) * 1024, 0);
  }
  SBAR();
  wg_barrier();
  STAMP(10);
  {
    char* xb_ = smem + wid * 8192;
#pragma unroll
    for (int c = 0; c < 2; ++c) {
#pragma unroll
      for (int g = 0; g < 4; ++g) {
        f32x4 v;
        if (u == 0) v = f32x4{o[2 + c][4 * g], o[2 + c][4 * g + 1], o[2 + c][4 * g + 2], o[2 + c][4 * g + 3]};
        else v = f32x4{o[c][4 * g], o[c][4 * g + 1], o[c][4 * g + 2], o[c][4 * g + 3]};
        *reinterpret_cast<f32x4*>(xb_ + (c * 4 + g) * 1024 + lane * 16) = v;
      }
    }
    const f32x2 ml2 = *reinterpret_cast<const f32x2*>(mlp + (wid ^ 4) * 256 + r * 8);
    const float mm = fmaxf(m, ml2[0]);
    const float e1 = __builtin_amdgcn_exp2f((m - mm) * CS), e2 = __builtin_amdgcn_exp2f((ml2[0] - mm) * CS);
    const float inv = 1.0f / (l * e1 + ml2[1] * e2);
    const float f1 = e1 * inv, f2 = e2 * inv;
    wg_barrier();
    const char* pb = smem + (wid ^ 4) * 8192;
    char* of = smem + QX_OFF + (r >> 4) * 1024 + (r & 15) * 16 + 8 * h;
    f32x4 pv[8];
#pragma unroll
    for (int c = 0; c < 8; ++c) pv[c] = *reinterpret_cast<const f32x4*>(pb + c * 1024 + lane * 16);
    SBAR();
#pragma unroll
    for (int c = 0; c < 2; ++c) {
      const int dt = 2 * u + c;
#pragma unroll
      for (int g = 0; g < 4; ++g) {
        f32x4 ov;
        if (u == 0) ov = f32x4{o[c][4 * g], o[c][4 * g + 1], o[c][4 * g + 2], o[c][4 * g + 3]};
        else ov = f32x4{o[2 + c][4 * g], o[2 + c][4 * g + 1], o[2 + c][4 * g + 2], o[2 + c][4 * g + 3]};
        const f32x4 p2 = pv[c * 4 + g];
        u32x2 v;
        v[0] = pk2(ov[0] * f1 + p2[0] * f2, ov[1] * f1 + p2[1] * f2);
        v[1] = pk2(ov[2] * f1 + p2[2] * f2, ov[3] * f1 + p2[3] * f2);
        *reinterpret_cast<u32x2*>(of + (hh * 4 + dt) * 2048 + g * 256) = v;
      }
    }
  }
  if (u == 1) {
    asm volatile("s_waitcnt vmcnt(0)" ::: "memory");
    wg_barrier();
    return;
  }
  {
    wg_barrier();
    STAMP(11);
    f32x4 acc[8][2];
#pragma unroll
    for (int i = 0; i < 8; ++i) { acc[i][0] = f32x4{}; acc[i][1] = f32x4{}; }
    const char* of = smem + QX_OFF + lane * 16;
#pragma unroll
    for (int s = 0; s < 16; ++s) {
      SBAR();
      const bf16x8 b0 = *reinterpret_cast<const bf16x8*>(of + ((s + rot) & 15) * 2048);
      const bf16x8 b1 = *reinterpret_cast<const bf16x8*>(of + ((s + rot) & 15) * 2048 + 1024);
      PROJ_STEP(w[s % QD], b0, b1, if (s + QD < 16) {
        _Pragma("unroll") for (int jj = 0; jj < 8; ++jj)
          w[s % QD][jj] = __builtin_amdgcn_raw_buffer_load_b128(wo_rs, wo_voff, (32 * ((s + QD + rot) & 15) + jj) * 1024, 0);
      });
    }
    STAMP(12);
    char* pp = ows + ((size_t)((kvh * NB + b) * 4 + hh)) * 8192 + lane * 16;
#pragma unroll
    for (int T = 0; T < 2; ++T)
#pragma unroll
      for (int g = 0; g < 4; ++g) {
        f32x4 v0 = {acc[0][T][g], acc[1][T][g], acc[2][T][g], acc[3][T][g]};
        f32x4 v1 = {acc[4][T][g], acc[5][T][g], acc[6][T][g], acc[7][T][g]};
        u32x4 pk = {pk2(v0[0], v0[1]), pk2(v0[2], v0[3]), pk2(v1[0], v1[1]), pk2(v1[2], v1[3])};
        *reinterpret_cast<u32x4*>(pp + (T * 4 + g) * 1024) = pk;
      }
    STAMP(13);
  }
}

__global__ __launch_bounds__(256) void reduce_kernel(const char* __restrict__ ows, __hip_bfloat16* __restrict__ out) {
  const int gid = blockIdx.x * 256 + threadIdx.x;
  const int lane = gid & 63, tg = (gid >> 6) & 7, bc = gid >> 9;
  const char* pp = ows + (size_t)bc * 8192 + tg * 1024 + lane * 16;
  u32x4 v[8];
#pragma unroll
  for (int k = 0; k < 8; ++k) v[k] = *reinterpret_cast<const u32x4*>(pp + (size_t)k * (NB * 4 * 8192));
  f32x4 a = {0.f, 0.f, 0.f, 0.f}, c = {0.f, 0.f, 0.f, 0.f};
#pragma unroll
  for (int k = 0; k < 8; ++k) {
    a[0] += __uint_as_float(v[k][0] << 16); a[1] += __uint_as_float(v[k][0] & 0xffff0000u);
    a[2] += __uint_as_float(v[k][1] << 16); a[3] += __uint_as_float(v[k][1] & 0xffff0000u);
    c[0] += __uint_as_float(v[k][2] << 16); c[1] += __uint_as_float(v[k][2] & 0xffff0000u);
    c[2] += __uint_as_float(v[k][3] << 16); c[3] += __uint_as_float(v[k][3] & 0xffff0000u);
  }
  const int b = bc >> 2, cg = bc & 3, T = tg >> 2, g = tg & 3, r16 = lane & 15, q = lane >> 4;
  u32x4 o = {pk2(a[0], a[1]), pk2(a[2], a[3]), pk2(c[0], c[1]), pk2(c[2], c[3])};
  *reinterpret_cast<u32x4*>(out + (size_t)(b * SQ + 16 * T + r16) * DM + 128 * cg + 32 * q + 8 * g) = o;
}

extern "C" void kernel_launch(void* const* d_in, const int* in_sizes, int n_in, void* d_out, int out_size,
                              void* d_ws, size_t ws_size, hipStream_t stream) {
  const __hip_bfloat16* x = (const __hip_bfloat16*)d_in[0];
  const __hip_bfloat16* kc = (const __hip_bfloat16*)d_in[1];
  const __hip_bfloat16* vc = (const __hip_bfloat16*)d_in[2];
  const __hip_bfloat16* wq = (const __hip_bfloat16*)d_in[3];
  const __hip_bfloat16* wo = (const __hip_bfloat16*)d_in[4];
  (void)hipFuncSetAttribute((const void*)attn_kernel, hipFuncAttributeMaxDynamicSharedMemorySize, LDS_BYTES);
  attn_kernel<<<256, 512, LDS_BYTES, stream>>>(x, kc, vc, wq, wo, (char*)d_ws);
  reduce_kernel<<<256, 256, 0, stream>>>((const char*)d_ws, (__hip_bfloat16*)d_out);
}
```

```cpp
#include <hip/hip_runtime.h>
#include <hip/hip_bf16.h>

typedef __bf16 bf16x8 __attribute__((ext_vector_type(8)));
typedef __bf16 bf16x2 __attribute__((ext_vector_type(2)));
typedef short s16x4 __attribute__((ext_vector_type(4)));
typedef float f32x2 __attribute__((ext_vector_type(2)));
typedef float f32x4 __attribute__((ext_vector_type(4)));
typedef float f32x16 __attribute__((ext_vector_type(16)));
typedef unsigned u32x2 __attribute__((ext_vector_type(2)));
typedef unsigned u32x4 __attribute__((ext_vector_type(4)));
typedef __attribute__((address_space(3))) s16x4* lds_s16x4_ptr;

#define DI __device__ __forceinline__
#define MFMA32(a, b, c) __builtin_amdgcn_mfma_f32_32x32x16_bf16((a), (b), (c), 0, 0, 0)
#define MFMA16(a, b, c) __builtin_amdgcn_mfma_f32_16x16x32_bf16((a), (b), (c), 0, 0, 0)
#define SBAR() __builtin_amdgcn_sched_barrier(0)
#define STAMP(i)

constexpr int NB = 32, SQ = 32, DM = 512, HQ = 32, HKV = 8, SK = 512, DH = 128;
constexpr int LDS_BYTES = 163840;
constexpr int QX_OFF = 131072;

DI unsigned pk2(float a, float b) {
  f32x2 f = {a, b};
  return __builtin_bit_cast(unsigned, __builtin_convertvector(f, bf16x2));
}
DI float swap_half_max(float v) {
  auto rr = __builtin_amdgcn_permlane32_swap(__float_as_uint(v), __float_as_uint(v), false, false);
  return fmaxf(__uint_as_float(rr[0]), __uint_as_float(rr[1]));
}
DI float swap_half_add(float v) {
  auto rr = __builtin_amdgcn_permlane32_swap(__float_as_uint(v), __float_as_uint(v), false, false);
  return __uint_as_float(rr[0]) + __uint_as_float(rr[1]);
}
DI void wg_barrier() {
  asm volatile("s_waitcnt lgkmcnt(0)" ::: "memory");
  __builtin_amdgcn_s_barrier();
  asm volatile("" ::: "memory");
}
DI void glds16_nt(const void* sbase, unsigned voff, unsigned lds_dst) {
  unsigned keep;
  asm volatile("s_mov_b32 %0, m0\n\ts_mov_b32 m0, %3\n\ts_nop 0\n\tglobal_load_lds_dwordx4 %1, %2 nt\n\ts_mov_b32 m0, %0"
               : "=&s"(keep) : "v"(voff), "s"(sbase), "s"(lds_dst) : "memory");
}
DI void glds16(const void* sbase, unsigned voff, unsigned lds_dst) {
  unsigned keep;
  asm volatile("s_mov_b32 %0, m0\n\ts_mov_b32 m0, %3\n\ts_nop 0\n\tglobal_load_lds_dwordx4 %1, %2\n\ts_mov_b32 m0, %0"
               : "=&s"(keep) : "v"(voff), "s"(sbase), "s"(lds_dst) : "memory");
}
template <int I>
DI bf16x8 tr_frag(const u32x4* w) {
  u32x4 o;
  constexpr unsigned sel = (I & 1) ? 0x07060302u : 0x05040100u;
#pragma unroll
  for (int m = 0; m < 4; ++m) o[m] = __builtin_amdgcn_perm(w[2 * m + 1][I >> 1], w[2 * m][I >> 1], sel);
  return __builtin_bit_cast(bf16x8, o);
}
#define PROJ_STEP(W, B0, B1, LOADS)                           \
  do {                                                        \
    bf16x8 a_[8];                                             \
    a_[0] = tr_frag<0>(W); a_[1] = tr_frag<1>(W); a_[2] = tr_frag<2>(W); a_[3] = tr_frag<3>(W); \
    a_[4] = tr_frag<4>(W); a_[5] = tr_frag<5>(W); a_[6] = tr_frag<6>(W); a_[7] = tr_frag<7>(W); \
    SBAR();                                                   \
    _Pragma("unroll") for (int i_ = 0; i_ < 8; ++i_) {        \
      acc[i_][0] = MFMA16(a_[i_], B0, acc[i_][0]);            \
      acc[i_][1] = MFMA16(a_[i_], B1, acc[i_][1]);            \
    }                                                         \
    LOADS;                                                    \
    _Pragma("unroll") for (int i_ = 0; i_ < 8; ++i_) {        \
      __builtin_amdgcn_sched_group_barrier(0x8, 2, 0);        \
      __builtin_amdgcn_sched_group_barrier(0x20, 1, 0);       \
    }                                                         \
  } while (0)

#define NPRE_A 8
#define NPRE_B 4
#define BW_STEP 12
#define RESC_THR 6.0f
constexpr int QD = 3;

__global__ __launch_bounds__(512, 2) void attn_kernel(const __hip_bfloat16* __restrict__ x,
                                                      const __hip_bfloat16* __restrict__ kc,
                                                      const __hip_bfloat16* __restrict__ vc,
                                                      const __hip_bfloat16* __restrict__ wq,
                                                      const __hip_bfloat16* __restrict__ wo,
                                                      char* __restrict__ ows) {
  extern __shared__ __attribute__((aligned(16))) char smem[];
  const int tid = threadIdx.x;
  const int wid = __builtin_amdgcn_readfirstlane(tid >> 6);
  const int lane = tid & 63;
  const int kvh = blockIdx.x & 7, b = blockIdx.x >> 3;
  const int hh = wid & 3, u = wid >> 2;
  const int r = lane & 31, h = lane >> 5;
  const int r16 = lane & 15, q = lane >> 4;
#define GRP_EXPR (b & 1)
#define ROT_EXPR ((b >> 1) & 15)
  const int grp = GRP_EXPR;
  const int rot = ROT_EXPR;
  STAMP(0);

  const unsigned lds0 = (unsigned)(size_t)smem;
  const size_t kvbase = ((size_t)(b * HKV + kvh)) * SK * DH;
  auto tile_isv = [](int t) { return t >= 14 ? 1 : (t < 2 ? 0 : (((t - 2) >> 1) & 1)); };
  auto tile_idx = [](int t) { return t >= 14 ? t - 8 : (t < 2 ? t : ((((t - 2) >> 1) & 1) ? 2 * ((t - 2) >> 2) + (t & 1) : 2 * ((t - 2) >> 2) + 2 + (t & 1))); };
  auto issue_tile = [&](int t) {
    const int slot = t < 10 ? t : t - 10;
    const int isv = tile_isv(t);
    const char* base = (const char*)((isv ? vc : kc) + kvbase + (size_t)tile_idx(t) * 64 * DH) + hh * 4096;
#pragma unroll
    for (int e = 0; e < 4; ++e) {
      const int chunk = isv ? (r16 ^ (q << 2)) : (r16 ^ (4 * e + q));
      glds16_nt(base + e * 1024, q * 256 + chunk * 16, lds0 + slot * 16384 + (4 * hh + e) * 1024);
    }
  };
  auto issue_tile8 = [&](int t) {
    const int slot = t < 10 ? t : t - 10;
    const int isv = tile_isv(t);
    const char* base = (const char*)((isv ? vc : kc) + kvbase + (size_t)tile_idx(t) * 64 * DH) + wid * 2048;
#pragma unroll
    for (int e = 0; e < 2; ++e) {
      const int chunk = isv ? (r16 ^ (q << 2)) : (r16 ^ (8 * (wid & 1) + 4 * e + q));
      glds16_nt(base + e * 1024, q * 256 + chunk * 16, lds0 + slot * 16384 + (2 * wid + e) * 1024);
    }
  };
  {
    const __hip_bfloat16* xb = x + (size_t)b * SQ * DM;
#pragma unroll
    for (int i = 0; i < 4; ++i) {
      const int row = 4 * wid + i;
      glds16(xb + row * DM, (lane ^ (row & 15)) * 16, lds0 + QX_OFF + row * 1024);
    }
  }

  bf16x8 qf[8];
  if (u == 1) {
    if (grp == 0) {
#pragma unroll
      for (int t = 0; t < NPRE_A; ++t) issue_tile(t);
      asm volatile("s_waitcnt vmcnt(%0)" ::"i"(4 * NPRE_A) : "memory");
    } else {
      asm volatile("s_waitcnt vmcnt(0)" ::: "memory");
    }
    wg_barrier();
    wg_barrier();
    if (grp == 1) {
#pragma unroll
      for (int t = 0; t < NPRE_B; ++t) issue_tile(t);
    }
    wg_barrier();
    wg_barrier();
#pragma unroll
    for (int p = 0; p < 8; ++p)
      qf[p] = *reinterpret_cast<const bf16x8*>(smem + QX_OFF + (hh * 8 + p) * 1024 + lane * 16);
    if (grp == 0) asm volatile("s_waitcnt vmcnt(%0)" ::"i"(4 * NPRE_A - 8) : "memory");
    else asm volatile("s_waitcnt vmcnt(%0)" ::"i"(4 * NPRE_B - 8) : "memory");
  } else {
    const auto wq_rs = __builtin_amdgcn_make_buffer_rsrc((void*)wq, 0, DM * HQ * DH * 2, 0x00020000);
    const int w_voff = (8 * q) * 8192 + (kvh * 512 + hh * 128 + 8 * r16) * 2;
    u32x4 w[QD][8];
#define PRE_W 1
#pragma unroll
    for (int d = 0; d < PRE_W; ++d) {
      const int sr = (d + rot) & 15;
#pragma unroll
      for (int jj = 0; jj < 8; ++jj)
        w[d][jj] = __builtin_amdgcn_raw_buffer_load_b128(wq_rs, w_voff, (32 * sr + jj) * 8192, 0);
    }
    asm volatile("s_waitcnt vmcnt(%0)" ::"i"(8 * PRE_W) : "memory");
    wg_barrier();
#pragma unroll
    for (int d = PRE_W; d < QD; ++d) {
      const int sr = (d + rot) & 15;
#pragma unroll
      for (int jj = 0; jj < 8; ++jj)
        w[d][jj] = __builtin_amdgcn_raw_buffer_load_b128(wq_rs, w_voff, (32 * sr + jj) * 8192, 0);
    }
    STAMP(14);
    f32x4 acc[8][2];
#pragma unroll
    for (int i = 0; i < 8; ++i) { acc[i][0] = f32x4{}; acc[i][1] = f32x4{}; }
    const char* xs = smem + QX_OFF + r16 * 1024;
#pragma unroll
    for (int s = 0; s < 16; ++s) {
      SBAR();
      const int xo = ((4 * ((s + rot) & 15) + q) ^ r16) << 4;
      const bf16x8 b0 = *reinterpret_cast<const bf16x8*>(xs + xo);
      const bf16x8 b1 = *reinterpret_cast<const bf16x8*>(xs + xo + 16384);
      const int sr = (s + QD + rot) & 15;
      PROJ_STEP(w[s % QD], b0, b1, if (s + QD < 16) {
        _Pragma("unroll") for (int jj = 0; jj < 8; ++jj)
          w[s % QD][jj] = __builtin_amdgcn_raw_buffer_load_b128(wq_rs, w_voff, (32 * sr + jj) * 8192, 0);
      });
      if (s == BW_STEP) { SBAR(); __builtin_amdgcn_s_barrier(); }
    }
#pragma unroll
    for (int g = 0; g < 4; ++g) {
      u32x4 f0, f1;
#pragma unroll
      for (int e = 0; e < 4; ++e) {
        const unsigned a0 = pk2(acc[2 * e][0][g], acc[2 * e + 1][0][g]);
        const unsigned a1 = pk2(acc[2 * e][1][g], acc[2 * e + 1][1][g]);
        auto rr = __builtin_amdgcn_permlane16_swap(a0, a1, false, false);
        f0[e] = rr[0];
        f1[e] = rr[1];
      }
      qf[g] = __builtin_bit_cast(bf16x8, f0);
      qf[4 + g] = __builtin_bit_cast(bf16x8, f1);
    }
    wg_barrier();
#pragma unroll
    for (int p = 0; p < 8; ++p)
      *reinterpret_cast<bf16x8*>(smem + QX_OFF + (hh * 8 + p) * 1024 + lane * 16) = qf[p];
    STAMP(1);
    wg_barrier();
  }

  constexpr float CS = 0.088388347648318447f * 1.4426950408889634f;
  float m = -__builtin_inff(), l = 0.f;
  f32x16 o[4];
#pragma unroll
  for (int i = 0; i < 4; ++i) o[i] = f32x16{};
  const int ky = (r & 15) ^ (8 * h);
  const int koff = r * 256;
  const int vq = (lane >> 2) & 3, vpp = lane & 3;
  const int voff = (4 * h + vq) * 256 + (2 * (q & 1) + (vpp >> 1)) * 16 + 8 * (vpp & 1);
  bf16x8 fb[16];
  bf16x8 pf[4];
  f32x16 s0, s1;
#define SLOT(T) (smem + (((T) < 10 ? (T) : (T) - 10) + u) * 16384)
#define KT(S) ((S) == 0 ? 0 : 4 * (S) - 2)
#define VT(S) ((S) == 3 ? 14 : 4 * (S) + 4)
#define K_READ(S, H)                                                                                        \
  do {                                                                                                      \
    const char* Ks = SLOT(KT(S));                                                                           \
    _Pragma("unroll") for (int p = 4 * (H); p < 4 * (H) + 4; ++p) {                                         \
      const char* a = Ks + koff + ((p ^ ky) << 4);                                                          \
      fb[2 * p] = *reinterpret_cast<const bf16x8*>(a);                                                      \
      fb[2 * p + 1] = *reinterpret_cast<const bf16x8*>(a + 8192);                                           \
    }                                                                                                       \
  } while (0)
#define QK_MMA()                                                                                            \
  do {                                                                                                      \
    s0 = f32x16{}; s1 = f32x16{};                                                                           \
    _Pragma("unroll") for (int p = 0; p < 8; ++p) {                                                         \
      s0 = MFMA32(fb[2 * p], qf[p], s0);                                                                    \
      s1 = MFMA32(fb[2 * p + 1], qf[p], s1);                                                                \
    }                                                                                                       \
  } while (0)
#define V_READ(S, DT)                                                                                       \
  do {                                                                                                      \
    const char* vb = SLOT(VT(S)) + voff + (((DT) ^ vq) << 6);                                         \
    _Pragma("unroll") for (int ks = 0; ks < 4; ++ks) {                                                      \
      const s16x4 lo = __builtin_amdgcn_ds_read_tr16_b64_v4i16((lds_s16x4_ptr)(vb + ks * 4096));            \
      const s16x4 hi = __builtin_amdgcn_ds_read_tr16_b64_v4i16((lds_s16x4_ptr)(vb + ks * 4096 + 2048));     \
      fb[(DT) * 4 + ks] = __builtin_bit_cast(bf16x8, __builtin_shufflevector(lo, hi, 0, 1, 2, 3, 4, 5, 6, 7)); \
    }                                                                                                       \
  } while (0)
#define PRIO(x)
#define PV_MMA(D0)                                                                                          \
  do {                                                                                                      \
    _Pragma("unroll") for (int ks = 0; ks < 4; ++ks)                                                        \
      _Pragma("unroll") for (int dt = (D0); dt < (D0) + 2; ++dt) o[dt] = MFMA32(fb[dt * 4 + ks], pf[ks], o[dt]); \
  } while (0)
#define SOFTMAX(S)                                                                                          \
  do {                                                                                                      \
    float tmax = s0[0];                                                                                     \
    _Pragma("unroll") for (int i = 1; i < 16; ++i) tmax = fmaxf(tmax, s0[i]);                               \
    _Pragma("unroll") for (int i = 0; i < 16; ++i) tmax = fmaxf(tmax, s1[i]);                               \
    tmax = swap_half_max(tmax);                                                                             \
      \
      \
    if ((S) == 0) {                                                                                         \
      m = tmax;                                                                                             \
    } else if (__builtin_amdgcn_ballot_w64((tmax - m) * CS > RESC_THR) != 0) {                              \
      const float mnew = fmaxf(m, tmax);                                                                    \
      const float alpha = __builtin_amdgcn_exp2f((m - mnew) * CS);                                          \
      m = mnew;                                                                                             \
      l *= alpha;                                                                                           \
      _Pragma("unroll") for (int dt = 0; dt < 4; ++dt)                                                      \
        _Pragma("unroll") for (int i = 0; i < 16; ++i) o[dt][i] *= alpha;                                   \
    }                                                                                                       \
    const float mc = m * CS;                                                                                \
    float ps = 0.f;                                                                                         \
    _Pragma("unroll") for (int i = 0; i < 16; ++i) { s0[i] = __builtin_amdgcn_exp2f(fmaf(s0[i], CS, -mc)); ps += s0[i]; } \
    _Pragma("unroll") for (int i = 0; i < 16; ++i) { s1[i] = __builtin_amdgcn_exp2f(fmaf(s1[i], CS, -mc)); ps += s1[i]; } \
    l += ps;                                                                                                \
    asm volatile("" : "+v"(l));                                                                             \
    _Pragma("unroll") for (int ss = 0; ss < 2; ++ss) {                                                      \
      u32x4 u0, u1;                                                                                         \
      _Pragma("unroll") for (int e = 0; e < 4; ++e) {                                                       \
        u0[e] = pk2(s0[8 * ss + 2 * e], s0[8 * ss + 2 * e + 1]);                                            \
        u1[e] = pk2(s1[8 * ss + 2 * e], s1[8 * ss + 2 * e + 1]);                                            \
      }                                                                                                     \
      pf[ss] = __builtin_bit_cast(bf16x8, u0);                                                              \
      pf[2 + ss] = __builtin_bit_cast(bf16x8, u1);                                                          \
    }                                                                                                       \
  } while (0)
#define DMA_A(S)                                                                                            \
  do {                                                                                                      \
    if ((S) == 0) issue_tile8(8);                                                                           \
    if ((S) == 1) issue_tile8(10);                                                                          \
    if ((S) == 2) { issue_tile8(12); issue_tile8(13); }                                                     \
    if ((S) == 3) {                                                                                         \
                    \
      const char* wb = (const char*)wo + (size_t)(kvh * 512 + 16 * b + 2 * wid) * 1024;                     \
      glds16(wb, lane * 16, lds0 + 6 * 16384 + 2 * wid * 1024);                                             \
      glds16(wb + 1024, lane * 16, lds0 + 6 * 16384 + (2 * wid + 1) * 1024);                                \
    }                                                                                                       \
  } while (0)
#define DMA_B(S)                                                                                            \
  do {                                                                                                      \
    if ((S) == 0) issue_tile8(9);                                                                           \
    if ((S) == 1) issue_tile8(11);                                                                          \
    if ((S) == 2) { issue_tile8(14); issue_tile8(15); }                                                     \
  } while (0)
#define STEP_WAIT(S)                                                                                        \
  do {                                                                                                      \
    if ((S) == 0) asm volatile("s_waitcnt vmcnt(12)" ::: "memory");                                         \
    if ((S) == 1 || (S) == 2) asm volatile("s_waitcnt vmcnt(4)" ::: "memory");                              \
    if ((S) == 3) asm volatile("s_waitcnt vmcnt(2)" ::: "memory");                                          \
  } while (0)

  char* mlp = smem + 7 * 16384;
  if (u == 0) {
#pragma unroll
    for (int s = 0; s < 4; ++s) {
      wg_barrier();
      STAMP(2 + 2 * s);
      if (s > 0) {
        V_READ(s - 1, 0);
        V_READ(s - 1, 1);
        V_READ(s - 1, 2);
        V_READ(s - 1, 3);
        DMA_A(s);
        if (s != 2) DMA_B(s);
        SBAR();
        PRIO(1);
        PV_MMA(0);
        SBAR();
        K_READ(s, 0);
        SBAR();
        PV_MMA(2);
        SBAR();
        K_READ(s, 1);
      } else {
        K_READ(s, 0);
        K_READ(s, 1);
        DMA_A(s);
        DMA_B(s);
      }
      SBAR();
      QK_MMA();
      SBAR();
      PRIO(0);
      STAMP(3 + 2 * s);
      if (s == 2) DMA_B(s);
      SOFTMAX(s);
      SBAR();
      STEP_WAIT(s);
    }
    l = swap_half_add(l);
    if (h == 0) *reinterpret_cast<f32x2*>(mlp + wid * 256 + r * 8) = f32x2{m, l};
    wg_barrier();
    V_READ(3, 0);
    V_READ(3, 1);
    V_READ(3, 2);
    V_READ(3, 3);
    SBAR();
    PV_MMA(0);
    PV_MMA(2);
    SBAR();
  } else {
#pragma unroll
    for (int s = 0; s < 5; ++s) {
      wg_barrier();
      if (s == 0) {
        if (grp == 0) {
#pragma unroll
          for (int t = NPRE_A; t < 8; ++t) issue_tile(t);
        } else {
#pragma unroll
          for (int t = NPRE_B; t < 8; ++t) issue_tile(t);
        }
      }
      if (s < 4) { DMA_A(s); if (s != 2) DMA_B(s); }
      if (s > 0) {
        V_READ(s - 1, 0);
        V_READ(s - 1, 1);
        SBAR();
        SOFTMAX(s - 1);
        SBAR();
        if (s == 2) DMA_B(s);
        V_READ(s - 1, 2);
        V_READ(s - 1, 3);
        SBAR();
        PRIO(1);
        PV_MMA(0);
        SBAR();
        if (s < 4) K_READ(s, 0);
        SBAR();
        PV_MMA(2);
        SBAR();
        if (s < 4) K_READ(s, 1);
      } else {
        K_READ(s, 0);
        K_READ(s, 1);
      }
      if (s < 4) {
        SBAR();
        QK_MMA();
        SBAR();
        PRIO(0);
        STEP_WAIT(s);
      }
    }
    PRIO(0);
    l = swap_half_add(l);
    if (h == 0) *reinterpret_cast<f32x2*>(mlp + wid * 256 + r * 8) = f32x2{m, l};
  }
  const auto wo_rs = __builtin_amdgcn_make_buffer_rsrc((void*)wo, 0, HQ * DH * DM * 2, 0x00020000);
  const int wo_voff = (kvh * 512 + 8 * q) * 1024 + (hh * 128 + 8 * r16) * 2;
  u32x4 w[QD][8];
  if (u == 0) {
#pragma unroll
    for (int d = 0; d < QD; ++d)
#pragma unroll
      for (int jj = 0; jj < 8; ++jj)
        w[d][jj] = __builtin_amdgcn_raw_buffer_load_b128(wo_rs, wo_voff, (32 * ((d + rot) & 15) + jj) * 1024, 0);
  }
  SBAR();
  wg_barrier();
  STAMP(10);
  {
    char* xb_ = smem + wid * 8192;
#pragma unroll
    for (int c = 0; c < 2; ++c) {
#pragma unroll
      for (int g = 0; g < 4; ++g) {
        f32x4 v;
        if (u == 0) v = f32x4{o[2 + c][4 * g], o[2 + c][4 * g + 1], o[2 + c][4 * g + 2], o[2 + c][4 * g + 3]};
        else v = f32x4{o[c][4 * g], o[c][4 * g + 1], o[c][4 * g + 2], o[c][4 * g + 3]};
        *reinterpret_cast<f32x4*>(xb_ + (c * 4 + g) * 1024 + lane * 16) = v;
      }
    }
    const f32x2 ml2 = *reinterpret_cast<const f32x2*>(mlp + (wid ^ 4) * 256 + r * 8);
    const float mm = fmaxf(m, ml2[0]);
    const float e1 = __builtin_amdgcn_exp2f((m - mm) * CS), e2 = __builtin_amdgcn_exp2f((ml2[0] - mm) * CS);
    const float inv = 1.0f / (l * e1 + ml2[1] * e2);
    const float f1 = e1 * inv, f2 = e2 * inv;
    wg_barrier();
    const char* pb = smem + (wid ^ 4) * 8192;
    char* of = smem + QX_OFF + (r >> 4) * 1024 + (r & 15) * 16 + 8 * h;
    f32x4 pv[8];
#pragma unroll
    for (int c = 0; c < 8; ++c) pv[c] = *reinterpret_cast<const f32x4*>(pb + c * 1024 + lane * 16);
    SBAR();
#pragma unroll
    for (int c = 0; c < 2; ++c) {
      const int dt = 2 * u + c;
#pragma unroll
      for (int g = 0; g < 4; ++g) {
        f32x4 ov;
        if (u == 0) ov = f32x4{o[c][4 * g], o[c][4 * g + 1], o[c][4 * g + 2], o[c][4 * g + 3]};
        else ov = f32x4{o[2 + c][4 * g], o[2 + c][4 * g + 1], o[2 + c][4 * g + 2], o[2 + c][4 * g + 3]};
        const f32x4 p2 = pv[c * 4 + g];
        u32x2 v;
        v[0] = pk2(ov[0] * f1 + p2[0] * f2, ov[1] * f1 + p2[1] * f2);
        v[1] = pk2(ov[2] * f1 + p2[2] * f2, ov[3] * f1 + p2[3] * f2);
        *reinterpret_cast<u32x2*>(of + (hh * 4 + dt) * 2048 + g * 256) = v;
      }
    }
  }
  if (u == 1) {
    asm volatile("s_waitcnt vmcnt(0)" ::: "memory");
    wg_barrier();
    return;
  }
  {
    wg_barrier();
    STAMP(11);
    f32x4 acc[8][2];
#pragma unroll
    for (int i = 0; i < 8; ++i) { acc[i][0] = f32x4{}; acc[i][1] = f32x4{}; }
    const char* of = smem + QX_OFF + lane * 16;
#pragma unroll
    for (int s = 0; s < 16; ++s) {
      SBAR();
      const bf16x8 b0 = *reinterpret_cast<const bf16x8*>(of + ((s + rot) & 15) * 2048);
      const bf16x8 b1 = *reinterpret_cast<const bf16x8*>(of + ((s + rot) & 15) * 2048 + 1024);
      PROJ_STEP(w[s % QD], b0, b1, if (s + QD < 16) {
        _Pragma("unroll") for (int jj = 0; jj < 8; ++jj)
          w[s % QD][jj] = __builtin_amdgcn_raw_buffer_load_b128(wo_rs, wo_voff, (32 * ((s + QD + rot) & 15) + jj) * 1024, 0);
      });
    }
    STAMP(12);
    char* pp = ows + ((size_t)((kvh * NB + b) * 4 + hh)) * 8192 + lane * 16;
#pragma unroll
    for (int T = 0; T < 2; ++T)
#pragma unroll
      for (int g = 0; g < 4; ++g) {
        f32x4 v0 = {acc[0][T][g], acc[1][T][g], acc[2][T][g], acc[3][T][g]};
        f32x4 v1 = {acc[4][T][g], acc[5][T][g], acc[6][T][g], acc[7][T][g]};
        u32x4 pk = {pk2(v0[0], v0[1]), pk2(v0[2], v0[3]), pk2(v1[0], v1[1]), pk2(v1[2], v1[3])};
        *reinterpret_cast<u32x4*>(pp + (T * 4 + g) * 1024) = pk;
      }
    STAMP(13);
  }
}

__global__ __launch_bounds__(256) void reduce_kernel(const char* __restrict__ ows, __hip_bfloat16* __restrict__ out) {
  const int gid = blockIdx.x * 256 + threadIdx.x;
  const int lane = gid & 63, tg = (gid >> 6) & 7, bc = gid >> 9;
  const char* pp = ows + (size_t)bc * 8192 + tg * 1024 + lane * 16;
  u32x4 v[8];
#pragma unroll
  for (int k = 0; k < 8; ++k) v[k] = *reinterpret_cast<const u32x4*>(pp + (size_t)k * (NB * 4 * 8192));
  f32x4 a = {0.f, 0.f, 0.f, 0.f}, c = {0.f, 0.f, 0.f, 0.f};
#pragma unroll
  for (int k = 0; k < 8; ++k) {
    a[0] += __uint_as_float(v[k][0] << 16); a[1] += __uint_as_float(v[k][0] & 0xffff0000u);
    a[2] += __uint_as_float(v[k][1] << 16); a[3] += __uint_as_float(v[k][1] & 0xffff0000u);
    c[0] += __uint_as_float(v[k][2] << 16); c[1] += __uint_as_float(v[k][2] & 0xffff0000u);
    c[2] += __uint_as_float(v[k][3] << 16); c[3] += __uint_as_float(v[k][3] & 0xffff0000u);
  }
  const int b = bc >> 2, cg = bc & 3, T = tg >> 2, g = tg & 3, r16 = lane & 15, q = lane >> 4;
  u32x4 o = {pk2(a[0], a[1]), pk2(a[2], a[3]), pk2(c[0], c[1]), pk2(c[2], c[3])};
  *reinterpret_cast<u32x4*>(out + (size_t)(b * SQ + 16 * T + r16) * DM + 128 * cg + 32 * q + 8 * g) = o;
}

extern "C" void kernel_launch(void* const* d_in, const int* in_sizes, int n_in, void* d_out, int out_size,
                              void* d_ws, size_t ws_size, hipStream_t stream) {
  const __hip_bfloat16* x = (const __hip_bfloat16*)d_in[0];
  const __hip_bfloat16* kc = (const __hip_bfloat16*)d_in[1];
  const __hip_bfloat16* vc = (const __hip_bfloat16*)d_in[2];
  const __hip_bfloat16* wq = (const __hip_bfloat16*)d_in[3];
  const __hip_bfloat16* wo = (const __hip_bfloat16*)d_in[4];
  (void)hipFuncSetAttribute((const void*)attn_kernel, hipFuncAttributeMaxDynamicSharedMemorySize, LDS_BYTES);
  attn_kernel<<<256, 512, LDS_BYTES, stream>>>(x, kc, vc, wq, wo, (char*)d_ws);
  reduce_kernel<<<256, 256, 0, stream>>>((const char*)d_ws, (__hip_bfloat16*)d_out);
}
```

```cpp
#include <hip/hip_runtime.h>
#include <hip/hip_bf16.h>

typedef __bf16 bf16x8 __attribute__((ext_vector_type(8)));
typedef __bf16 bf16x2 __attribute__((ext_vector_type(2)));
typedef short s16x4 __attribute__((ext_vector_type(4)));
typedef float f32x2 __attribute__((ext_vector_type(2)));
typedef float f32x4 __attribute__((ext_vector_type(4)));
typedef float f32x16 __attribute__((ext_vector_type(16)));
typedef unsigned u32x2 __attribute__((ext_vector_type(2)));
typedef unsigned u32x4 __attribute__((ext_vector_type(4)));
typedef __attribute__((address_space(3))) s16x4* lds_s16x4_ptr;

#define DI __device__ __forceinline__
#define MFMA32(a, b, c) __builtin_amdgcn_mfma_f32_32x32x16_bf16((a), (b), (c), 0, 0, 0)
#define MFMA16(a, b, c) __builtin_amdgcn_mfma_f32_16x16x32_bf16((a), (b), (c), 0, 0, 0)
#define SBAR() __builtin_amdgcn_sched_barrier(0)
#define STAMP(i)

constexpr int NB = 32, SQ = 32, DM = 512, HQ = 32, HKV = 8, SK = 512, DH = 128;
constexpr int LDS_BYTES = 163840;
constexpr int QX_OFF = 131072;

DI unsigned pk2(float a, float b) {
  f32x2 f = {a, b};
  return __builtin_bit_cast(unsigned, __builtin_convertvector(f, bf16x2));
}
DI float swap_half_max(float v) {
  auto rr = __builtin_amdgcn_permlane32_swap(__float_as_uint(v), __float_as_uint(v), false, false);
  return fmaxf(__uint_as_float(rr[0]), __uint_as_float(rr[1]));
}
DI float swap_half_add(float v) {
  auto rr = __builtin_amdgcn_permlane32_swap(__float_as_uint(v), __float_as_uint(v), false, false);
  return __uint_as_float(rr[0]) + __uint_as_float(rr[1]);
}
DI void wg_barrier() {
  asm volatile("s_waitcnt lgkmcnt(0)" ::: "memory");
  __builtin_amdgcn_s_barrier();
  asm volatile("" ::: "memory");
}
DI void glds16_nt(const void* sbase, unsigned voff, unsigned lds_dst) {
  unsigned keep;
  asm volatile("s_mov_b32 %0, m0\n\ts_mov_b32 m0, %3\n\ts_nop 0\n\tglobal_load_lds_dwordx4 %1, %2 nt\n\ts_mov_b32 m0, %0"
               : "=&s"(keep) : "v"(voff), "s"(sbase), "s"(lds_dst) : "memory");
}
DI void glds16(const void* sbase, unsigned voff, unsigned lds_dst) {
  unsigned keep;
  asm volatile("s_mov_b32 %0, m0\n\ts_mov_b32 m0, %3\n\ts_nop 0\n\tglobal_load_lds_dwordx4 %1, %2\n\ts_mov_b32 m0, %0"
               : "=&s"(keep) : "v"(voff), "s"(sbase), "s"(lds_dst) : "memory");
}
template <int I>
DI bf16x8 tr_frag(const u32x4* w) {
  u32x4 o;
  constexpr unsigned sel = (I & 1) ? 0x07060302u : 0x05040100u;
#pragma unroll
  for (int m = 0; m < 4; ++m) o[m] = __builtin_amdgcn_perm(w[2 * m + 1][I >> 1], w[2 * m][I >> 1], sel);
  return __builtin_bit_cast(bf16x8, o);
}
#define PROJ_STEP(W, B0, B1, LOADS)                           \
  do {                                                        \
    bf16x8 a_[8];                                             \
    a_[0] = tr_frag<0>(W); a_[1] = tr_frag<1>(W); a_[2] = tr_frag<2>(W); a_[3] = tr_frag<3>(W); \
    a_[4] = tr_frag<4>(W); a_[5] = tr_frag<5>(W); a_[6] = tr_frag<6>(W); a_[7] = tr_frag<7>(W); \
    SBAR();                                                   \
    _Pragma("unroll") for (int i_ = 0; i_ < 8; ++i_) {        \
      acc[i_][0] = MFMA16(a_[i_], B0, acc[i_][0]);            \
      acc[i_][1] = MFMA16(a_[i_], B1, acc[i_][1]);            \
    }                                                         \
    LOADS;                                                    \
    _Pragma("unroll") for (int i_ = 0; i_ < 8; ++i_) {        \
      __builtin_amdgcn_sched_group_barrier(0x8, 2, 0);        \
      __builtin_amdgcn_sched_group_barrier(0x20, 1, 0);       \
    }                                                         \
  } while (0)

#define NPRE_A 8
#define NPRE_B 4
#define BW_STEP 12
#define RESC_THR 6.0f
constexpr int QD = 3;

__global__ __launch_bounds__(512, 2) void attn_kernel(const __hip_bfloat16* __restrict__ x,
                                                      const __hip_bfloat16* __restrict__ kc,
                                                      const __hip_bfloat16* __restrict__ vc,
                                                      const __hip_bfloat16* __restrict__ wq,
                                                      const __hip_bfloat16* __restrict__ wo,
                                                      char* __restrict__ ows) {
  extern __shared__ __attribute__((aligned(16))) char smem[];
  const int tid = threadIdx.x;
  const int wid = __builtin_amdgcn_readfirstlane(tid >> 6);
  const int lane = tid & 63;
  const int kvh = blockIdx.x & 7, b = blockIdx.x >> 3;
  const int hh = wid & 3, u = wid >> 2;
  const int r = lane & 31, h = lane >> 5;
  const int r16 = lane & 15, q = lane >> 4;
#define GRP_EXPR (b & 1)
#define ROT_EXPR ((b >> 1) & 15)
  const int grp = GRP_EXPR;
  const int rot = ROT_EXPR;
  STAMP(0);

  const unsigned lds0 = (unsigned)(size_t)smem;
  const size_t kvbase = ((size_t)(b * HKV + kvh)) * SK * DH;
  auto tile_isv = [](int t) { return t >= 14 ? 1 : (t < 2 ? 0 : (((t - 2) >> 1) & 1)); };
  auto tile_idx = [](int t) { return t >= 14 ? t - 8 : (t < 2 ? t : ((((t - 2) >> 1) & 1) ? 2 * ((t - 2) >> 2) + (t & 1) : 2 * ((t - 2) >> 2) + 2 + (t & 1))); };
  auto issue_tile = [&](int t) {
    const int slot = t < 10 ? t : t - 10;
    const int isv = tile_isv(t);
    const char* base = (const char*)((isv ? vc : kc) + kvbase + (size_t)tile_idx(t) * 64 * DH) + hh * 4096;
#pragma unroll
    for (int e = 0; e < 4; ++e) {
      const int chunk = isv ? (r16 ^ (q << 2)) : (r16 ^ (4 * e + q));
      glds16_nt(base + e * 1024, q * 256 + chunk * 16, lds0 + slot * 16384 + (4 * hh + e) * 1024);
    }
  };
  auto issue_tile8 = [&](int t) {
    const int slot = t < 10 ? t : t - 10;
    const int isv = tile_isv(t);
    const char* base = (const char*)((isv ? vc : kc) + kvbase + (size_t)tile_idx(t) * 64 * DH) + wid * 2048;
#pragma unroll
    for (int e = 0; e < 2; ++e) {
      const int chunk = isv ? (r16 ^ (q << 2)) : (r16 ^ (8 * (wid & 1) + 4 * e + q));
      glds16_nt(base + e * 1024, q * 256 + chunk * 16, lds0 + slot * 16384 + (2 * wid + e) * 1024);
    }
  };
  {
    const __hip_bfloat16* xb = x + (size_t)b * SQ * DM;
#pragma unroll
    for (int i = 0; i < 4; ++i) {
      const int row = 4 * wid + i;
      glds16(xb + row * DM, (lane ^ (row & 15)) * 16, lds0 + QX_OFF + row * 1024);
    }
  }

  bf16x8 qf[8];
  if (u == 1) {
    if (grp == 0) {
#pragma unroll
      for (int t = 0; t < NPRE_A; ++t) issue_tile(t);
      asm volatile("s_waitcnt vmcnt(%0)" ::"i"(4 * NPRE_A) : "memory");
    } else {
      asm volatile("s_waitcnt vmcnt(0)" ::: "memory");
    }
    wg_barrier();
    wg_barrier();
    if (grp == 1) {
#pragma unroll
      for (int t = 0; t < NPRE_B; ++t) issue_tile(t);
    }
    wg_barrier();
    wg_barrier();
#pragma unroll
    for (int p = 0; p < 8; ++p)
      qf[p] = *reinterpret_cast<const bf16x8*>(smem + QX_OFF + (hh * 8 + p) * 1024 + lane * 16);
    if (grp == 0) asm volatile("s_waitcnt vmcnt(%0)" ::"i"(4 * NPRE_A - 8) : "memory");
    else asm volatile("s_waitcnt vmcnt(%0)" ::"i"(4 * NPRE_B - 8) : "memory");
  } else {
    const auto wq_rs = __builtin_amdgcn_make_buffer_rsrc((void*)wq, 0, DM * HQ * DH * 2, 0x00020000);
    const int w_voff = (8 * q) * 8192 + (kvh * 512 + hh * 128 + 8 * r16) * 2;
    u32x4 w[QD][8];
#define PRE_W 1
#pragma unroll
    for (int d = 0; d < PRE_W; ++d) {
      const int sr = (d + rot) & 15;
#pragma unroll
      for (int jj = 0; jj < 8; ++jj)
        w[d][jj] = __builtin_amdgcn_raw_buffer_load_b128(wq_rs, w_voff, (32 * sr + jj) * 8192, 0);
    }
    asm volatile("s_waitcnt vmcnt(%0)" ::"i"(8 * PRE_W) : "memory");
    wg_barrier();
#pragma unroll
    for (int d = PRE_W; d < QD; ++d) {
      const int sr = (d + rot) & 15;
#pragma unroll
      for (int jj = 0; jj < 8; ++jj)
        w[d][jj] = __builtin_amdgcn_raw_buffer_load_b128(wq_rs, w_voff, (32 * sr + jj) * 8192, 0);
    }
    STAMP(14);
    f32x4 acc[8][2];
#pragma unroll
    for (int i = 0; i < 8; ++i) { acc[i][0] = f32x4{}; acc[i][1] = f32x4{}; }
    const char* xs = smem + QX_OFF + r16 * 1024;
#pragma unroll
    for (int s = 0; s < 16; ++s) {
      SBAR();
      const int xo = ((4 * ((s + rot) & 15) + q) ^ r16) << 4;
      const bf16x8 b0 = *reinterpret_cast<const bf16x8*>(xs + xo);
      const bf16x8 b1 = *reinterpret_cast<const bf16x8*>(xs + xo + 16384);
      const int sr = (s + QD + rot) & 15;
      PROJ_STEP(w[s % QD], b0, b1, if (s + QD < 16) {
        _Pragma("unroll") for (int jj = 0; jj < 8; ++jj)
          w[s % QD][jj] = __builtin_amdgcn_raw_buffer_load_b128(wq_rs, w_voff, (32 * sr + jj) * 8192, 0);
      });
      if (s == BW_STEP) { SBAR(); __builtin_amdgcn_s_barrier(); }
    }
#pragma unroll
    for (int g = 0; g < 4; ++g) {
      u32x4 f0, f1;
#pragma unroll
      for (int e = 0; e < 4; ++e) {
        const unsigned a0 = pk2(acc[2 * e][0][g], acc[2 * e + 1][0][g]);
        const unsigned a1 = pk2(acc[2 * e][1][g], acc[2 * e + 1][1][g]);
        auto rr = __builtin_amdgcn_permlane16_swap(a0, a1, false, false);
        f0[e] = rr[0];
        f1[e] = rr[1];
      }
      qf[g] = __builtin_bit_cast(bf16x8, f0);
      qf[4 + g] = __builtin_bit_cast(bf16x8, f1);
    }
    wg_barrier();
#pragma unroll
    for (int p = 0; p < 8; ++p)
      *reinterpret_cast<bf16x8*>(smem + QX_OFF + (hh * 8 + p) * 1024 + lane * 16) = qf[p];
    STAMP(1);
    wg_barrier();
  }

  constexpr float CS = 0.088388347648318447f * 1.4426950408889634f;
  float m = -__builtin_inff(), l = 0.f;
  f32x16 o[4];
#pragma unroll
  for (int i = 0; i < 4; ++i) o[i] = f32x16{};
  const int ky = (r & 15) ^ (8 * h);
  const int koff = r * 256;
  const int vq = (lane >> 2) & 3, vpp = lane & 3;
  const int voff = (4 * h + vq) * 256 + (2 * (q & 1) + (vpp >> 1)) * 16 + 8 * (vpp & 1);
  bf16x8 fb[16];
  bf16x8 pf[4];
  f32x16 s0, s1;
#define SLOT(T) (smem + (((T) < 10 ? (T) : (T) - 10) + u) * 16384)
#define KT(S) ((S) == 0 ? 0 : 4 * (S) - 2)
#define VT(S) ((S) == 3 ? 14 : 4 * (S) + 4)
#define K_READ(S, H)                                                                                        \
  do {                                                                                                      \
    const char* Ks = SLOT(KT(S));                                                                           \
    _Pragma("unroll") for (int p = 4 * (H); p < 4 * (H) + 4; ++p) {                                         \
      const char* a = Ks + koff + ((p ^ ky) << 4);                                                          \
      fb[2 * p] = *reinterpret_cast<const bf16x8*>(a);                                                      \
      fb[2 * p + 1] = *reinterpret_cast<const bf16x8*>(a + 8192);                                           \
    }                                                                                                       \
  } while (0)
#define QK_MMA()                                                                                            \
  do {                                                                                                      \
    s0 = f32x16{}; s1 = f32x16{};                                                                           \
    _Pragma("unroll") for (int p = 0; p < 8; ++p) {                                                         \
      s0 = MFMA32(fb[2 * p], qf[p], s0);                                                                    \
      s1 = MFMA32(fb[2 * p + 1], qf[p], s1);                                                                \
    }                                                                                                       \
  } while (0)
#define V_READ(S, DT)                                                                                       \
  do {                                                                                                      \
    const char* vb = SLOT(VT(S)) + voff + (((DT) ^ vq) << 6);                                         \
    _Pragma("unroll") for (int ks = 0; ks < 4; ++ks) {                                                      \
      const s16x4 lo = __builtin_amdgcn_ds_read_tr16_b64_v4i16((lds_s16x4_ptr)(vb + ks * 4096));            \
      const s16x4 hi = __builtin_amdgcn_ds_read_tr16_b64_v4i16((lds_s16x4_ptr)(vb + ks * 4096 + 2048));     \
      fb[(DT) * 4 + ks] = __builtin_bit_cast(bf16x8, __builtin_shufflevector(lo, hi, 0, 1, 2, 3, 4, 5, 6, 7)); \
    }                                                                                                       \
  } while (0)
#define PRIO(x)
#define PV_MMA(D0)                                                                                          \
  do {                                                                                                      \
    _Pragma("unroll") for (int ks = 0; ks < 4; ++ks)                                                        \
      _Pragma("unroll") for (int dt = (D0); dt < (D0) + 2; ++dt) o[dt] = MFMA32(fb[dt * 4 + ks], pf[ks], o[dt]); \
  } while (0)
#define SOFTMAX(S)                                                                                          \
  do {                                                                                                      \
    float tmax = s0[0];                                                                                     \
    _Pragma("unroll") for (int i = 1; i < 16; ++i) tmax = fmaxf(tmax, s0[i]);                               \
    _Pragma("unroll") for (int i = 0; i < 16; ++i) tmax = fmaxf(tmax, s1[i]);                               \
    tmax = swap_half_max(tmax);                                                                             \
      \
      \
    if ((S) == 0) {                                                                                         \
      m = tmax;                                                                                             \
    } else if (__builtin_amdgcn_ballot_w64((tmax - m) * CS > RESC_THR) != 0) {                              \
      const float mnew = fmaxf(m, tmax);                                                                    \
      const float alpha = __builtin_amdgcn_exp2f((m - mnew) * CS);                                          \
      m = mnew;                                                                                             \
      l *= alpha;                                                                                           \
      _Pragma("unroll") for (int dt = 0; dt < 4; ++dt)                                                      \
        _Pragma("unroll") for (int i = 0; i < 16; ++i) o[dt][i] *= alpha;                                   \
    }                                                                                                       \
    const float mc = m * CS;                                                                                \
    float ps = 0.f;                                                                                         \
    _Pragma("unroll") for (int i = 0; i < 16; ++i) { s0[i] = __builtin_amdgcn_exp2f(fmaf(s0[i], CS, -mc)); ps += s0[i]; } \
    _Pragma("unroll") for (int i = 0; i < 16; ++i) { s1[i] = __builtin_amdgcn_exp2f(fmaf(s1[i], CS, -mc)); ps += s1[i]; } \
    l += ps;                                                                                                \
    asm volatile("" : "+v"(l));                                                                             \
    _Pragma("unroll") for (int ss = 0; ss < 2; ++ss) {                                                      \
      u32x4 u0, u1;                                                                                         \
      _Pragma("unroll") for (int e = 0; e < 4; ++e) {                                                       \
        u0[e] = pk2(s0[8 * ss + 2 * e], s0[8 * ss + 2 * e + 1]);                                            \
        u1[e] = pk2(s1[8 * ss + 2 * e], s1[8 * ss + 2 * e + 1]);                                            \
      }                                                                                                     \
      pf[ss] = __builtin_bit_cast(bf16x8, u0);                                                              \
      pf[2 + ss] = __builtin_bit_cast(bf16x8, u1);                                                          \
    }                                                                                                       \
  } while (0)
#define DMA_A(S)                                                                                            \
  do {                                                                                                      \
    if ((S) == 0) issue_tile8(8);                                                                           \
    if ((S) == 1) issue_tile8(10);                                                                          \
    if ((S) == 2) { issue_tile8(12); issue_tile8(13); }                                                     \
    if ((S) == 3) {                                                                                         \
                    \
      const char* wb = (const char*)wo + (size_t)(kvh * 512 + 16 * b + 2 * wid) * 1024;                     \
      glds16(wb, lane * 16, lds0 + 6 * 16384 + 2 * wid * 1024);                                             \
      glds16(wb + 1024, lane * 16, lds0 + 6 * 16384 + (2 * wid + 1) * 1024);                                \
    }                                                                                                       \
  } while (0)
#define DMA_B(S)                                                                                            \
  do {                                                                                                      \
    if ((S) == 0) issue_tile8(9);                                                                           \
    if ((S) == 1) issue_tile8(11);                                                                          \
    if ((S) == 2) { issue_tile8(14); issue_tile8(15); }                                                     \
  } while (0)
#define STEP_WAIT(S)                                                                                        \
  do {                                                                                                      \
    if ((S) == 0) asm volatile("s_waitcnt vmcnt(12)" ::: "memory");                                         \
    if ((S) == 1 || (S) == 2) asm volatile("s_waitcnt vmcnt(4)" ::: "memory");                              \
    if ((S) == 3) asm volatile("s_waitcnt vmcnt(2)" ::: "memory");                                          \
  } while (0)

  char* mlp = smem + 7 * 16384;
  if (u == 0) {
#pragma unroll
    for (int s = 0; s < 4; ++s) {
      wg_barrier();
      STAMP(2 + 2 * s);
      if (s > 0) {
        V_READ(s - 1, 0);
        V_READ(s - 1, 1);
        V_READ(s - 1, 2);
        V_READ(s - 1, 3);
        DMA_A(s);
        if (s != 2) DMA_B(s);
        SBAR();
        PRIO(1);
        PV_MMA(0);
        SBAR();
        K_READ(s, 0);
        SBAR();
        PV_MMA(2);
        SBAR();
        K_READ(s, 1);
      } else {
        K_READ(s, 0);
        K_READ(s, 1);
        DMA_A(s);
        DMA_B(s);
      }
      SBAR();
      QK_MMA();
      SBAR();
      PRIO(0);
      STAMP(3 + 2 * s);
      if (s == 2) DMA_B(s);
      SOFTMAX(s);
      SBAR();
      STEP_WAIT(s);
    }
    l = swap_half_add(l);
    if (h == 0) *reinterpret_cast<f32x2*>(mlp + wid * 256 + r * 8) = f32x2{m, l};
    wg_barrier();
    V_READ(3, 0);
    V_READ(3, 1);
    V_READ(3, 2);
    V_READ(3, 3);
    SBAR();
    PV_MMA(0);
    PV_MMA(2);
    SBAR();
  } else {
#pragma unroll
    for (int s = 0; s < 5; ++s) {
      wg_barrier();
      if (s == 0) {
        if (grp == 0) {
#pragma unroll
          for (int t = NPRE_A; t < 8; ++t) issue_tile(t);
        } else {
#pragma unroll
          for (int t = NPRE_B; t < 8; ++t) issue_tile(t);
        }
      }
      if (s < 4) { DMA_A(s); if (s != 2) DMA_B(s); }
      if (s > 0) {
        V_READ(s - 1, 0);
        V_READ(s - 1, 1);
        SBAR();
        SOFTMAX(s - 1);
        SBAR();
        if (s == 2) DMA_B(s);
        V_READ(s - 1, 2);
        V_READ(s - 1, 3);
        SBAR();
        PRIO(1);
        PV_MMA(0);
        SBAR();
        if (s < 4) K_READ(s, 0);
        SBAR();
        PV_MMA(2);
        SBAR();
        if (s < 4) K_READ(s, 1);
      } else {
        K_READ(s, 0);
        K_READ(s, 1);
      }
      if (s < 4) {
        SBAR();
        QK_MMA();
        SBAR();
        PRIO(0);
        STEP_WAIT(s);
      }
    }
    PRIO(0);
    l = swap_half_add(l);
    if (h == 0) *reinterpret_cast<f32x2*>(mlp + wid * 256 + r * 8) = f32x2{m, l};
  }
  const auto wo_rs = __builtin_amdgcn_make_buffer_rsrc((void*)wo, 0, HQ * DH * DM * 2, 0x00020000);
  const int wo_voff = (kvh * 512 + 8 * q) * 1024 + (hh * 128 + 8 * r16) * 2;
  u32x4 w[QD][8];
  if (u == 0) {
#pragma unroll
    for (int d = 0; d < QD; ++d)
#pragma unroll
      for (int jj = 0; jj < 8; ++jj)
        w[d][jj] = __builtin_amdgcn_raw_buffer_load_b128(wo_rs, wo_voff, (32 * ((d + rot) & 15) + jj) * 1024, 0);
  }
  SBAR();
  wg_barrier();
  STAMP(10);
  {
    char* xb_ = smem + wid * 8192;
#pragma unroll
    for (int c = 0; c < 2; ++c) {
#pragma unroll
      for (int g = 0; g < 4; ++g) {
        f32x4 v;
        if (u == 0) v = f32x4{o[2 + c][4 * g], o[2 + c][4 * g + 1], o[2 + c][4 * g + 2], o[2 + c][4 * g + 3]};
        else v = f32x4{o[c][4 * g], o[c][4 * g + 1], o[c][4 * g + 2], o[c][4 * g + 3]};
        *reinterpret_cast<f32x4*>(xb_ + (c * 4 + g) * 1024 + lane * 16) = v;
      }
    }
    const f32x2 ml2 = *reinterpret_cast<const f32x2*>(mlp + (wid ^ 4) * 256 + r * 8);
    const float mm = fmaxf(m, ml2[0]);
    const float e1 = __builtin_amdgcn_exp2f((m - mm) * CS), e2 = __builtin_amdgcn_exp2f((ml2[0] - mm) * CS);
    const float inv = 1.0f / (l * e1 + ml2[1] * e2);
    const float f1 = e1 * inv, f2 = e2 * inv;
    wg_barrier();
    const char* pb = smem + (wid ^ 4) * 8192;
    char* of = smem + QX_OFF + (r >> 4) * 1024 + (r & 15) * 16 + 8 * h;
    f32x4 pv[8];
#pragma unroll
    for (int c = 0; c < 8; ++c) pv[c] = *reinterpret_cast<const f32x4*>(pb + c * 1024 + lane * 16);
    SBAR();
#pragma unroll
    for (int c = 0; c < 2; ++c) {
      const int dt = 2 * u + c;
#pragma unroll
      for (int g = 0; g < 4; ++g) {
        f32x4 ov;
        if (u == 0) ov = f32x4{o[c][4 * g], o[c][4 * g + 1], o[c][4 * g + 2], o[c][4 * g + 3]};
        else ov = f32x4{o[2 + c][4 * g], o[2 + c][4 * g + 1], o[2 + c][4 * g + 2], o[2 + c][4 * g + 3]};
        const f32x4 p2 = pv[c * 4 + g];
        u32x2 v;
        v[0] = pk2(ov[0] * f1 + p2[0] * f2, ov[1] * f1 + p2[1] * f2);
        v[1] = pk2(ov[2] * f1 + p2[2] * f2, ov[3] * f1 + p2[3] * f2);
        *reinterpret_cast<u32x2*>(of + (hh * 4 + dt) * 2048 + g * 256) = v;
      }
    }
  }
  if (u == 1) {
    asm volatile("s_waitcnt vmcnt(0)" ::: "memory");
    wg_barrier();
    return;
  }
  {
    wg_barrier();
    STAMP(11);
    f32x4 acc[8][2];
#pragma unroll
    for (int i = 0; i < 8; ++i) { acc[i][0] = f32x4{}; acc[i][1] = f32x4{}; }
    const char* of = smem + QX_OFF + lane * 16;
#pragma unroll
    for (int s = 0; s < 16; ++s) {
      SBAR();
      const bf16x8 b0 = *reinterpret_cast<const bf16x8*>(of + ((s + rot) & 15) * 2048);
      const bf16x8 b1 = *reinterpret_cast<const bf16x8*>(of + ((s + rot) & 15) * 2048 + 1024);
      PROJ_STEP(w[s % QD], b0, b1, if (s + QD < 16) {
        _Pragma("unroll") for (int jj = 0; jj < 8; ++jj)
          w[s % QD][jj] = __builtin_amdgcn_raw_buffer_load_b128(wo_rs, wo_voff, (32 * ((s + QD + rot) & 15) + jj) * 1024, 0);
      });
    }
    STAMP(12);
    char* pp = ows + ((size_t)((kvh * NB + b) * 4 + hh)) * 8192 + lane * 16;
#pragma unroll
    for (int T = 0; T < 2; ++T)
#pragma unroll
      for (int g = 0; g < 4; ++g) {
        f32x4 v0 = {acc[0][T][g], acc[1][T][g], acc[2][T][g], acc[3][T][g]};
        f32x4 v1 = {acc[4][T][g], acc[5][T][g], acc[6][T][g], acc[7][T][g]};
        u32x4 pk = {pk2(v0[0], v0[1]), pk2(v0[2], v0[3]), pk2(v1[0], v1[1]), pk2(v1[2], v1[3])};
        *reinterpret_cast<u32x4*>(pp + (T * 4 + g) * 1024) = pk;
      }
    STAMP(13);
  }
}

#define RT 256
__global__ __launch_bounds__(RT) void reduce_kernel(const char* __restrict__ ows, __hip_bfloat16* __restrict__ out) {
  const int gid = blockIdx.x * RT + threadIdx.x;
  const int lane = gid & 63, tg = (gid >> 6) & 7, bc = gid >> 9;
  const char* pp = ows + (size_t)bc * 8192 + tg * 1024 + lane * 16;
  u32x4 v[8];
#pragma unroll
  for (int k = 0; k < 8; ++k) v[k] = *reinterpret_cast<const u32x4*>(pp + (size_t)k * (NB * 4 * 8192));
  f32x4 a = {0.f, 0.f, 0.f, 0.f}, c = {0.f, 0.f, 0.f, 0.f};
#pragma unroll
  for (int k = 0; k < 8; ++k) {
    a[0] += __uint_as_float(v[k][0] << 16); a[1] += __uint_as_float(v[k][0] & 0xffff0000u);
    a[2] += __uint_as_float(v[k][1] << 16); a[3] += __uint_as_float(v[k][1] & 0xffff0000u);
    c[0] += __uint_as_float(v[k][2] << 16); c[1] += __uint_as_float(v[k][2] & 0xffff0000u);
    c[2] += __uint_as_float(v[k][3] << 16); c[3] += __uint_as_float(v[k][3] & 0xffff0000u);
  }
  const int b = bc >> 2, cg = bc & 3, T = tg >> 2, g = tg & 3, r16 = lane & 15, q = lane >> 4;
  u32x4 o = {pk2(a[0], a[1]), pk2(a[2], a[3]), pk2(c[0], c[1]), pk2(c[2], c[3])};
  *reinterpret_cast<u32x4*>(out + (size_t)(b * SQ + 16 * T + r16) * DM + 128 * cg + 32 * q + 8 * g) = o;
}

extern "C" void kernel_launch(void* const* d_in, const int* in_sizes, int n_in, void* d_out, int out_size,
                              void* d_ws, size_t ws_size, hipStream_t stream) {
  const __hip_bfloat16* x = (const __hip_bfloat16*)d_in[0];
  const __hip_bfloat16* kc = (const __hip_bfloat16*)d_in[1];
  const __hip_bfloat16* vc = (const __hip_bfloat16*)d_in[2];
  const __hip_bfloat16* wq = (const __hip_bfloat16*)d_in[3];
  const __hip_bfloat16* wo = (const __hip_bfloat16*)d_in[4];
  (void)hipFuncSetAttribute((const void*)attn_kernel, hipFuncAttributeMaxDynamicSharedMemorySize, LDS_BYTES);
  attn_kernel<<<256, 512, LDS_BYTES, stream>>>(x, kc, vc, wq, wo, (char*)d_ws);
  reduce_kernel<<<65536 / RT, RT, 0, stream>>>((const char*)d_ws, (__hip_bfloat16*)d_out);
}
```

```cpp
#define ST_AUX 16
#include <hip/hip_runtime.h>
#include <hip/hip_bf16.h>

typedef __bf16 bf16x8 __attribute__((ext_vector_type(8)));
typedef __bf16 bf16x2 __attribute__((ext_vector_type(2)));
typedef short s16x4 __attribute__((ext_vector_type(4)));
typedef float f32x2 __attribute__((ext_vector_type(2)));
typedef float f32x4 __attribute__((ext_vector_type(4)));
typedef float f32x16 __attribute__((ext_vector_type(16)));
typedef unsigned u32x2 __attribute__((ext_vector_type(2)));
typedef unsigned u32x4 __attribute__((ext_vector_type(4)));
typedef __attribute__((address_space(3))) s16x4* lds_s16x4_ptr;

#define DI __device__ __forceinline__
#define MFMA32(a, b, c) __builtin_amdgcn_mfma_f32_32x32x16_bf16((a), (b), (c), 0, 0, 0)
#define MFMA16(a, b, c) __builtin_amdgcn_mfma_f32_16x16x32_bf16((a), (b), (c), 0, 0, 0)
#define SBAR() __builtin_amdgcn_sched_barrier(0)
#define STAMP(i)

constexpr int NB = 32, SQ = 32, DM = 512, HQ = 32, HKV = 8, SK = 512, DH = 128;
constexpr int LDS_BYTES = 163840;
constexpr int QX_OFF = 131072;

DI unsigned pk2(float a, float b) {
  f32x2 f = {a, b};
  return __builtin_bit_cast(unsigned, __builtin_convertvector(f, bf16x2));
}
DI float swap_half_max(float v) {
  auto rr = __builtin_amdgcn_permlane32_swap(__float_as_uint(v), __float_as_uint(v), false, false);
  return fmaxf(__uint_as_float(rr[0]), __uint_as_float(rr[1]));
}
DI float swap_half_add(float v) {
  auto rr = __builtin_amdgcn_permlane32_swap(__float_as_uint(v), __float_as_uint(v), false, false);
  return __uint_as_float(rr[0]) + __uint_as_float(rr[1]);
}
DI void wg_barrier() {
  asm volatile("s_waitcnt lgkmcnt(0)" ::: "memory");
  __builtin_amdgcn_s_barrier();
  asm volatile("" ::: "memory");
}
DI void glds16_nt(const void* sbase, unsigned voff, unsigned lds_dst) {
  unsigned keep;
  asm volatile("s_mov_b32 %0, m0\n\ts_mov_b32 m0, %3\n\ts_nop 0\n\tglobal_load_lds_dwordx4 %1, %2 nt\n\ts_mov_b32 m0, %0"
               : "=&s"(keep) : "v"(voff), "s"(sbase), "s"(lds_dst) : "memory");
}
DI void glds16(const void* sbase, unsigned voff, unsigned lds_dst) {
  unsigned keep;
  asm volatile("s_mov_b32 %0, m0\n\ts_mov_b32 m0, %3\n\ts_nop 0\n\tglobal_load_lds_dwordx4 %1, %2\n\ts_mov_b32 m0, %0"
               : "=&s"(keep) : "v"(voff), "s"(sbase), "s"(lds_dst) : "memory");
}
template <int I>
DI bf16x8 tr_frag(const u32x4* w) {
  u32x4 o;
  constexpr unsigned sel = (I & 1) ? 0x07060302u : 0x05040100u;
#pragma unroll
  for (int m = 0; m < 4; ++m) o[m] = __builtin_amdgcn_perm(w[2 * m + 1][I >> 1], w[2 * m][I >> 1], sel);
  return __builtin_bit_cast(bf16x8, o);
}
#define PROJ_STEP(W, B0, B1, LOADS)                           \
  do {                                                        \
    bf16x8 a_[8];                                             \
    a_[0] = tr_frag<0>(W); a_[1] = tr_frag<1>(W); a_[2] = tr_frag<2>(W); a_[3] = tr_frag<3>(W); \
    a_[4] = tr_frag<4>(W); a_[5] = tr_frag<5>(W); a_[6] = tr_frag<6>(W); a_[7] = tr_frag<7>(W); \
    SBAR();                                                   \
    _Pragma("unroll") for (int i_ = 0; i_ < 8; ++i_) {        \
      acc[i_][0] = MFMA16(a_[i_], B0, acc[i_][0]);            \
      acc[i_][1] = MFMA16(a_[i_], B1, acc[i_][1]);            \
    }                                                         \
    LOADS;                                                    \
    _Pragma("unroll") for (int i_ = 0; i_ < 8; ++i_) {        \
      __builtin_amdgcn_sched_group_barrier(0x8, 2, 0);        \
      __builtin_amdgcn_sched_group_barrier(0x20, 1, 0);       \
    }                                                         \
  } while (0)

#define NPRE_A 8
#define NPRE_B 4
#define BW_STEP 12
#define RESC_THR 6.0f
constexpr int QD = 3;

__global__ __launch_bounds__(512, 2) void attn_kernel(const __hip_bfloat16* __restrict__ x,
                                                      const __hip_bfloat16* __restrict__ kc,
                                                      const __hip_bfloat16* __restrict__ vc,
                                                      const __hip_bfloat16* __restrict__ wq,
                                                      const __hip_bfloat16* __restrict__ wo,
                                                      char* __restrict__ ows) {
  extern __shared__ __attribute__((aligned(16))) char smem[];
  const int tid = threadIdx.x;
  const int wid = __builtin_amdgcn_readfirstlane(tid >> 6);
  const int lane = tid & 63;
  const int kvh = blockIdx.x & 7, b = blockIdx.x >> 3;
  const int hh = wid & 3, u = wid >> 2;
  const int r = lane & 31, h = lane >> 5;
  const int r16 = lane & 15, q = lane >> 4;
#define GRP_EXPR (b & 1)
#define ROT_EXPR ((b >> 1) & 15)
  const int grp = GRP_EXPR;
  const int rot = ROT_EXPR;
  STAMP(0);

  const unsigned lds0 = (unsigned)(size_t)smem;
  const size_t kvbase = ((size_t)(b * HKV + kvh)) * SK * DH;
  auto tile_isv = [](int t) { return t >= 14 ? 1 : (t < 2 ? 0 : (((t - 2) >> 1) & 1)); };
  auto tile_idx = [](int t) { return t >= 14 ? t - 8 : (t < 2 ? t : ((((t - 2) >> 1) & 1) ? 2 * ((t - 2) >> 2) + (t & 1) : 2 * ((t - 2) >> 2) + 2 + (t & 1))); };
  auto issue_tile = [&](int t) {
    const int slot = t < 10 ? t : t - 10;
    const int isv = tile_isv(t);
    const char* base = (const char*)((isv ? vc : kc) + kvbase + (size_t)tile_idx(t) * 64 * DH) + hh * 4096;
#pragma unroll
    for (int e = 0; e < 4; ++e) {
      const int chunk = isv ? (r16 ^ (q << 2)) : (r16 ^ (4 * e + q));
      glds16_nt(base + e * 1024, q * 256 + chunk * 16, lds0 + slot * 16384 + (4 * hh + e) * 1024);
    }
  };
  auto issue_tile8 = [&](int t) {
    const int slot = t < 10 ? t : t - 10;
    const int isv = tile_isv(t);
    const char* base = (const char*)((isv ? vc : kc) + kvbase + (size_t)tile_idx(t) * 64 * DH) + wid * 2048;
#pragma unroll
    for (int e = 0; e < 2; ++e) {
      const int chunk = isv ? (r16 ^ (q << 2)) : (r16 ^ (8 * (wid & 1) + 4 * e + q));
      glds16_nt(base + e * 1024, q * 256 + chunk * 16, lds0 + slot * 16384 + (2 * wid + e) * 1024);
    }
  };
  {
    const __hip_bfloat16* xb = x + (size_t)b * SQ * DM;
#pragma unroll
    for (int i = 0; i < 4; ++i) {
      const int row = 4 * wid + i;
      glds16(xb + row * DM, (lane ^ (row & 15)) * 16, lds0 + QX_OFF + row * 1024);
    }
  }

  bf16x8 qf[8];
  if (u == 1) {
    if (grp == 0) {
#pragma unroll
      for (int t = 0; t < NPRE_A; ++t) issue_tile(t);
      asm volatile("s_waitcnt vmcnt(%0)" ::"i"(4 * NPRE_A) : "memory");
    } else {
      asm volatile("s_waitcnt vmcnt(0)" ::: "memory");
    }
    wg_barrier();
    wg_barrier();
    if (grp == 1) {
#pragma unroll
      for (int t = 0; t < NPRE_B; ++t) issue_tile(t);
    }
    wg_barrier();
    wg_barrier();
#pragma unroll
    for (int p = 0; p < 8; ++p)
      qf[p] = *reinterpret_cast<const bf16x8*>(smem + QX_OFF + (hh * 8 + p) * 1024 + lane * 16);
    if (grp == 0) asm volatile("s_waitcnt vmcnt(%0)" ::"i"(4 * NPRE_A - 8) : "memory");
    else asm volatile("s_waitcnt vmcnt(%0)" ::"i"(4 * NPRE_B - 8) : "memory");
  } else {
    const auto wq_rs = __builtin_amdgcn_make_buffer_rsrc((void*)wq, 0, DM * HQ * DH * 2, 0x00020000);
    const int w_voff = (8 * q) * 8192 + (kvh * 512 + hh * 128 + 8 * r16) * 2;
    u32x4 w[QD][8];
#define PRE_W 1
#pragma unroll
    for (int d = 0; d < PRE_W; ++d) {
      const int sr = (d + rot) & 15;
#pragma unroll
      for (int jj = 0; jj < 8; ++jj)
        w[d][jj] = __builtin_amdgcn_raw_buffer_load_b128(wq_rs, w_voff, (32 * sr + jj) * 8192, 0);
    }
    asm volatile("s_waitcnt vmcnt(%0)" ::"i"(8 * PRE_W) : "memory");
    wg_barrier();
#pragma unroll
    for (int d = PRE_W; d < QD; ++d) {
      const int sr = (d + rot) & 15;
#pragma unroll
      for (int jj = 0; jj < 8; ++jj)
        w[d][jj] = __builtin_amdgcn_raw_buffer_load_b128(wq_rs, w_voff, (32 * sr + jj) * 8192, 0);
    }
    STAMP(14);
    f32x4 acc[8][2];
#pragma unroll
    for (int i = 0; i < 8; ++i) { acc[i][0] = f32x4{}; acc[i][1] = f32x4{}; }
    const char* xs = smem + QX_OFF + r16 * 1024;
#pragma unroll
    for (int s = 0; s < 16; ++s) {
      SBAR();
      const int xo = ((4 * ((s + rot) & 15) + q) ^ r16) << 4;
      const bf16x8 b0 = *reinterpret_cast<const bf16x8*>(xs + xo);
      const bf16x8 b1 = *reinterpret_cast<const bf16x8*>(xs + xo + 16384);
      const int sr = (s + QD + rot) & 15;
      PROJ_STEP(w[s % QD], b0, b1, if (s + QD < 16) {
        _Pragma("unroll") for (int jj = 0; jj < 8; ++jj)
          w[s % QD][jj] = __builtin_amdgcn_raw_buffer_load_b128(wq_rs, w_voff, (32 * sr + jj) * 8192, 0);
      });
      if (s == BW_STEP) { SBAR(); __builtin_amdgcn_s_barrier(); }
    }
#pragma unroll
    for (int g = 0; g < 4; ++g) {
      u32x4 f0, f1;
#pragma unroll
      for (int e = 0; e < 4; ++e) {
        const unsigned a0 = pk2(acc[2 * e][0][g], acc[2 * e + 1][0][g]);
        const unsigned a1 = pk2(acc[2 * e][1][g], acc[2 * e + 1][1][g]);
        auto rr = __builtin_amdgcn_permlane16_swap(a0, a1, false, false);
        f0[e] = rr[0];
        f1[e] = rr[1];
      }
      qf[g] = __builtin_bit_cast(bf16x8, f0);
      qf[4 + g] = __builtin_bit_cast(bf16x8, f1);
    }
    wg_barrier();
#pragma unroll
    for (int p = 0; p < 8; ++p)
      *reinterpret_cast<bf16x8*>(smem + QX_OFF + (hh * 8 + p) * 1024 + lane * 16) = qf[p];
    STAMP(1);
    wg_barrier();
  }

  constexpr float CS = 0.088388347648318447f * 1.4426950408889634f;
  float m = -__builtin_inff(), l = 0.f;
  f32x16 o[4];
#pragma unroll
  for (int i = 0; i < 4; ++i) o[i] = f32x16{};
  const int ky = (r & 15) ^ (8 * h);
  const int koff = r * 256;
  const int vq = (lane >> 2) & 3, vpp = lane & 3;
  const int voff = (4 * h + vq) * 256 + (2 * (q & 1) + (vpp >> 1)) * 16 + 8 * (vpp & 1);
  bf16x8 fb[16];
  bf16x8 pf[4];
  f32x16 s0, s1;
#define SLOT(T) (smem + (((T) < 10 ? (T) : (T) - 10) + u) * 16384)
#define KT(S) ((S) == 0 ? 0 : 4 * (S) - 2)
#define VT(S) ((S) == 3 ? 14 : 4 * (S) + 4)
#define K_READ(S, H)                                                                                        \
  do {                                                                                                      \
    const char* Ks = SLOT(KT(S));                                                                           \
    _Pragma("unroll") for (int p = 4 * (H); p < 4 * (H) + 4; ++p) {                                         \
      const char* a = Ks + koff + ((p ^ ky) << 4);                                                          \
      fb[2 * p] = *reinterpret_cast<const bf16x8*>(a);                                                      \
      fb[2 * p + 1] = *reinterpret_cast<const bf16x8*>(a + 8192);                                           \
    }                                                                                                       \
  } while (0)
#define QK_MMA()                                                                                            \
  do {                                                                                                      \
    s0 = f32x16{}; s1 = f32x16{};                                                                           \
    _Pragma("unroll") for (int p = 0; p < 8; ++p) {                                                         \
      s0 = MFMA32(fb[2 * p], qf[p], s0);                                                                    \
      s1 = MFMA32(fb[2 * p + 1], qf[p], s1);                                                                \
    }                                                                                                       \
  } while (0)
#define V_READ(S, DT)                                                                                       \
  do {                                                                                                      \
    const char* vb = SLOT(VT(S)) + voff + (((DT) ^ vq) << 6);                                         \
    _Pragma("unroll") for (int ks = 0; ks < 4; ++ks) {                                                      \
      const s16x4 lo = __builtin_amdgcn_ds_read_tr16_b64_v4i16((lds_s16x4_ptr)(vb + ks * 4096));            \
      const s16x4 hi = __builtin_amdgcn_ds_read_tr16_b64_v4i16((lds_s16x4_ptr)(vb + ks * 4096 + 2048));     \
      fb[(DT) * 4 + ks] = __builtin_bit_cast(bf16x8, __builtin_shufflevector(lo, hi, 0, 1, 2, 3, 4, 5, 6, 7)); \
    }                                                                                                       \
  } while (0)
#define PRIO(x)
#define PV_MMA(D0)                                                                                          \
  do {                                                                                                      \
    _Pragma("unroll") for (int ks = 0; ks < 4; ++ks)                                                        \
      _Pragma("unroll") for (int dt = (D0); dt < (D0) + 2; ++dt) o[dt] = MFMA32(fb[dt * 4 + ks], pf[ks], o[dt]); \
  } while (0)
#define SOFTMAX(S)                                                                                          \
  do {                                                                                                      \
    float tmax = s0[0];                                                                                     \
    _Pragma("unroll") for (int i = 1; i < 16; ++i) tmax = fmaxf(tmax, s0[i]);                               \
    _Pragma("unroll") for (int i = 0; i < 16; ++i) tmax = fmaxf(tmax, s1[i]);                               \
    tmax = swap_half_max(tmax);                                                                             \
      \
      \
    if ((S) == 0) {                                                                                         \
      m = tmax;                                                                                             \
    } else if (__builtin_amdgcn_ballot_w64((tmax - m) * CS > RESC_THR) != 0) {                              \
      const float mnew = fmaxf(m, tmax);                                                                    \
      const float alpha = __builtin_amdgcn_exp2f((m - mnew) * CS);                                          \
      m = mnew;                                                                                             \
      l *= alpha;                                                                                           \
      _Pragma("unroll") for (int dt = 0; dt < 4; ++dt)                                                      \
        _Pragma("unroll") for (int i = 0; i < 16; ++i) o[dt][i] *= alpha;                                   \
    }                                                                                                       \
    const float mc = m * CS;                                                                                \
    float ps = 0.f;                                                                                         \
    _Pragma("unroll") for (int i = 0; i < 16; ++i) { s0[i] = __builtin_amdgcn_exp2f(fmaf(s0[i], CS, -mc)); ps += s0[i]; } \
    _Pragma("unroll") for (int i = 0; i < 16; ++i) { s1[i] = __builtin_amdgcn_exp2f(fmaf(s1[i], CS, -mc)); ps += s1[i]; } \
    l += ps;                                                                                                \
    asm volatile("" : "+v"(l));                                                                             \
    _Pragma("unroll") for (int ss = 0; ss < 2; ++ss) {                                                      \
      u32x4 u0, u1;                                                                                         \
      _Pragma("unroll") for (int e = 0; e < 4; ++e) {                                                       \
        u0[e] = pk2(s0[8 * ss + 2 * e], s0[8 * ss + 2 * e + 1]);                                            \
        u1[e] = pk2(s1[8 * ss + 2 * e], s1[8 * ss + 2 * e + 1]);                                            \
      }                                                                                                     \
      pf[ss] = __builtin_bit_cast(bf16x8, u0);                                                              \
      pf[2 + ss] = __builtin_bit_cast(bf16x8, u1);                                                          \
    }                                                                                                       \
  } while (0)
#define DMA_A(S)                                                                                            \
  do {                                                                                                      \
    if ((S) == 0) issue_tile8(8);                                                                           \
    if ((S) == 1) issue_tile8(10);                                                                          \
    if ((S) == 2) { issue_tile8(12); issue_tile8(13); }                                                     \
    if ((S) == 3) {                                                                                         \
                    \
      const char* wb = (const char*)wo + (size_t)(kvh * 512 + 16 * b + 2 * wid) * 1024;                     \
      glds16(wb, lane * 16, lds0 + 6 * 16384 + 2 * wid * 1024);                                             \
      glds16(wb + 1024, lane * 16, lds0 + 6 * 16384 + (2 * wid + 1) * 1024);                                \
    }                                                                                                       \
  } while (0)
#define DMA_B(S)                                                                                            \
  do {                                                                                                      \
    if ((S) == 0) issue_tile8(9);                                                                           \
    if ((S) == 1) issue_tile8(11);                                                                          \
    if ((S) == 2) { issue_tile8(14); issue_tile8(15); }                                                     \
  } while (0)
#define STEP_WAIT(S)                                                                                        \
  do {                                                                                                      \
    if ((S) == 0) asm volatile("s_waitcnt vmcnt(12)" ::: "memory");                                         \
    if ((S) == 1 || (S) == 2) asm volatile("s_waitcnt vmcnt(4)" ::: "memory");                              \
    if ((S) == 3) asm volatile("s_waitcnt vmcnt(2)" ::: "memory");                                          \
  } while (0)

  char* mlp = smem + 7 * 16384;
  if (u == 0) {
#pragma unroll
    for (int s = 0; s < 4; ++s) {
      wg_barrier();
      STAMP(2 + 2 * s);
      if (s > 0) {
        V_READ(s - 1, 0);
        V_READ(s - 1, 1);
        V_READ(s - 1, 2);
        V_READ(s - 1, 3);
        DMA_A(s);
        if (s != 2) DMA_B(s);
        SBAR();
        PRIO(1);
        PV_MMA(0);
        SBAR();
        K_READ(s, 0);
        SBAR();
        PV_MMA(2);
        SBAR();
        K_READ(s, 1);
      } else {
        K_READ(s, 0);
        K_READ(s, 1);
        DMA_A(s);
        DMA_B(s);
      }
      SBAR();
      QK_MMA();
      SBAR();
      PRIO(0);
      STAMP(3 + 2 * s);
      if (s == 2) DMA_B(s);
      SOFTMAX(s);
      SBAR();
      STEP_WAIT(s);
    }
    l = swap_half_add(l);
    if (h == 0) *reinterpret_cast<f32x2*>(mlp + wid * 256 + r * 8) = f32x2{m, l};
    wg_barrier();
    V_READ(3, 0);
    V_READ(3, 1);
    V_READ(3, 2);
    V_READ(3, 3);
    SBAR();
    PV_MMA(0);
    PV_MMA(2);
    SBAR();
  } else {
#pragma unroll
    for (int s = 0; s < 5; ++s) {
      wg_barrier();
      if (s == 0) {
        if (grp == 0) {
#pragma unroll
          for (int t = NPRE_A; t < 8; ++t) issue_tile(t);
        } else {
#pragma unroll
          for (int t = NPRE_B; t < 8; ++t) issue_tile(t);
        }
      }
      if (s < 4) { DMA_A(s); if (s != 2) DMA_B(s); }
      if (s > 0) {
        V_READ(s - 1, 0);
        V_READ(s - 1, 1);
        SBAR();
        SOFTMAX(s - 1);
        SBAR();
        if (s == 2) DMA_B(s);
        V_READ(s - 1, 2);
        V_READ(s - 1, 3);
        SBAR();
        PRIO(1);
        PV_MMA(0);
        SBAR();
        if (s < 4) K_READ(s, 0);
        SBAR();
        PV_MMA(2);
        SBAR();
        if (s < 4) K_READ(s, 1);
      } else {
        K_READ(s, 0);
        K_READ(s, 1);
      }
      if (s < 4) {
        SBAR();
        QK_MMA();
        SBAR();
        PRIO(0);
        STEP_WAIT(s);
      }
    }
    PRIO(0);
    l = swap_half_add(l);
    if (h == 0) *reinterpret_cast<f32x2*>(mlp + wid * 256 + r * 8) = f32x2{m, l};
  }
  const auto wo_rs = __builtin_amdgcn_make_buffer_rsrc((void*)wo, 0, HQ * DH * DM * 2, 0x00020000);
  const int wo_voff = (kvh * 512 + 8 * q) * 1024 + (hh * 128 + 8 * r16) * 2;
  u32x4 w[QD][8];
  if (u == 0) {
#pragma unroll
    for (int d = 0; d < QD; ++d)
#pragma unroll
      for (int jj = 0; jj < 8; ++jj)
        w[d][jj] = __builtin_amdgcn_raw_buffer_load_b128(wo_rs, wo_voff, (32 * ((d + rot) & 15) + jj) * 1024, 0);
  }
  SBAR();
  wg_barrier();
  STAMP(10);
  {
    char* xb_ = smem + wid * 8192;
#pragma unroll
    for (int c = 0; c < 2; ++c) {
#pragma unroll
      for (int g = 0; g < 4; ++g) {
        f32x4 v;
        if (u == 0) v = f32x4{o[2 + c][4 * g], o[2 + c][4 * g + 1], o[2 + c][4 * g + 2], o[2 + c][4 * g + 3]};
        else v = f32x4{o[c][4 * g], o[c][4 * g + 1], o[c][4 * g + 2], o[c][4 * g + 3]};
        *reinterpret_cast<f32x4*>(xb_ + (c * 4 + g) * 1024 + lane * 16) = v;
      }
    }
    const f32x2 ml2 = *reinterpret_cast<const f32x2*>(mlp + (wid ^ 4) * 256 + r * 8);
    const float mm = fmaxf(m, ml2[0]);
    const float e1 = __builtin_amdgcn_exp2f((m - mm) * CS), e2 = __builtin_amdgcn_exp2f((ml2[0] - mm) * CS);
    const float inv = 1.0f / (l * e1 + ml2[1] * e2);
    const float f1 = e1 * inv, f2 = e2 * inv;
    wg_barrier();
    const char* pb = smem + (wid ^ 4) * 8192;
    char* of = smem + QX_OFF + (r >> 4) * 1024 + (r & 15) * 16 + 8 * h;
    f32x4 pv[8];
#pragma unroll
    for (int c = 0; c < 8; ++c) pv[c] = *reinterpret_cast<const f32x4*>(pb + c * 1024 + lane * 16);
    SBAR();
#pragma unroll
    for (int c = 0; c < 2; ++c) {
      const int dt = 2 * u + c;
#pragma unroll
      for (int g = 0; g < 4; ++g) {
        f32x4 ov;
        if (u == 0) ov = f32x4{o[c][4 * g], o[c][4 * g + 1], o[c][4 * g + 2], o[c][4 * g + 3]};
        else ov = f32x4{o[2 + c][4 * g], o[2 + c][4 * g + 1], o[2 + c][4 * g + 2], o[2 + c][4 * g + 3]};
        const f32x4 p2 = pv[c * 4 + g];
        u32x2 v;
        v[0] = pk2(ov[0] * f1 + p2[0] * f2, ov[1] * f1 + p2[1] * f2);
        v[1] = pk2(ov[2] * f1 + p2[2] * f2, ov[3] * f1 + p2[3] * f2);
        *reinterpret_cast<u32x2*>(of + (hh * 4 + dt) * 2048 + g * 256) = v;
      }
    }
  }
  if (u == 1) {
    asm volatile("s_waitcnt vmcnt(0)" ::: "memory");
    wg_barrier();
    return;
  }
  {
    wg_barrier();
    STAMP(11);
    f32x4 acc[8][2];
#pragma unroll
    for (int i = 0; i < 8; ++i) { acc[i][0] = f32x4{}; acc[i][1] = f32x4{}; }
    const char* of = smem + QX_OFF + lane * 16;
#pragma unroll
    for (int s = 0; s < 16; ++s) {
      SBAR();
      const bf16x8 b0 = *reinterpret_cast<const bf16x8*>(of + ((s + rot) & 15) * 2048);
      const bf16x8 b1 = *reinterpret_cast<const bf16x8*>(of + ((s + rot) & 15) * 2048 + 1024);
      PROJ_STEP(w[s % QD], b0, b1, if (s + QD < 16) {
        _Pragma("unroll") for (int jj = 0; jj < 8; ++jj)
          w[s % QD][jj] = __builtin_amdgcn_raw_buffer_load_b128(wo_rs, wo_voff, (32 * ((s + QD + rot) & 15) + jj) * 1024, 0);
      });
    }
    STAMP(12);
    char* pp = ows + ((size_t)((kvh * NB + b) * 4 + hh)) * 8192 + lane * 16;
    const auto p_rs = __builtin_amdgcn_make_buffer_rsrc((void*)ows, 0, 8 << 20, 0x00020000);
    const int p_voff = ((kvh * NB + b) * 4 + hh) * 8192 + lane * 16;
#pragma unroll
    for (int T = 0; T < 2; ++T)
#pragma unroll
      for (int g = 0; g < 4; ++g) {
        f32x4 v0 = {acc[0][T][g], acc[1][T][g], acc[2][T][g], acc[3][T][g]};
        f32x4 v1 = {acc[4][T][g], acc[5][T][g], acc[6][T][g], acc[7][T][g]};
        u32x4 pk = {pk2(v0[0], v0[1]), pk2(v0[2], v0[3]), pk2(v1[0], v1[1]), pk2(v1[2], v1[3])};
        __builtin_amdgcn_raw_buffer_store_b128(pk, p_rs, p_voff, (T * 4 + g) * 1024, ST_AUX);
      }
    STAMP(13);
  }
}

#define RT 256
__global__ __launch_bounds__(RT) void reduce_kernel(const char* __restrict__ ows, __hip_bfloat16* __restrict__ out) {
  const int gid = blockIdx.x * RT + threadIdx.x;
  const int lane = gid & 63, tg = (gid >> 6) & 7, bc = gid >> 9;
  const char* pp = ows + (size_t)bc * 8192 + tg * 1024 + lane * 16;
  u32x4 v[8];
#pragma unroll
  for (int k = 0; k < 8; ++k) v[k] = *reinterpret_cast<const u32x4*>(pp + (size_t)k * (NB * 4 * 8192));
  f32x4 a = {0.f, 0.f, 0.f, 0.f}, c = {0.f, 0.f, 0.f, 0.f};
#pragma unroll
  for (int k = 0; k < 8; ++k) {
    a[0] += __uint_as_float(v[k][0] << 16); a[1] += __uint_as_float(v[k][0] & 0xffff0000u);
    a[2] += __uint_as_float(v[k][1] << 16); a[3] += __uint_as_float(v[k][1] & 0xffff0000u);
    c[0] += __uint_as_float(v[k][2] << 16); c[1] += __uint_as_float(v[k][2] & 0xffff0000u);
    c[2] += __uint_as_float(v[k][3] << 16); c[3] += __uint_as_float(v[k][3] & 0xffff0000u);
  }
  const int b = bc >> 2, cg = bc & 3, T = tg >> 2, g = tg & 3, r16 = lane & 15, q = lane >> 4;
  u32x4 o = {pk2(a[0], a[1]), pk2(a[2], a[3]), pk2(c[0], c[1]), pk2(c[2], c[3])};
  *reinterpret_cast<u32x4*>(out + (size_t)(b * SQ + 16 * T + r16) * DM + 128 * cg + 32 * q + 8 * g) = o;
}

extern "C" void kernel_launch(void* const* d_in, const int* in_sizes, int n_in, void* d_out, int out_size,
                              void* d_ws, size_t ws_size, hipStream_t stream) {
  const __hip_bfloat16* x = (const __hip_bfloat16*)d_in[0];
  const __hip_bfloat16* kc = (const __hip_bfloat16*)d_in[1];
  const __hip_bfloat16* vc = (const __hip_bfloat16*)d_in[2];
  const __hip_bfloat16* wq = (const __hip_bfloat16*)d_in[3];
  const __hip_bfloat16* wo = (const __hip_bfloat16*)d_in[4];
  (void)hipFuncSetAttribute((const void*)attn_kernel, hipFuncAttributeMaxDynamicSharedMemorySize, LDS_BYTES);
  attn_kernel<<<256, 512, LDS_BYTES, stream>>>(x, kc, vc, wq, wo, (char*)d_ws);
  reduce_kernel<<<65536 / RT, RT, 0, stream>>>((const char*)d_ws, (__hip_bfloat16*)d_out);
}
```

```cpp
#define ROLL
#include <hip/hip_runtime.h>
#include <hip/hip_bf16.h>

typedef __bf16 bf16x8 __attribute__((ext_vector_type(8)));
typedef __bf16 bf16x2 __attribute__((ext_vector_type(2)));
typedef short s16x4 __attribute__((ext_vector_type(4)));
typedef float f32x2 __attribute__((ext_vector_type(2)));
typedef float f32x4 __attribute__((ext_vector_type(4)));
typedef float f32x16 __attribute__((ext_vector_type(16)));
typedef unsigned u32x2 __attribute__((ext_vector_type(2)));
typedef unsigned u32x4 __attribute__((ext_vector_type(4)));
typedef __attribute__((address_space(3))) s16x4* lds_s16x4_ptr;

#define DI __device__ __forceinline__
#define MFMA32(a, b, c) __builtin_amdgcn_mfma_f32_32x32x16_bf16((a), (b), (c), 0, 0, 0)
#define MFMA16(a, b, c) __builtin_amdgcn_mfma_f32_16x16x32_bf16((a), (b), (c), 0, 0, 0)
#define SBAR() __builtin_amdgcn_sched_barrier(0)
#define STAMP(i)

constexpr int NB = 32, SQ = 32, DM = 512, HQ = 32, HKV = 8, SK = 512, DH = 128;
constexpr int LDS_BYTES = 163840;
constexpr int QX_OFF = 131072;

DI unsigned pk2(float a, float b) {
  f32x2 f = {a, b};
  return __builtin_bit_cast(unsigned, __builtin_convertvector(f, bf16x2));
}
DI float swap_half_max(float v) {
  auto rr = __builtin_amdgcn_permlane32_swap(__float_as_uint(v), __float_as_uint(v), false, false);
  return fmaxf(__uint_as_float(rr[0]), __uint_as_float(rr[1]));
}
DI float swap_half_add(float v) {
  auto rr = __builtin_amdgcn_permlane32_swap(__float_as_uint(v), __float_as_uint(v), false, false);
  return __uint_as_float(rr[0]) + __uint_as_float(rr[1]);
}
DI void wg_barrier() {
  asm volatile("s_waitcnt lgkmcnt(0)" ::: "memory");
  __builtin_amdgcn_s_barrier();
  asm volatile("" ::: "memory");
}
DI void glds16_nt(const void* sbase, unsigned voff, unsigned lds_dst) {
  unsigned keep;
  asm volatile("s_mov_b32 %0, m0\n\ts_mov_b32 m0, %3\n\ts_nop 0\n\tglobal_load_lds_dwordx4 %1, %2 nt\n\ts_mov_b32 m0, %0"
               : "=&s"(keep) : "v"(voff), "s"(sbase), "s"(lds_dst) : "memory");
}
DI void glds16(const void* sbase, unsigned voff, unsigned lds_dst) {
  unsigned keep;
  asm volatile("s_mov_b32 %0, m0\n\ts_mov_b32 m0, %3\n\ts_nop 0\n\tglobal_load_lds_dwordx4 %1, %2\n\ts_mov_b32 m0, %0"
               : "=&s"(keep) : "v"(voff), "s"(sbase), "s"(lds_dst) : "memory");
}
template <int I>
DI bf16x8 tr_frag(const u32x4* w) {
  u32x4 o;
  constexpr unsigned sel = (I & 1) ? 0x07060302u : 0x05040100u;
#pragma unroll
  for (int m = 0; m < 4; ++m) o[m] = __builtin_amdgcn_perm(w[2 * m + 1][I >> 1], w[2 * m][I >> 1], sel);
  return __builtin_bit_cast(bf16x8, o);
}
#define PROJ_STEP(W, B0, B1, LOADS)                           \
  do {                                                        \
    bf16x8 a_[8];                                             \
    a_[0] = tr_frag<0>(W); a_[1] = tr_frag<1>(W); a_[2] = tr_frag<2>(W); a_[3] = tr_frag<3>(W); \
    a_[4] = tr_frag<4>(W); a_[5] = tr_frag<5>(W); a_[6] = tr_frag<6>(W); a_[7] = tr_frag<7>(W); \
    SBAR();                                                   \
    _Pragma("unroll") for (int i_ = 0; i_ < 8; ++i_) {        \
      acc[i_][0] = MFMA16(a_[i_], B0, acc[i_][0]);            \
      acc[i_][1] = MFMA16(a_[i_], B1, acc[i_][1]);            \
    }                                                         \
    LOADS;                                                    \
    _Pragma("unroll") for (int i_ = 0; i_ < 8; ++i_) {        \
      __builtin_amdgcn_sched_group_barrier(0x8, 2, 0);        \
      __builtin_amdgcn_sched_group_barrier(0x20, 1, 0);       \
    }                                                         \
  } while (0)

#define NPRE_A 8
#define NPRE_B 4
#define BW_STEP 12
#define RESC_THR 6.0f
#define ST_AUX 16
#define QDEPTH 2
constexpr int QD = QDEPTH;

__global__ __launch_bounds__(512, 2) void attn_kernel(const __hip_bfloat16* __restrict__ x,
                                                      const __hip_bfloat16* __restrict__ kc,
                                                      const __hip_bfloat16* __restrict__ vc,
                                                      const __hip_bfloat16* __restrict__ wq,
                                                      const __hip_bfloat16* __restrict__ wo,
                                                      char* __restrict__ ows) {
  extern __shared__ __attribute__((aligned(16))) char smem[];
  const int tid = threadIdx.x;
  const int wid = __builtin_amdgcn_readfirstlane(tid >> 6);
  const int lane = tid & 63;
  const int kvh = blockIdx.x & 7, b = blockIdx.x >> 3;
  const int hh = wid & 3, u = wid >> 2;
  const int r = lane & 31, h = lane >> 5;
  const int r16 = lane & 15, q = lane >> 4;
#define GRP_EXPR (b & 1)
#define ROT_EXPR ((b >> 1) & 15)
  const int grp = GRP_EXPR;
  const int rot = ROT_EXPR;
  STAMP(0);

  const unsigned lds0 = (unsigned)(size_t)smem;
  const size_t kvbase = ((size_t)(b * HKV + kvh)) * SK * DH;
  auto tile_isv = [](int t) { return t >= 14 ? 1 : (t < 2 ? 0 : (((t - 2) >> 1) & 1)); };
  auto tile_idx = [](int t) { return t >= 14 ? t - 8 : (t < 2 ? t : ((((t - 2) >> 1) & 1) ? 2 * ((t - 2) >> 2) + (t & 1) : 2 * ((t - 2) >> 2) + 2 + (t & 1))); };
  auto issue_tile = [&](int t) {
    const int slot = t < 10 ? t : t - 10;
    const int isv = tile_isv(t);
    const char* base = (const char*)((isv ? vc : kc) + kvbase + (size_t)tile_idx(t) * 64 * DH) + hh * 4096;
#pragma unroll
    for (int e = 0; e < 4; ++e) {
      const int chunk = isv ? (r16 ^ (q << 2)) : (r16 ^ (4 * e + q));
      glds16_nt(base + e * 1024, q * 256 + chunk * 16, lds0 + slot * 16384 + (4 * hh + e) * 1024);
    }
  };
  auto issue_tile8 = [&](int t) {
    const int slot = t < 10 ? t : t - 10;
    const int isv = tile_isv(t);
    const char* base = (const char*)((isv ? vc : kc) + kvbase + (size_t)tile_idx(t) * 64 * DH) + wid * 2048;
#pragma unroll
    for (int e = 0; e < 2; ++e) {
      const int chunk = isv ? (r16 ^ (q << 2)) : (r16 ^ (8 * (wid & 1) + 4 * e + q));
      glds16_nt(base + e * 1024, q * 256 + chunk * 16, lds0 + slot * 16384 + (2 * wid + e) * 1024);
    }
  };
  {
    const __hip_bfloat16* xb = x + (size_t)b * SQ * DM;
#pragma unroll
    for (int i = 0; i < 4; ++i) {
      const int row = 4 * wid + i;
      glds16(xb + row * DM, (lane ^ (row & 15)) * 16, lds0 + QX_OFF + row * 1024);
    }
  }

  bf16x8 qf[8];
  if (u == 1) {
    if (grp == 0) {
#pragma unroll
      for (int t = 0; t < NPRE_A; ++t) issue_tile(t);
      asm volatile("s_waitcnt vmcnt(%0)" ::"i"(4 * NPRE_A) : "memory");
    } else {
      asm volatile("s_waitcnt vmcnt(0)" ::: "memory");
    }
    wg_barrier();
    wg_barrier();
    if (grp == 1) {
#pragma unroll
      for (int t = 0; t < NPRE_B; ++t) issue_tile(t);
    }
    wg_barrier();
    wg_barrier();
#pragma unroll
    for (int p = 0; p < 8; ++p)
      qf[p] = *reinterpret_cast<const bf16x8*>(smem + QX_OFF + (hh * 8 + p) * 1024 + lane * 16);
    if (grp == 0) asm volatile("s_waitcnt vmcnt(%0)" ::"i"(4 * NPRE_A - 8) : "memory");
    else asm volatile("s_waitcnt vmcnt(%0)" ::"i"(4 * NPRE_B - 8) : "memory");
  } else {
    const auto wq_rs = __builtin_amdgcn_make_buffer_rsrc((void*)wq, 0, DM * HQ * DH * 2, 0x00020000);
    const int w_voff = (8 * q) * 8192 + (kvh * 512 + hh * 128 + 8 * r16) * 2;
    u32x4 w[QD][8];
#define PRE_W 1
#pragma unroll
    for (int d = 0; d < PRE_W; ++d) {
      const int sr = (d + rot) & 15;
#pragma unroll
      for (int jj = 0; jj < 8; ++jj)
        w[d][jj] = __builtin_amdgcn_raw_buffer_load_b128(wq_rs, w_voff, (32 * sr + jj) * 8192, 0);
    }
    asm volatile("s_waitcnt vmcnt(%0)" ::"i"(8 * PRE_W) : "memory");
    wg_barrier();
#pragma unroll
    for (int d = PRE_W; d < QD; ++d) {
      const int sr = (d + rot) & 15;
#pragma unroll
      for (int jj = 0; jj < 8; ++jj)
        w[d][jj] = __builtin_amdgcn_raw_buffer_load_b128(wq_rs, w_voff, (32 * sr + jj) * 8192, 0);
    }
    STAMP(14);
    f32x4 acc[8][2];
#pragma unroll
    for (int i = 0; i < 8; ++i) { acc[i][0] = f32x4{}; acc[i][1] = f32x4{}; }
    const char* xs = smem + QX_OFF + r16 * 1024;
#define Q_STEP(S, BUF, LD)                                                                              \
  do {                                                                                                  \
    SBAR();                                                                                             \
    const int xo = ((4 * (((S) + rot) & 15) + q) ^ r16) << 4;                                           \
    const bf16x8 b0 = *reinterpret_cast<const bf16x8*>(xs + xo);                                        \
    const bf16x8 b1 = *reinterpret_cast<const bf16x8*>(xs + xo + 16384);                                \
    const int sr = ((S) + 2 + rot) & 15;                                                                \
    PROJ_STEP(w[BUF], b0, b1, if (LD) {                                                                 \
      _Pragma("unroll") for (int jj = 0; jj < 8; ++jj)                                                  \
        w[BUF][jj] = __builtin_amdgcn_raw_buffer_load_b128(wq_rs, w_voff, (32 * sr + jj) * 8192, 0);    \
    });                                                                                                 \
  } while (0)
#pragma unroll 1
    for (int it = 0; it < 7; ++it) {
      Q_STEP(2 * it, 0, true);
      Q_STEP(2 * it + 1, 1, true);
    }
    SBAR();
    __builtin_amdgcn_s_barrier();
    Q_STEP(14, 0, false);
    Q_STEP(15, 1, false);
#pragma unroll
    for (int g = 0; g < 4; ++g) {
      u32x4 f0, f1;
#pragma unroll
      for (int e = 0; e < 4; ++e) {
        const unsigned a0 = pk2(acc[2 * e][0][g], acc[2 * e + 1][0][g]);
        const unsigned a1 = pk2(acc[2 * e][1][g], acc[2 * e + 1][1][g]);
        auto rr = __builtin_amdgcn_permlane16_swap(a0, a1, false, false);
        f0[e] = rr[0];
        f1[e] = rr[1];
      }
      qf[g] = __builtin_bit_cast(bf16x8, f0);
      qf[4 + g] = __builtin_bit_cast(bf16x8, f1);
    }
    wg_barrier();
#pragma unroll
    for (int p = 0; p < 8; ++p)
      *reinterpret_cast<bf16x8*>(smem + QX_OFF + (hh * 8 + p) * 1024 + lane * 16) = qf[p];
    STAMP(1);
    wg_barrier();
  }

  constexpr float CS = 0.088388347648318447f * 1.4426950408889634f;
  float m = -__builtin_inff(), l = 0.f;
  f32x16 o[4];
#pragma unroll
  for (int i = 0; i < 4; ++i) o[i] = f32x16{};
  const int ky = (r & 15) ^ (8 * h);
  const int koff = r * 256;
  const int vq = (lane >> 2) & 3, vpp = lane & 3;
  const int voff = (4 * h + vq) * 256 + (2 * (q & 1) + (vpp >> 1)) * 16 + 8 * (vpp & 1);
  bf16x8 fb[16];
  bf16x8 pf[4];
  f32x16 s0, s1;
#define SLOT(T) (smem + (((T) < 10 ? (T) : (T) - 10) + u) * 16384)
#define KT(S) ((S) == 0 ? 0 : 4 * (S) - 2)
#define VT(S) ((S) == 3 ? 14 : 4 * (S) + 4)
#define K_READ(S, H)                                                                                        \
  do {                                                                                                      \
    const char* Ks = SLOT(KT(S));                                                                           \
    _Pragma("unroll") for (int p = 4 * (H); p < 4 * (H) + 4; ++p) {                                         \
      const char* a = Ks + koff + ((p ^ ky) << 4);                                                          \
      fb[2 * p] = *reinterpret_cast<const bf16x8*>(a);                                                      \
      fb[2 * p + 1] = *reinterpret_cast<const bf16x8*>(a + 8192);                                           \
    }                                                                                                       \
  } while (0)
#define QK_MMA()                                                                                            \
  do {                                                                                                      \
    s0 = f32x16{}; s1 = f32x16{};                                                                           \
    _Pragma("unroll") for (int p = 0; p < 8; ++p) {                                                         \
      s0 = MFMA32(fb[2 * p], qf[p], s0);                                                                    \
      s1 = MFMA32(fb[2 * p + 1], qf[p], s1);                                                                \
    }                                                                                                       \
  } while (0)
#define V_READ(S, DT)                                                                                       \
  do {                                                                                                      \
    const char* vb = SLOT(VT(S)) + voff + (((DT) ^ vq) << 6);                                         \
    _Pragma("unroll") for (int ks = 0; ks < 4; ++ks) {                                                      \
      const s16x4 lo = __builtin_amdgcn_ds_read_tr16_b64_v4i16((lds_s16x4_ptr)(vb + ks * 4096));            \
      const s16x4 hi = __builtin_amdgcn_ds_read_tr16_b64_v4i16((lds_s16x4_ptr)(vb + ks * 4096 + 2048));     \
      fb[(DT) * 4 + ks] = __builtin_bit_cast(bf16x8, __builtin_shufflevector(lo, hi, 0, 1, 2, 3, 4, 5, 6, 7)); \
    }                                                                                                       \
  } while (0)
#define PRIO(x)
#define PV_MMA(D0)                                                                                          \
  do {                                                                                                      \
    _Pragma("unroll") for (int ks = 0; ks < 4; ++ks)                                                        \
      _Pragma("unroll") for (int dt = (D0); dt < (D0) + 2; ++dt) o[dt] = MFMA32(fb[dt * 4 + ks], pf[ks], o[dt]); \
  } while (0)
#define SOFTMAX(S)                                                                                          \
  do {                                                                                                      \
    float tmax = s0[0];                                                                                     \
    _Pragma("unroll") for (int i = 1; i < 16; ++i) tmax = fmaxf(tmax, s0[i]);                               \
    _Pragma("unroll") for (int i = 0; i < 16; ++i) tmax = fmaxf(tmax, s1[i]);                               \
    tmax = swap_half_max(tmax);                                                                             \
      \
      \
    if ((S) == 0) {                                                                                         \
      m = tmax;                                                                                             \
    } else if (__builtin_amdgcn_ballot_w64((tmax - m) * CS > RESC_THR) != 0) {                              \
      const float mnew = fmaxf(m, tmax);                                                                    \
      const float alpha = __builtin_amdgcn_exp2f((m - mnew) * CS);                                          \
      m = mnew;                                                                                             \
      l *= alpha;                                                                                           \
      _Pragma("unroll") for (int dt = 0; dt < 4; ++dt)                                                      \
        _Pragma("unroll") for (int i = 0; i < 16; ++i) o[dt][i] *= alpha;                                   \
    }                                                                                                       \
    const float mc = m * CS;                                                                                \
    float ps = 0.f;                                                                                         \
    _Pragma("unroll") for (int i = 0; i < 16; ++i) { s0[i] = __builtin_amdgcn_exp2f(fmaf(s0[i], CS, -mc)); ps += s0[i]; } \
    _Pragma("unroll") for (int i = 0; i < 16; ++i) { s1[i] = __builtin_amdgcn_exp2f(fmaf(s1[i], CS, -mc)); ps += s1[i]; } \
    l += ps;                                                                                                \
    asm volatile("" : "+v"(l));                                                                             \
    _Pragma("unroll") for (int ss = 0; ss < 2; ++ss) {                                                      \
      u32x4 u0, u1;                                                                                         \
      _Pragma("unroll") for (int e = 0; e < 4; ++e) {                                                       \
        u0[e] = pk2(s0[8 * ss + 2 * e], s0[8 * ss + 2 * e + 1]);                                            \
        u1[e] = pk2(s1[8 * ss + 2 * e], s1[8 * ss + 2 * e + 1]);                                            \
      }                                                                                                     \
      pf[ss] = __builtin_bit_cast(bf16x8, u0);                                                              \
      pf[2 + ss] = __builtin_bit_cast(bf16x8, u1);                                                          \
    }                                                                                                       \
  } while (0)
#define DMA_A(S)                                                                                            \
  do {                                                                                                      \
    if ((S) == 0) issue_tile8(8);                                                                           \
    if ((S) == 1) issue_tile8(10);                                                                          \
    if ((S) == 2) { issue_tile8(12); issue_tile8(13); }                                                     \
    if ((S) == 3) {                                                                                         \
                    \
      const char* wb = (const char*)wo + (size_t)(kvh * 512 + 16 * b + 2 * wid) * 1024;                     \
      glds16(wb, lane * 16, lds0 + 6 * 16384 + 2 * wid * 1024);                                             \
      glds16(wb + 1024, lane * 16, lds0 + 6 * 16384 + (2 * wid + 1) * 1024);                                \
    }                                                                                                       \
  } while (0)
#define DMA_B(S)                                                                                            \
  do {                                                                                                      \
    if ((S) == 0) issue_tile8(9);                                                                           \
    if ((S) == 1) issue_tile8(11);                                                                          \
    if ((S) == 2) { issue_tile8(14); issue_tile8(15); }                                                     \
  } while (0)
#define STEP_WAIT(S)                                                                                        \
  do {                                                                                                      \
    if ((S) == 0) asm volatile("s_waitcnt vmcnt(12)" ::: "memory");                                         \
    if ((S) == 1 || (S) == 2) asm volatile("s_waitcnt vmcnt(4)" ::: "memory");                              \
    if ((S) == 3) asm volatile("s_waitcnt vmcnt(2)" ::: "memory");                                          \
  } while (0)

  char* mlp = smem + 7 * 16384;
  if (u == 0) {
#pragma unroll
    for (int s = 0; s < 4; ++s) {
      wg_barrier();
      STAMP(2 + 2 * s);
      if (s > 0) {
        V_READ(s - 1, 0);
        V_READ(s - 1, 1);
        V_READ(s - 1, 2);
        V_READ(s - 1, 3);
        DMA_A(s);
        if (s != 2) DMA_B(s);
        SBAR();
        PRIO(1);
        PV_MMA(0);
        SBAR();
        K_READ(s, 0);
        SBAR();
        PV_MMA(2);
        SBAR();
        K_READ(s, 1);
      } else {
        K_READ(s, 0);
        K_READ(s, 1);
        DMA_A(s);
        DMA_B(s);
      }
      SBAR();
      QK_MMA();
      SBAR();
      PRIO(0);
      STAMP(3 + 2 * s);
      if (s == 2) DMA_B(s);
      SOFTMAX(s);
      SBAR();
      STEP_WAIT(s);
    }
    l = swap_half_add(l);
    if (h == 0) *reinterpret_cast<f32x2*>(mlp + wid * 256 + r * 8) = f32x2{m, l};
    wg_barrier();
    V_READ(3, 0);
    V_READ(3, 1);
    V_READ(3, 2);
    V_READ(3, 3);
    SBAR();
    PV_MMA(0);
    PV_MMA(2);
    SBAR();
  } else {
#pragma unroll
    for (int s = 0; s < 5; ++s) {
      wg_barrier();
      if (s == 0) {
        if (grp == 0) {
#pragma unroll
          for (int t = NPRE_A; t < 8; ++t) issue_tile(t);
        } else {
#pragma unroll
          for (int t = NPRE_B; t < 8; ++t) issue_tile(t);
        }
      }
      if (s < 4) { DMA_A(s); if (s != 2) DMA_B(s); }
      if (s > 0) {
        V_READ(s - 1, 0);
        V_READ(s - 1, 1);
        SBAR();
        SOFTMAX(s - 1);
        SBAR();
        if (s == 2) DMA_B(s);
        V_READ(s - 1, 2);
        V_READ(s - 1, 3);
        SBAR();
        PRIO(1);
        PV_MMA(0);
        SBAR();
        if (s < 4) K_READ(s, 0);
        SBAR();
        PV_MMA(2);
        SBAR();
        if (s < 4) K_READ(s, 1);
      } else {
        K_READ(s, 0);
        K_READ(s, 1);
      }
      if (s < 4) {
        SBAR();
        QK_MMA();
        SBAR();
        PRIO(0);
        STEP_WAIT(s);
      }
    }
    PRIO(0);
    l = swap_half_add(l);
    if (h == 0) *reinterpret_cast<f32x2*>(mlp + wid * 256 + r * 8) = f32x2{m, l};
  }
  const auto wo_rs = __builtin_amdgcn_make_buffer_rsrc((void*)wo, 0, HQ * DH * DM * 2, 0x00020000);
  const int wo_voff = (kvh * 512 + 8 * q) * 1024 + (hh * 128 + 8 * r16) * 2;
  u32x4 w[QD][8];
  if (u == 0) {
#pragma unroll
    for (int d = 0; d < QD; ++d)
#pragma unroll
      for (int jj = 0; jj < 8; ++jj)
        w[d][jj] = __builtin_amdgcn_raw_buffer_load_b128(wo_rs, wo_voff, (32 * ((d + rot) & 15) + jj) * 1024, 0);
  }
  SBAR();
  wg_barrier();
  STAMP(10);
  {
    char* xb_ = smem + wid * 8192;
#pragma unroll
    for (int c = 0; c < 2; ++c) {
#pragma unroll
      for (int g = 0; g < 4; ++g) {
        f32x4 v;
        if (u == 0) v = f32x4{o[2 + c][4 * g], o[2 + c][4 * g + 1], o[2 + c][4 * g + 2], o[2 + c][4 * g + 3]};
        else v = f32x4{o[c][4 * g], o[c][4 * g + 1], o[c][4 * g + 2], o[c][4 * g + 3]};
        *reinterpret_cast<f32x4*>(xb_ + (c * 4 + g) * 1024 + lane * 16) = v;
      }
    }
    const f32x2 ml2 = *reinterpret_cast<const f32x2*>(mlp + (wid ^ 4) * 256 + r * 8);
    const float mm = fmaxf(m, ml2[0]);
    const float e1 = __builtin_amdgcn_exp2f((m - mm) * CS), e2 = __builtin_amdgcn_exp2f((ml2[0] - mm) * CS);
    const float inv = 1.0f / (l * e1 + ml2[1] * e2);
    const float f1 = e1 * inv, f2 = e2 * inv;
    wg_barrier();
    const char* pb = smem + (wid ^ 4) * 8192;
    char* of = smem + QX_OFF + (r >> 4) * 1024 + (r & 15) * 16 + 8 * h;
    f32x4 pv[8];
#pragma unroll
    for (int c = 0; c < 8; ++c) pv[c] = *reinterpret_cast<const f32x4*>(pb + c * 1024 + lane * 16);
    SBAR();
#pragma unroll
    for (int c = 0; c < 2; ++c) {
      const int dt = 2 * u + c;
#pragma unroll
      for (int g = 0; g < 4; ++g) {
        f32x4 ov;
        if (u == 0) ov = f32x4{o[c][4 * g], o[c][4 * g + 1], o[c][4 * g + 2], o[c][4 * g + 3]};
        else ov = f32x4{o[2 + c][4 * g], o[2 + c][4 * g + 1], o[2 + c][4 * g + 2], o[2 + c][4 * g + 3]};
        const f32x4 p2 = pv[c * 4 + g];
        u32x2 v;
        v[0] = pk2(ov[0] * f1 + p2[0] * f2, ov[1] * f1 + p2[1] * f2);
        v[1] = pk2(ov[2] * f1 + p2[2] * f2, ov[3] * f1 + p2[3] * f2);
        *reinterpret_cast<u32x2*>(of + (hh * 4 + dt) * 2048 + g * 256) = v;
      }
    }
  }
  if (u == 1) {
    asm volatile("s_waitcnt vmcnt(0)" ::: "memory");
    wg_barrier();
    return;
  }
  {
    wg_barrier();
    STAMP(11);
    f32x4 acc[8][2];
#pragma unroll
    for (int i = 0; i < 8; ++i) { acc[i][0] = f32x4{}; acc[i][1] = f32x4{}; }
    const char* of = smem + QX_OFF + lane * 16;
#define O_STEP(S, BUF, LD)                                                                              \
  do {                                                                                                  \
    SBAR();                                                                                             \
    const bf16x8 b0 = *reinterpret_cast<const bf16x8*>(of + (((S) + rot) & 15) * 2048);                 \
    const bf16x8 b1 = *reinterpret_cast<const bf16x8*>(of + (((S) + rot) & 15) * 2048 + 1024);          \
    PROJ_STEP(w[BUF], b0, b1, if (LD) {                                                                 \
      _Pragma("unroll") for (int jj = 0; jj < 8; ++jj)                                                  \
        w[BUF][jj] = __builtin_amdgcn_raw_buffer_load_b128(wo_rs, wo_voff, (32 * (((S) + 2 + rot) & 15) + jj) * 1024, 0); \
    });                                                                                                 \
  } while (0)
#pragma unroll 1
    for (int it = 0; it < 7; ++it) {
      O_STEP(2 * it, 0, true);
      O_STEP(2 * it + 1, 1, true);
    }
    O_STEP(14, 0, false);
    O_STEP(15, 1, false);
    STAMP(12);
    char* pp = ows + ((size_t)((kvh * NB + b) * 4 + hh)) * 8192 + lane * 16;
    const auto p_rs = __builtin_amdgcn_make_buffer_rsrc((void*)ows, 0, 8 << 20, 0x00020000);
    const int p_voff = ((kvh * NB + b) * 4 + hh) * 8192 + lane * 16;
#pragma unroll
    for (int T = 0; T < 2; ++T)
#pragma unroll
      for (int g = 0; g < 4; ++g) {
        f32x4 v0 = {acc[0][T][g], acc[1][T][g], acc[2][T][g], acc[3][T][g]};
        f32x4 v1 = {acc[4][T][g], acc[5][T][g], acc[6][T][g], acc[7][T][g]};
        u32x4 pk = {pk2(v0[0], v0[1]), pk2(v0[2], v0[3]), pk2(v1[0], v1[1]), pk2(v1[2], v1[3])};
        __builtin_amdgcn_raw_buffer_store_b128(pk, p_rs, p_voff, (T * 4 + g) * 1024, ST_AUX);
      }
    STAMP(13);
  }
}

#define RT 256
__global__ __launch_bounds__(RT) void reduce_kernel(const char* __restrict__ ows, __hip_bfloat16* __restrict__ out) {
  const int gid = blockIdx.x * RT + threadIdx.x;
  const int lane = gid & 63, tg = (gid >> 6) & 7, bc = gid >> 9;
  const char* pp = ows + (size_t)bc * 8192 + tg * 1024 + lane * 16;
  u32x4 v[8];
#pragma unroll
  for (int k = 0; k < 8; ++k) v[k] = *reinterpret_cast<const u32x4*>(pp + (size_t)k * (NB * 4 * 8192));
  f32x4 a = {0.f, 0.f, 0.f, 0.f}, c = {0.f, 0.f, 0.f, 0.f};
#pragma unroll
  for (int k = 0; k < 8; ++k) {
    a[0] += __uint_as_float(v[k][0] << 16); a[1] += __uint_as_float(v[k][0] & 0xffff0000u);
    a[2] += __uint_as_float(v[k][1] << 16); a[3] += __uint_as_float(v[k][1] & 0xffff0000u);
    c[0] += __uint_as_float(v[k][2] << 16); c[1] += __uint_as_float(v[k][2] & 0xffff0000u);
    c[2] += __uint_as_float(v[k][3] << 16); c[3] += __uint_as_float(v[k][3] & 0xffff0000u);
  }
  const int b = bc >> 2, cg = bc & 3, T = tg >> 2, g = tg & 3, r16 = lane & 15, q = lane >> 4;
  u32x4 o = {pk2(a[0], a[1]), pk2(a[2], a[3]), pk2(c[0], c[1]), pk2(c[2], c[3])};
  *reinterpret_cast<u32x4*>(out + (size_t)(b * SQ + 16 * T + r16) * DM + 128 * cg + 32 * q + 8 * g) = o;
}

extern "C" void kernel_launch(void* const* d_in, const int* in_sizes, int n_in, void* d_out, int out_size,
                              void* d_ws, size_t ws_size, hipStream_t stream) {
  const __hip_bfloat16* x = (const __hip_bfloat16*)d_in[0];
  const __hip_bfloat16* kc = (const __hip_bfloat16*)d_in[1];
  const __hip_bfloat16* vc = (const __hip_bfloat16*)d_in[2];
  const __hip_bfloat16* wq = (const __hip_bfloat16*)d_in[3];
  const __hip_bfloat16* wo = (const __hip_bfloat16*)d_in[4];
  (void)hipFuncSetAttribute((const void*)attn_kernel, hipFuncAttributeMaxDynamicSharedMemorySize, LDS_BYTES);
  attn_kernel<<<256, 512, LDS_BYTES, stream>>>(x, kc, vc, wq, wo, (char*)d_ws);
  reduce_kernel<<<65536 / RT, RT, 0, stream>>>((const char*)d_ws, (__hip_bfloat16*)d_out);
}
```

```cpp
#define CODE_WARM 32
#include <hip/hip_runtime.h>
#include <hip/hip_bf16.h>

typedef __bf16 bf16x8 __attribute__((ext_vector_type(8)));
typedef __bf16 bf16x2 __attribute__((ext_vector_type(2)));
typedef short s16x4 __attribute__((ext_vector_type(4)));
typedef float f32x2 __attribute__((ext_vector_type(2)));
typedef float f32x4 __attribute__((ext_vector_type(4)));
typedef float f32x16 __attribute__((ext_vector_type(16)));
typedef unsigned u32x2 __attribute__((ext_vector_type(2)));
typedef unsigned u32x4 __attribute__((ext_vector_type(4)));
typedef __attribute__((address_space(3))) s16x4* lds_s16x4_ptr;

#define DI __device__ __forceinline__
#define MFMA32(a, b, c) __builtin_amdgcn_mfma_f32_32x32x16_bf16((a), (b), (c), 0, 0, 0)
#define MFMA16(a, b, c) __builtin_amdgcn_mfma_f32_16x16x32_bf16((a), (b), (c), 0, 0, 0)
#define SBAR() __builtin_amdgcn_sched_barrier(0)
#define STAMP(i)

constexpr int NB = 32, SQ = 32, DM = 512, HQ = 32, HKV = 8, SK = 512, DH = 128;
constexpr int LDS_BYTES = 163840;
constexpr int QX_OFF = 131072;

DI unsigned pk2(float a, float b) {
  f32x2 f = {a, b};
  return __builtin_bit_cast(unsigned, __builtin_convertvector(f, bf16x2));
}
DI float swap_half_max(float v) {
  auto rr = __builtin_amdgcn_permlane32_swap(__float_as_uint(v), __float_as_uint(v), false, false);
  return fmaxf(__uint_as_float(rr[0]), __uint_as_float(rr[1]));
}
DI float swap_half_add(float v) {
  auto rr = __builtin_amdgcn_permlane32_swap(__float_as_uint(v), __float_as_uint(v), false, false);
  return __uint_as_float(rr[0]) + __uint_as_float(rr[1]);
}
DI void wg_barrier() {
  asm volatile("s_waitcnt lgkmcnt(0)" ::: "memory");
  __builtin_amdgcn_s_barrier();
  asm volatile("" ::: "memory");
}
DI void glds16_nt(const void* sbase, unsigned voff, unsigned lds_dst) {
  unsigned keep;
  asm volatile("s_mov_b32 %0, m0\n\ts_mov_b32 m0, %3\n\ts_nop 0\n\tglobal_load_lds_dwordx4 %1, %2 nt\n\ts_mov_b32 m0, %0"
               : "=&s"(keep) : "v"(voff), "s"(sbase), "s"(lds_dst) : "memory");
}
DI void glds16(const void* sbase, unsigned voff, unsigned lds_dst) {
  unsigned keep;
  asm volatile("s_mov_b32 %0, m0\n\ts_mov_b32 m0, %3\n\ts_nop 0\n\tglobal_load_lds_dwordx4 %1, %2\n\ts_mov_b32 m0, %0"
               : "=&s"(keep) : "v"(voff), "s"(sbase), "s"(lds_dst) : "memory");
}
template <int I>
DI bf16x8 tr_frag(const u32x4* w) {
  u32x4 o;
  constexpr unsigned sel = (I & 1) ? 0x07060302u : 0x05040100u;
#pragma unroll
  for (int m = 0; m < 4; ++m) o[m] = __builtin_amdgcn_perm(w[2 * m + 1][I >> 1], w[2 * m][I >> 1], sel);
  return __builtin_bit_cast(bf16x8, o);
}
#define PROJ_STEP(W, B0, B1, LOADS)                           \
  do {                                                        \
    bf16x8 a_[8];                                             \
    a_[0] = tr_frag<0>(W); a_[1] = tr_frag<1>(W); a_[2] = tr_frag<2>(W); a_[3] = tr_frag<3>(W); \
    a_[4] = tr_frag<4>(W); a_[5] = tr_frag<5>(W); a_[6] = tr_frag<6>(W); a_[7] = tr_frag<7>(W); \
    SBAR();                                                   \
    _Pragma("unroll") for (int i_ = 0; i_ < 8; ++i_) {        \
      acc[i_][0] = MFMA16(a_[i_], B0, acc[i_][0]);            \
      acc[i_][1] = MFMA16(a_[i_], B1, acc[i_][1]);            \
    }                                                         \
    LOADS;                                                    \
    _Pragma("unroll") for (int i_ = 0; i_ < 8; ++i_) {        \
      __builtin_amdgcn_sched_group_barrier(0x8, 2, 0);        \
      __builtin_amdgcn_sched_group_barrier(0x20, 1, 0);       \
    }                                                         \
  } while (0)

#define NPRE_A 8
#define NPRE_B 4
#define BW_STEP 12
#define RESC_THR 6.0f
#define ST_AUX 16
#define ROLL
#define QDEPTH 2
constexpr int QD = QDEPTH;

__global__ __launch_bounds__(512, 2) void attn_kernel(const __hip_bfloat16* __restrict__ x,
                                                      const __hip_bfloat16* __restrict__ kc,
                                                      const __hip_bfloat16* __restrict__ vc,
                                                      const __hip_bfloat16* __restrict__ wq,
                                                      const __hip_bfloat16* __restrict__ wo,
                                                      char* __restrict__ ows) {
  extern __shared__ __attribute__((aligned(16))) char smem[];
  const int tid = threadIdx.x;
  const int wid = __builtin_amdgcn_readfirstlane(tid >> 6);
  const int lane = tid & 63;
  const int kvh = blockIdx.x & 7, b = blockIdx.x >> 3;
  const int hh = wid & 3, u = wid >> 2;
  const int r = lane & 31, h = lane >> 5;
  const int r16 = lane & 15, q = lane >> 4;
#define GRP_EXPR (b & 1)
#define ROT_EXPR ((b >> 1) & 15)
  const int grp = GRP_EXPR;
  const int rot = ROT_EXPR;
  STAMP(0);

  const unsigned lds0 = (unsigned)(size_t)smem;
  const size_t kvbase = ((size_t)(b * HKV + kvh)) * SK * DH;
  auto tile_isv = [](int t) { return t >= 14 ? 1 : (t < 2 ? 0 : (((t - 2) >> 1) & 1)); };
  auto tile_idx = [](int t) { return t >= 14 ? t - 8 : (t < 2 ? t : ((((t - 2) >> 1) & 1) ? 2 * ((t - 2) >> 2) + (t & 1) : 2 * ((t - 2) >> 2) + 2 + (t & 1))); };
  auto issue_tile = [&](int t) {
    const int slot = t < 10 ? t : t - 10;
    const int isv = tile_isv(t);
    const char* base = (const char*)((isv ? vc : kc) + kvbase + (size_t)tile_idx(t) * 64 * DH) + hh * 4096;
#pragma unroll
    for (int e = 0; e < 4; ++e) {
      const int chunk = isv ? (r16 ^ (q << 2)) : (r16 ^ (4 * e + q));
      glds16_nt(base + e * 1024, q * 256 + chunk * 16, lds0 + slot * 16384 + (4 * hh + e) * 1024);
    }
  };
  auto issue_tile8 = [&](int t) {
    const int slot = t < 10 ? t : t - 10;
    const int isv = tile_isv(t);
    const char* base = (const char*)((isv ? vc : kc) + kvbase + (size_t)tile_idx(t) * 64 * DH) + wid * 2048;
#pragma unroll
    for (int e = 0; e < 2; ++e) {
      const int chunk = isv ? (r16 ^ (q << 2)) : (r16 ^ (8 * (wid & 1) + 4 * e + q));
      glds16_nt(base + e * 1024, q * 256 + chunk * 16, lds0 + slot * 16384 + (2 * wid + e) * 1024);
    }
  };
  u32x4 cw = {0u, 0u, 0u, 0u};
  if (wid == 7 && b < CODE_WARM) {
    unsigned long long pc;
    asm volatile("s_getpc_b64 %0" : "=s"(pc));
    const char* cp = (const char*)(pc & ~255ull) + b * 1024 + lane * 16;
    asm volatile("global_load_dwordx4 %0, %1, off" : "=v"(cw) : "v"(cp) : "memory");
  }
  {
    const __hip_bfloat16* xb = x + (size_t)b * SQ * DM;
#pragma unroll
    for (int i = 0; i < 4; ++i) {
      const int row = 4 * wid + i;
      glds16(xb + row * DM, (lane ^ (row & 15)) * 16, lds0 + QX_OFF + row * 1024);
    }
  }

  bf16x8 qf[8];
  if (u == 1) {
    if (grp == 0) {
#pragma unroll
      for (int t = 0; t < NPRE_A; ++t) issue_tile(t);
      asm volatile("s_waitcnt vmcnt(%0)" ::"i"(4 * NPRE_A) : "memory");
    } else {
      asm volatile("s_waitcnt vmcnt(0)" ::: "memory");
    }
    asm volatile("" ::"v"(cw));
    wg_barrier();
    wg_barrier();
    if (grp == 1) {
#pragma unroll
      for (int t = 0; t < NPRE_B; ++t) issue_tile(t);
    }
    wg_barrier();
    wg_barrier();
#pragma unroll
    for (int p = 0; p < 8; ++p)
      qf[p] = *reinterpret_cast<const bf16x8*>(smem + QX_OFF + (hh * 8 + p) * 1024 + lane * 16);
    if (grp == 0) asm volatile("s_waitcnt vmcnt(%0)" ::"i"(4 * NPRE_A - 8) : "memory");
    else asm volatile("s_waitcnt vmcnt(%0)" ::"i"(4 * NPRE_B - 8) : "memory");
  } else {
    const auto wq_rs = __builtin_amdgcn_make_buffer_rsrc((void*)wq, 0, DM * HQ * DH * 2, 0x00020000);
    const int w_voff = (8 * q) * 8192 + (kvh * 512 + hh * 128 + 8 * r16) * 2;
    u32x4 w[QD][8];
#define PRE_W 1
#pragma unroll
    for (int d = 0; d < PRE_W; ++d) {
      const int sr = (d + rot) & 15;
#pragma unroll
      for (int jj = 0; jj < 8; ++jj)
        w[d][jj] = __builtin_amdgcn_raw_buffer_load_b128(wq_rs, w_voff, (32 * sr + jj) * 8192, 0);
    }
    asm volatile("s_waitcnt vmcnt(%0)" ::"i"(8 * PRE_W) : "memory");
    wg_barrier();
#pragma unroll
    for (int d = PRE_W; d < QD; ++d) {
      const int sr = (d + rot) & 15;
#pragma unroll
      for (int jj = 0; jj < 8; ++jj)
        w[d][jj] = __builtin_amdgcn_raw_buffer_load_b128(wq_rs, w_voff, (32 * sr + jj) * 8192, 0);
    }
    STAMP(14);
    f32x4 acc[8][2];
#pragma unroll
    for (int i = 0; i < 8; ++i) { acc[i][0] = f32x4{}; acc[i][1] = f32x4{}; }
    const char* xs = smem + QX_OFF + r16 * 1024;
#define Q_STEP(S, BUF, LD)                                                                              \
  do {                                                                                                  \
    SBAR();                                                                                             \
    const int xo = ((4 * (((S) + rot) & 15) + q) ^ r16) << 4;                                           \
    const bf16x8 b0 = *reinterpret_cast<const bf16x8*>(xs + xo);                                        \
    const bf16x8 b1 = *reinterpret_cast<const bf16x8*>(xs + xo + 16384);                                \
    const int sr = ((S) + 2 + rot) & 15;                                                                \
    PROJ_STEP(w[BUF], b0, b1, if (LD) {                                                                 \
      _Pragma("unroll") for (int jj = 0; jj < 8; ++jj)                                                  \
        w[BUF][jj] = __builtin_amdgcn_raw_buffer_load_b128(wq_rs, w_voff, (32 * sr + jj) * 8192, 0);    \
    });                                                                                                 \
  } while (0)
#pragma unroll 1
    for (int it = 0; it < 7; ++it) {
      Q_STEP(2 * it, 0, true);
      Q_STEP(2 * it + 1, 1, true);
    }
    SBAR();
    __builtin_amdgcn_s_barrier();
    Q_STEP(14, 0, false);
    Q_STEP(15, 1, false);
#pragma unroll
    for (int g = 0; g < 4; ++g) {
      u32x4 f0, f1;
#pragma unroll
      for (int e = 0; e < 4; ++e) {
        const unsigned a0 = pk2(acc[2 * e][0][g], acc[2 * e + 1][0][g]);
        const unsigned a1 = pk2(acc[2 * e][1][g], acc[2 * e + 1][1][g]);
        auto rr = __builtin_amdgcn_permlane16_swap(a0, a1, false, false);
        f0[e] = rr[0];
        f1[e] = rr[1];
      }
      qf[g] = __builtin_bit_cast(bf16x8, f0);
      qf[4 + g] = __builtin_bit_cast(bf16x8, f1);
    }
    wg_barrier();
#pragma unroll
    for (int p = 0; p < 8; ++p)
      *reinterpret_cast<bf16x8*>(smem + QX_OFF + (hh * 8 + p) * 1024 + lane * 16) = qf[p];
    STAMP(1);
    wg_barrier();
  }

  constexpr float CS = 0.088388347648318447f * 1.4426950408889634f;
  float m = -__builtin_inff(), l = 0.f;
  f32x16 o[4];
#pragma unroll
  for (int i = 0; i < 4; ++i) o[i] = f32x16{};
  const int ky = (r & 15) ^ (8 * h);
  const int koff = r * 256;
  const int vq = (lane >> 2) & 3, vpp = lane & 3;
  const int voff = (4 * h + vq) * 256 + (2 * (q & 1) + (vpp >> 1)) * 16 + 8 * (vpp & 1);
  bf16x8 fb[16];
  bf16x8 pf[4];
  f32x16 s0, s1;
#define SLOT(T) (smem + (((T) < 10 ? (T) : (T) - 10) + u) * 16384)
#define KT(S) ((S) == 0 ? 0 : 4 * (S) - 2)
#define VT(S) ((S) == 3 ? 14 : 4 * (S) + 4)
#define K_READ(S, H)                                                                                        \
  do {                                                                                                      \
    const char* Ks = SLOT(KT(S));                                                                           \
    _Pragma("unroll") for (int p = 4 * (H); p < 4 * (H) + 4; ++p) {                                         \
      const char* a = Ks + koff + ((p ^ ky) << 4);                                                          \
      fb[2 * p] = *reinterpret_cast<const bf16x8*>(a);                                                      \
      fb[2 * p + 1] = *reinterpret_cast<const bf16x8*>(a + 8192);                                           \
    }                                                                                                       \
  } while (0)
#define QK_MMA()                                                                                            \
  do {                                                                                                      \
    s0 = f32x16{}; s1 = f32x16{};                                                                           \
    _Pragma("unroll") for (int p = 0; p < 8; ++p) {                                                         \
      s0 = MFMA32(fb[2 * p], qf[p], s0);                                                                    \
      s1 = MFMA32(fb[2 * p + 1], qf[p], s1);                                                                \
    }                                                                                                       \
  } while (0)
#define V_READ(S, DT)                                                                                       \
  do {                                                                                                      \
    const char* vb = SLOT(VT(S)) + voff + (((DT) ^ vq) << 6);                                         \
    _Pragma("unroll") for (int ks = 0; ks < 4; ++ks) {                                                      \
      const s16x4 lo = __builtin_amdgcn_ds_read_tr16_b64_v4i16((lds_s16x4_ptr)(vb + ks * 4096));            \
      const s16x4 hi = __builtin_amdgcn_ds_read_tr16_b64_v4i16((lds_s16x4_ptr)(vb + ks * 4096 + 2048));     \
      fb[(DT) * 4 + ks] = __builtin_bit_cast(bf16x8, __builtin_shufflevector(lo, hi, 0, 1, 2, 3, 4, 5, 6, 7)); \
    }                                                                                                       \
  } while (0)
#define PRIO(x)
#define PV_MMA(D0)                                                                                          \
  do {                                                                                                      \
    _Pragma("unroll") for (int ks = 0; ks < 4; ++ks)                                                        \
      _Pragma("unroll") for (int dt = (D0); dt < (D0) + 2; ++dt) o[dt] = MFMA32(fb[dt * 4 + ks], pf[ks], o[dt]); \
  } while (0)
#define SOFTMAX(S)                                                                                          \
  do {                                                                                                      \
    float tmax = s0[0];                                                                                     \
    _Pragma("unroll") for (int i = 1; i < 16; ++i) tmax = fmaxf(tmax, s0[i]);                               \
    _Pragma("unroll") for (int i = 0; i < 16; ++i) tmax = fmaxf(tmax, s1[i]);                               \
    tmax = swap_half_max(tmax);                                                                             \
      \
      \
    if ((S) == 0) {                                                                                         \
      m = tmax;                                                                                             \
    } else if (__builtin_amdgcn_ballot_w64((tmax - m) * CS > RESC_THR) != 0) {                              \
      const float mnew = fmaxf(m, tmax);                                                                    \
      const float alpha = __builtin_amdgcn_exp2f((m - mnew) * CS);                                          \
      m = mnew;                                                                                             \
      l *= alpha;                                                                                           \
      _Pragma("unroll") for (int dt = 0; dt < 4; ++dt)                                                      \
        _Pragma("unroll") for (int i = 0; i < 16; ++i) o[dt][i] *= alpha;                                   \
    }                                                                                                       \
    const float mc = m * CS;                                                                                \
    float ps = 0.f;                                                                                         \
    _Pragma("unroll") for (int i = 0; i < 16; ++i) { s0[i] = __builtin_amdgcn_exp2f(fmaf(s0[i], CS, -mc)); ps += s0[i]; } \
    _Pragma("unroll") for (int i = 0; i < 16; ++i) { s1[i] = __builtin_amdgcn_exp2f(fmaf(s1[i], CS, -mc)); ps += s1[i]; } \
    l += ps;                                                                                                \
    asm volatile("" : "+v"(l));                                                                             \
    _Pragma("unroll") for (int ss = 0; ss < 2; ++ss) {                                                      \
      u32x4 u0, u1;                                                                                         \
      _Pragma("unroll") for (int e = 0; e < 4; ++e) {                                                       \
        u0[e] = pk2(s0[8 * ss + 2 * e], s0[8 * ss + 2 * e + 1]);                                            \
        u1[e] = pk2(s1[8 * ss + 2 * e], s1[8 * ss + 2 * e + 1]);                                            \
      }                                                                                                     \
      pf[ss] = __builtin_bit_cast(bf16x8, u0);                                                              \
      pf[2 + ss] = __builtin_bit_cast(bf16x8, u1);                                                          \
    }                                                                                                       \
  } while (0)
#define DMA_A(S)                                                                                            \
  do {                                                                                                      \
    if ((S) == 0) issue_tile8(8);                                                                           \
    if ((S) == 1) issue_tile8(10);                                                                          \
    if ((S) == 2) { issue_tile8(12); issue_tile8(13); }                                                     \
    if ((S) == 3) {                                                                                         \
                    \
      const char* wb = (const char*)wo + (size_t)(kvh * 512 + 16 * b + 2 * wid) * 1024;                     \
      glds16(wb, lane * 16, lds0 + 6 * 16384 + 2 * wid * 1024);                                             \
      glds16(wb + 1024, lane * 16, lds0 + 6 * 16384 + (2 * wid + 1) * 1024);                                \
    }                                                                                                       \
  } while (0)
#define DMA_B(S)                                                                                            \
  do {                                                                                                      \
    if ((S) == 0) issue_tile8(9);                                                                           \
    if ((S) == 1) issue_tile8(11);                                                                          \
    if ((S) == 2) { issue_tile8(14); issue_tile8(15); }                                                     \
  } while (0)
#define STEP_WAIT(S)                                                                                        \
  do {                                                                                                      \
    if ((S) == 0) asm volatile("s_waitcnt vmcnt(12)" ::: "memory");                                         \
    if ((S) == 1 || (S) == 2) asm volatile("s_waitcnt vmcnt(4)" ::: "memory");                              \
    if ((S) == 3) asm volatile("s_waitcnt vmcnt(2)" ::: "memory");                                          \
  } while (0)

  char* mlp = smem + 7 * 16384;
  if (u == 0) {
#pragma unroll
    for (int s = 0; s < 4; ++s) {
      wg_barrier();
      STAMP(2 + 2 * s);
      if (s > 0) {
        V_READ(s - 1, 0);
        V_READ(s - 1, 1);
        V_READ(s - 1, 2);
        V_READ(s - 1, 3);
        DMA_A(s);
        if (s != 2) DMA_B(s);
        SBAR();
        PRIO(1);
        PV_MMA(0);
        SBAR();
        K_READ(s, 0);
        SBAR();
        PV_MMA(2);
        SBAR();
        K_READ(s, 1);
      } else {
        K_READ(s, 0);
        K_READ(s, 1);
        DMA_A(s);
        DMA_B(s);
      }
      SBAR();
      QK_MMA();
      SBAR();
      PRIO(0);
      STAMP(3 + 2 * s);
      if (s == 2) DMA_B(s);
      SOFTMAX(s);
      SBAR();
      STEP_WAIT(s);
    }
    l = swap_half_add(l);
    if (h == 0) *reinterpret_cast<f32x2*>(mlp + wid * 256 + r * 8) = f32x2{m, l};
    wg_barrier();
    V_READ(3, 0);
    V_READ(3, 1);
    V_READ(3, 2);
    V_READ(3, 3);
    SBAR();
    PV_MMA(0);
    PV_MMA(2);
    SBAR();
  } else {
#pragma unroll
    for (int s = 0; s < 5; ++s) {
      wg_barrier();
      if (s == 0) {
        if (grp == 0) {
#pragma unroll
          for (int t = NPRE_A; t < 8; ++t) issue_tile(t);
        } else {
#pragma unroll
          for (int t = NPRE_B; t < 8; ++t) issue_tile(t);
        }
      }
      if (s < 4) { DMA_A(s); if (s != 2) DMA_B(s); }
      if (s > 0) {
        V_READ(s - 1, 0);
        V_READ(s - 1, 1);
        SBAR();
        SOFTMAX(s - 1);
        SBAR();
        if (s == 2) DMA_B(s);
        V_READ(s - 1, 2);
        V_READ(s - 1, 3);
        SBAR();
        PRIO(1);
        PV_MMA(0);
        SBAR();
        if (s < 4) K_READ(s, 0);
        SBAR();
        PV_MMA(2);
        SBAR();
        if (s < 4) K_READ(s, 1);
      } else {
        K_READ(s, 0);
        K_READ(s, 1);
      }
      if (s < 4) {
        SBAR();
        QK_MMA();
        SBAR();
        PRIO(0);
        STEP_WAIT(s);
      }
    }
    PRIO(0);
    l = swap_half_add(l);
    if (h == 0) *reinterpret_cast<f32x2*>(mlp + wid * 256 + r * 8) = f32x2{m, l};
  }
  const auto wo_rs = __builtin_amdgcn_make_buffer_rsrc((void*)wo, 0, HQ * DH * DM * 2, 0x00020000);
  const int wo_voff = (kvh * 512 + 8 * q) * 1024 + (hh * 128 + 8 * r16) * 2;
  u32x4 w[QD][8];
  if (u == 0) {
#pragma unroll
    for (int d = 0; d < QD; ++d)
#pragma unroll
      for (int jj = 0; jj < 8; ++jj)
        w[d][jj] = __builtin_amdgcn_raw_buffer_load_b128(wo_rs, wo_voff, (32 * ((d + rot) & 15) + jj) * 1024, 0);
  }
  SBAR();
  wg_barrier();
  STAMP(10);
  {
    char* xb_ = smem + wid * 8192;
#pragma unroll
    for (int c = 0; c < 2; ++c) {
#pragma unroll
      for (int g = 0; g < 4; ++g) {
        f32x4 v;
        if (u == 0) v = f32x4{o[2 + c][4 * g], o[2 + c][4 * g + 1], o[2 + c][4 * g + 2], o[2 + c][4 * g + 3]};
        else v = f32x4{o[c][4 * g], o[c][4 * g + 1], o[c][4 * g + 2], o[c][4 * g + 3]};
        *reinterpret_cast<f32x4*>(xb_ + (c * 4 + g) * 1024 + lane * 16) = v;
      }
    }
    const f32x2 ml2 = *reinterpret_cast<const f32x2*>(mlp + (wid ^ 4) * 256 + r * 8);
    const float mm = fmaxf(m, ml2[0]);
    const float e1 = __builtin_amdgcn_exp2f((m - mm) * CS), e2 = __builtin_amdgcn_exp2f((ml2[0] - mm) * CS);
    const float inv = 1.0f / (l * e1 + ml2[1] * e2);
    const float f1 = e1 * inv, f2 = e2 * inv;
    wg_barrier();
    const char* pb = smem + (wid ^ 4) * 8192;
    char* of = smem + QX_OFF + (r >> 4) * 1024 + (r & 15) * 16 + 8 * h;
    f32x4 pv[8];
#pragma unroll
    for (int c = 0; c < 8; ++c) pv[c] = *reinterpret_cast<const f32x4*>(pb + c * 1024 + lane * 16);
    SBAR();
#pragma unroll
    for (int c = 0; c < 2; ++c) {
      const int dt = 2 * u + c;
#pragma unroll
      for (int g = 0; g < 4; ++g) {
        f32x4 ov;
        if (u == 0) ov = f32x4{o[c][4 * g], o[c][4 * g + 1], o[c][4 * g + 2], o[c][4 * g + 3]};
        else ov = f32x4{o[2 + c][4 * g], o[2 + c][4 * g + 1], o[2 + c][4 * g + 2], o[2 + c][4 * g + 3]};
        const f32x4 p2 = pv[c * 4 + g];
        u32x2 v;
        v[0] = pk2(ov[0] * f1 + p2[0] * f2, ov[1] * f1 + p2[1] * f2);
        v[1] = pk2(ov[2] * f1 + p2[2] * f2, ov[3] * f1 + p2[3] * f2);
        *reinterpret_cast<u32x2*>(of + (hh * 4 + dt) * 2048 + g * 256) = v;
      }
    }
  }
  if (u == 1) {
    asm volatile("s_waitcnt vmcnt(0)" ::: "memory");
    wg_barrier();
    return;
  }
  {
    wg_barrier();
    STAMP(11);
    f32x4 acc[8][2];
#pragma unroll
    for (int i = 0; i < 8; ++i) { acc[i][0] = f32x4{}; acc[i][1] = f32x4{}; }
    const char* of = smem + QX_OFF + lane * 16;
#define O_STEP(S, BUF, LD)                                                                              \
  do {                                                                                                  \
    SBAR();                                                                                             \
    const bf16x8 b0 = *reinterpret_cast<const bf16x8*>(of + (((S) + rot) & 15) * 2048);                 \
    const bf16x8 b1 = *reinterpret_cast<const bf16x8*>(of + (((S) + rot) & 15) * 2048 + 1024);          \
    PROJ_STEP(w[BUF], b0, b1, if (LD) {                                                                 \
      _Pragma("unroll") for (int jj = 0; jj < 8; ++jj)                                                  \
        w[BUF][jj] = __builtin_amdgcn_raw_buffer_load_b128(wo_rs, wo_voff, (32 * (((S) + 2 + rot) & 15) + jj) * 1024, 0); \
    });                                                                                                 \
  } while (0)
#pragma unroll 1
    for (int it = 0; it < 7; ++it) {
      O_STEP(2 * it, 0, true);
      O_STEP(2 * it + 1, 1, true);
    }
    O_STEP(14, 0, false);
    O_STEP(15, 1, false);
    STAMP(12);
    char* pp = ows + ((size_t)((kvh * NB + b) * 4 + hh)) * 8192 + lane * 16;
    const auto p_rs = __builtin_amdgcn_make_buffer_rsrc((void*)ows, 0, 8 << 20, 0x00020000);
    const int p_voff = ((kvh * NB + b) * 4 + hh) * 8192 + lane * 16;
#pragma unroll
    for (int T = 0; T < 2; ++T)
#pragma unroll
      for (int g = 0; g < 4; ++g) {
        f32x4 v0 = {acc[0][T][g], acc[1][T][g], acc[2][T][g], acc[3][T][g]};
        f32x4 v1 = {acc[4][T][g], acc[5][T][g], acc[6][T][g], acc[7][T][g]};
        u32x4 pk = {pk2(v0[0], v0[1]), pk2(v0[2], v0[3]), pk2(v1[0], v1[1]), pk2(v1[2], v1[3])};
        __builtin_amdgcn_raw_buffer_store_b128(pk, p_rs, p_voff, (T * 4 + g) * 1024, ST_AUX);
      }
    STAMP(13);
  }
}

#define RT 256
__global__ __launch_bounds__(RT) void reduce_kernel(const char* __restrict__ ows, __hip_bfloat16* __restrict__ out) {
  const int gid = blockIdx.x * RT + threadIdx.x;
  const int lane = gid & 63, tg = (gid >> 6) & 7, bc = gid >> 9;
  const char* pp = ows + (size_t)bc * 8192 + tg * 1024 + lane * 16;
  u32x4 v[8];
#pragma unroll
  for (int k = 0; k < 8; ++k) v[k] = *reinterpret_cast<const u32x4*>(pp + (size_t)k * (NB * 4 * 8192));
  f32x4 a = {0.f, 0.f, 0.f, 0.f}, c = {0.f, 0.f, 0.f, 0.f};
#pragma unroll
  for (int k = 0; k < 8; ++k) {
    a[0] += __uint_as_float(v[k][0] << 16); a[1] += __uint_as_float(v[k][0] & 0xffff0000u);
    a[2] += __uint_as_float(v[k][1] << 16); a[3] += __uint_as_float(v[k][1] & 0xffff0000u);
    c[0] += __uint_as_float(v[k][2] << 16); c[1] += __uint_as_float(v[k][2] & 0xffff0000u);
    c[2] += __uint_as_float(v[k][3] << 16); c[3] += __uint_as_float(v[k][3] & 0xffff0000u);
  }
  const int b = bc >> 2, cg = bc & 3, T = tg >> 2, g = tg & 3, r16 = lane & 15, q = lane >> 4;
  u32x4 o = {pk2(a[0], a[1]), pk2(a[2], a[3]), pk2(c[0], c[1]), pk2(c[2], c[3])};
  *reinterpret_cast<u32x4*>(out + (size_t)(b * SQ + 16 * T + r16) * DM + 128 * cg + 32 * q + 8 * g) = o;
}

extern "C" void kernel_launch(void* const* d_in, const int* in_sizes, int n_in, void* d_out, int out_size,
                              void* d_ws, size_t ws_size, hipStream_t stream) {
  const __hip_bfloat16* x = (const __hip_bfloat16*)d_in[0];
  const __hip_bfloat16* kc = (const __hip_bfloat16*)d_in[1];
  const __hip_bfloat16* vc = (const __hip_bfloat16*)d_in[2];
  const __hip_bfloat16* wq = (const __hip_bfloat16*)d_in[3];
  const __hip_bfloat16* wo = (const __hip_bfloat16*)d_in[4];
  (void)hipFuncSetAttribute((const void*)attn_kernel, hipFuncAttributeMaxDynamicSharedMemorySize, LDS_BYTES);
  attn_kernel<<<256, 512, LDS_BYTES, stream>>>(x, kc, vc, wq, wo, (char*)d_ws);
  reduce_kernel<<<65536 / RT, RT, 0, stream>>>((const char*)d_ws, (__hip_bfloat16*)d_out);
}
```

```cpp
#include <hip/hip_runtime.h>
#include <hip/hip_bf16.h>

typedef __bf16 bf16x8 __attribute__((ext_vector_type(8)));
typedef __bf16 bf16x2 __attribute__((ext_vector_type(2)));
typedef short s16x4 __attribute__((ext_vector_type(4)));
typedef float f32x2 __attribute__((ext_vector_type(2)));
typedef float f32x4 __attribute__((ext_vector_type(4)));
typedef float f32x16 __attribute__((ext_vector_type(16)));
typedef unsigned u32x2 __attribute__((ext_vector_type(2)));
typedef unsigned u32x4 __attribute__((ext_vector_type(4)));
typedef __attribute__((address_space(3))) s16x4* lds_s16x4_ptr;

#define DI __device__ __forceinline__
#define MFMA32(a, b, c) __builtin_amdgcn_mfma_f32_32x32x16_bf16((a), (b), (c), 0, 0, 0)
#define MFMA16(a, b, c) __builtin_amdgcn_mfma_f32_16x16x32_bf16((a), (b), (c), 0, 0, 0)
#define SBAR() __builtin_amdgcn_sched_barrier(0)
#define STAMP(i)

constexpr int NB = 32, SQ = 32, DM = 512, HQ = 32, HKV = 8, SK = 512, DH = 128;
constexpr int LDS_BYTES = 163840;
constexpr int QX_OFF = 131072;

DI unsigned pk2(float a, float b) {
  f32x2 f = {a, b};
  return __builtin_bit_cast(unsigned, __builtin_convertvector(f, bf16x2));
}
DI float swap_half_max(float v) {
  auto rr = __builtin_amdgcn_permlane32_swap(__float_as_uint(v), __float_as_uint(v), false, false);
  return fmaxf(__uint_as_float(rr[0]), __uint_as_float(rr[1]));
}
DI float swap_half_add(float v) {
  auto rr = __builtin_amdgcn_permlane32_swap(__float_as_uint(v), __float_as_uint(v), false, false);
  return __uint_as_float(rr[0]) + __uint_as_float(rr[1]);
}
DI void wg_barrier() {
  asm volatile("s_waitcnt lgkmcnt(0)" ::: "memory");
  __builtin_amdgcn_s_barrier();
  asm volatile("" ::: "memory");
}
DI void glds16_nt(const void* sbase, unsigned voff, unsigned lds_dst) {
  unsigned keep;
  asm volatile("s_mov_b32 %0, m0\n\ts_mov_b32 m0, %3\n\ts_nop 0\n\tglobal_load_lds_dwordx4 %1, %2 nt\n\ts_mov_b32 m0, %0"
               : "=&s"(keep) : "v"(voff), "s"(sbase), "s"(lds_dst) : "memory");
}
DI void glds16(const void* sbase, unsigned voff, unsigned lds_dst) {
  unsigned keep;
  asm volatile("s_mov_b32 %0, m0\n\ts_mov_b32 m0, %3\n\ts_nop 0\n\tglobal_load_lds_dwordx4 %1, %2\n\ts_mov_b32 m0, %0"
               : "=&s"(keep) : "v"(voff), "s"(sbase), "s"(lds_dst) : "memory");
}
template <int I>
DI bf16x8 tr_frag(const u32x4* w) {
  u32x4 o;
  constexpr unsigned sel = (I & 1) ? 0x07060302u : 0x05040100u;
#pragma unroll
  for (int m = 0; m < 4; ++m) o[m] = __builtin_amdgcn_perm(w[2 * m + 1][I >> 1], w[2 * m][I >> 1], sel);
  return __builtin_bit_cast(bf16x8, o);
}
#define PROJ_STEP(W, B0, B1, LOADS)                           \
  do {                                                        \
    bf16x8 a_[8];                                             \
    a_[0] = tr_frag<0>(W); a_[1] = tr_frag<1>(W); a_[2] = tr_frag<2>(W); a_[3] = tr_frag<3>(W); \
    a_[4] = tr_frag<4>(W); a_[5] = tr_frag<5>(W); a_[6] = tr_frag<6>(W); a_[7] = tr_frag<7>(W); \
    SBAR();                                                   \
    _Pragma("unroll") for (int i_ = 0; i_ < 8; ++i_) {        \
      acc[i_][0] = MFMA16(a_[i_], B0, acc[i_][0]);            \
      acc[i_][1] = MFMA16(a_[i_], B1, acc[i_][1]);            \
    }                                                         \
    LOADS;                                                    \
    _Pragma("unroll") for (int i_ = 0; i_ < 8; ++i_) {        \
      __builtin_amdgcn_sched_group_barrier(0x8, 2, 0);        \
      __builtin_amdgcn_sched_group_barrier(0x20, 1, 0);       \
    }                                                         \
  } while (0)

#define NPRE_A 8
#define NPRE_B 4
#define BW_STEP 12
#define RESC_THR 6.0f
#define ST_AUX 16
#define ROLL
#define CODE_WARM 23
#define QDEPTH 2
constexpr int QD = QDEPTH;

__global__ __launch_bounds__(512, 2) void attn_kernel(const __hip_bfloat16* __restrict__ x,
                                                      const __hip_bfloat16* __restrict__ kc,
                                                      const __hip_bfloat16* __restrict__ vc,
                                                      const __hip_bfloat16* __restrict__ wq,
                                                      const __hip_bfloat16* __restrict__ wo,
                                                      char* __restrict__ ows) {
  extern __shared__ __attribute__((aligned(16))) char smem[];
  const int tid = threadIdx.x;
  const int wid = __builtin_amdgcn_readfirstlane(tid >> 6);
  const int lane = tid & 63;
  const int kvh = blockIdx.x & 7, b = blockIdx.x >> 3;
  const int hh = wid & 3, u = wid >> 2;
  const int r = lane & 31, h = lane >> 5;
  const int r16 = lane & 15, q = lane >> 4;
#define GRP_EXPR (b & 1)
#define ROT_EXPR ((b >> 1) & 15)
  const int grp = GRP_EXPR;
  const int rot = ROT_EXPR;
  STAMP(0);

  const unsigned lds0 = (unsigned)(size_t)smem;
  const size_t kvbase = ((size_t)(b * HKV + kvh)) * SK * DH;
  auto tile_isv = [](int t) { return t >= 14 ? 1 : (t < 2 ? 0 : (((t - 2) >> 1) & 1)); };
  auto tile_idx = [](int t) { return t >= 14 ? t - 8 : (t < 2 ? t : ((((t - 2) >> 1) & 1) ? 2 * ((t - 2) >> 2) + (t & 1) : 2 * ((t - 2) >> 2) + 2 + (t & 1))); };
  auto issue_tile = [&](int t) {
    const int slot = t < 10 ? t : t - 10;
    const int isv = tile_isv(t);
    const char* base = (const char*)((isv ? vc : kc) + kvbase + (size_t)tile_idx(t) * 64 * DH) + hh * 4096;
#pragma unroll
    for (int e = 0; e < 4; ++e) {
      const int chunk = isv ? (r16 ^ (q << 2)) : (r16 ^ (4 * e + q));
      glds16_nt(base + e * 1024, q * 256 + chunk * 16, lds0 + slot * 16384 + (4 * hh + e) * 1024);
    }
  };
  auto issue_tile8 = [&](int t) {
    const int slot = t < 10 ? t : t - 10;
    const int isv = tile_isv(t);
    const char* base = (const char*)((isv ? vc : kc) + kvbase + (size_t)tile_idx(t) * 64 * DH) + wid * 2048;
#pragma unroll
    for (int e = 0; e < 2; ++e) {
      const int chunk = isv ? (r16 ^ (q << 2)) : (r16 ^ (8 * (wid & 1) + 4 * e + q));
      glds16_nt(base + e * 1024, q * 256 + chunk * 16, lds0 + slot * 16384 + (2 * wid + e) * 1024);
    }
  };
  u32x4 cw = {0u, 0u, 0u, 0u};
  if (wid >= 6 && b + 32 * (7 - wid) < CODE_WARM) {
    unsigned long long pc;
    asm volatile("s_getpc_b64 %0" : "=s"(pc));
    const char* cp = (const char*)(pc & ~255ull) + (b + 32 * (7 - wid)) * 1024 + lane * 16;
    asm volatile("global_load_dwordx4 %0, %1, off" : "=v"(cw) : "v"(cp) : "memory");
  }
  {
    const __hip_bfloat16* xb = x + (size_t)b * SQ * DM;
#pragma unroll
    for (int i = 0; i < 4; ++i) {
      const int row = 4 * wid + i;
      glds16(xb + row * DM, (lane ^ (row & 15)) * 16, lds0 + QX_OFF + row * 1024);
    }
  }

  bf16x8 qf[8];
  if (u == 1) {
    if (grp == 0) {
#pragma unroll
      for (int t = 0; t < NPRE_A; ++t) issue_tile(t);
      asm volatile("s_waitcnt vmcnt(%0)" ::"i"(4 * NPRE_A) : "memory");
    } else {
      asm volatile("s_waitcnt vmcnt(0)" ::: "memory");
    }
    asm volatile("" ::"v"(cw));
    wg_barrier();
    wg_barrier();
    if (grp == 1) {
#pragma unroll
      for (int t = 0; t < NPRE_B; ++t) issue_tile(t);
    }
    wg_barrier();
    wg_barrier();
#pragma unroll
    for (int p = 0; p < 8; ++p)
      qf[p] = *reinterpret_cast<const bf16x8*>(smem + QX_OFF + (hh * 8 + p) * 1024 + lane * 16);
    if (grp == 0) asm volatile("s_waitcnt vmcnt(%0)" ::"i"(4 * NPRE_A - 8) : "memory");
    else asm volatile("s_waitcnt vmcnt(%0)" ::"i"(4 * NPRE_B - 8) : "memory");
  } else {
    const auto wq_rs = __builtin_amdgcn_make_buffer_rsrc((void*)wq, 0, DM * HQ * DH * 2, 0x00020000);
    const int w_voff = (8 * q) * 8192 + (kvh * 512 + hh * 128 + 8 * r16) * 2;
    u32x4 w[QD][8];
#define PRE_W 1
#pragma unroll
    for (int d = 0; d < PRE_W; ++d) {
      const int sr = (d + rot) & 15;
#pragma unroll
      for (int jj = 0; jj < 8; ++jj)
        w[d][jj] = __builtin_amdgcn_raw_buffer_load_b128(wq_rs, w_voff, (32 * sr + jj) * 8192, 0);
    }
    asm volatile("s_waitcnt vmcnt(%0)" ::"i"(8 * PRE_W) : "memory");
    wg_barrier();
#pragma unroll
    for (int d = PRE_W; d < QD; ++d) {
      const int sr = (d + rot) & 15;
#pragma unroll
      for (int jj = 0; jj < 8; ++jj)
        w[d][jj] = __builtin_amdgcn_raw_buffer_load_b128(wq_rs, w_voff, (32 * sr + jj) * 8192, 0);
    }
    STAMP(14);
    f32x4 acc[8][2];
#pragma unroll
    for (int i = 0; i < 8; ++i) { acc[i][0] = f32x4{}; acc[i][1] = f32x4{}; }
    const char* xs = smem + QX_OFF + r16 * 1024;
#define Q_STEP(S, BUF, LD)                                                                              \
  do {                                                                                                  \
    SBAR();                                                                                             \
    const int xo = ((4 * (((S) + rot) & 15) + q) ^ r16) << 4;                                           \
    const bf16x8 b0 = *reinterpret_cast<const bf16x8*>(xs + xo);                                        \
    const bf16x8 b1 = *reinterpret_cast<const bf16x8*>(xs + xo + 16384);                                \
    const int sr = ((S) + 2 + rot) & 15;                                                                \
    PROJ_STEP(w[BUF], b0, b1, if (LD) {                                                                 \
      _Pragma("unroll") for (int jj = 0; jj < 8; ++jj)                                                  \
        w[BUF][jj] = __builtin_amdgcn_raw_buffer_load_b128(wq_rs, w_voff, (32 * sr + jj) * 8192, 0);    \
    });                                                                                                 \
  } while (0)
#pragma unroll 1
    for (int it = 0; it < 7; ++it) {
      Q_STEP(2 * it, 0, true);
      Q_STEP(2 * it + 1, 1, true);
    }
    SBAR();
    __builtin_amdgcn_s_barrier();
    Q_STEP(14, 0, false);
    Q_STEP(15, 1, false);
#pragma unroll
    for (int g = 0; g < 4; ++g) {
      u32x4 f0, f1;
#pragma unroll
      for (int e = 0; e < 4; ++e) {
        const unsigned a0 = pk2(acc[2 * e][0][g], acc[2 * e + 1][0][g]);
        const unsigned a1 = pk2(acc[2 * e][1][g], acc[2 * e + 1][1][g]);
        auto rr = __builtin_amdgcn_permlane16_swap(a0, a1, false, false);
        f0[e] = rr[0];
        f1[e] = rr[1];
      }
      qf[g] = __builtin_bit_cast(bf16x8, f0);
      qf[4 + g] = __builtin_bit_cast(bf16x8, f1);
    }
    wg_barrier();
#pragma unroll
    for (int p = 0; p < 8; ++p)
      *reinterpret_cast<bf16x8*>(smem + QX_OFF + (hh * 8 + p) * 1024 + lane * 16) = qf[p];
    STAMP(1);
    wg_barrier();
  }

  constexpr float CS = 0.088388347648318447f * 1.4426950408889634f;
  float m = -__builtin_inff(), l = 0.f;
  f32x16 o[4];
#pragma unroll
  for (int i = 0; i < 4; ++i) o[i] = f32x16{};
  const int ky = (r & 15) ^ (8 * h);
  const int koff = r * 256;
  const int vq = (lane >> 2) & 3, vpp = lane & 3;
  const int voff = (4 * h + vq) * 256 + (2 * (q & 1) + (vpp >> 1)) * 16 + 8 * (vpp & 1);
  bf16x8 fb[16];
  bf16x8 pf[4];
  f32x16 s0, s1;
#define SLOT(T) (smem + (((T) < 10 ? (T) : (T) - 10) + u) * 16384)
#define KT(S) ((S) == 0 ? 0 : 4 * (S) - 2)
#define VT(S) ((S) == 3 ? 14 : 4 * (S) + 4)
#define K_READ(S, H)                                                                                        \
  do {                                                                                                      \
    const char* Ks = SLOT(KT(S));                                                                           \
    _Pragma("unroll") for (int p = 4 * (H); p < 4 * (H) + 4; ++p) {                                         \
      const char* a = Ks + koff + ((p ^ ky) << 4);                                                          \
      fb[2 * p] = *reinterpret_cast<const bf16x8*>(a);                                                      \
      fb[2 * p + 1] = *reinterpret_cast<const bf16x8*>(a + 8192);                                           \
    }                                                                                                       \
  } while (0)
#define QK_MMA()                                                                                            \
  do {                                                                                                      \
    s0 = f32x16{}; s1 = f32x16{};                                                                           \
    _Pragma("unroll") for (int p = 0; p < 8; ++p) {                                                         \
      s0 = MFMA32(fb[2 * p], qf[p], s0);                                                                    \
      s1 = MFMA32(fb[2 * p + 1], qf[p], s1);                                                                \
    }                                                                                                       \
  } while (0)
#define V_READ(S, DT)                                                                                       \
  do {                                                                                                      \
    const char* vb = SLOT(VT(S)) + voff + (((DT) ^ vq) << 6);                                         \
    _Pragma("unroll") for (int ks = 0; ks < 4; ++ks) {                                                      \
      const s16x4 lo = __builtin_amdgcn_ds_read_tr16_b64_v4i16((lds_s16x4_ptr)(vb + ks * 4096));            \
      const s16x4 hi = __builtin_amdgcn_ds_read_tr16_b64_v4i16((lds_s16x4_ptr)(vb + ks * 4096 + 2048));     \
      fb[(DT) * 4 + ks] = __builtin_bit_cast(bf16x8, __builtin_shufflevector(lo, hi, 0, 1, 2, 3, 4, 5, 6, 7)); \
    }                                                                                                       \
  } while (0)
#define PRIO(x)
#define PV_MMA(D0)                                                                                          \
  do {                                                                                                      \
    _Pragma("unroll") for (int ks = 0; ks < 4; ++ks)                                                        \
      _Pragma("unroll") for (int dt = (D0); dt < (D0) + 2; ++dt) o[dt] = MFMA32(fb[dt * 4 + ks], pf[ks], o[dt]); \
  } while (0)
#define SOFTMAX(S)                                                                                          \
  do {                                                                                                      \
    float tmax = s0[0];                                                                                     \
    _Pragma("unroll") for (int i = 1; i < 16; ++i) tmax = fmaxf(tmax, s0[i]);                               \
    _Pragma("unroll") for (int i = 0; i < 16; ++i) tmax = fmaxf(tmax, s1[i]);                               \
    tmax = swap_half_max(tmax);                                                                             \
      \
      \
    if ((S) == 0) {                                                                                         \
      m = tmax;                                                                                             \
    } else if (__builtin_amdgcn_ballot_w64((tmax - m) * CS > RESC_THR) != 0) {                              \
      const float mnew = fmaxf(m, tmax);                                                                    \
      const float alpha = __builtin_amdgcn_exp2f((m - mnew) * CS);                                          \
      m = mnew;                                                                                             \
      l *= alpha;                                                                                           \
      _Pragma("unroll") for (int dt = 0; dt < 4; ++dt)                                                      \
        _Pragma("unroll") for (int i = 0; i < 16; ++i) o[dt][i] *= alpha;                                   \
    }                                                                                                       \
    const float mc = m * CS;                                                                                \
    float ps = 0.f;                                                                                         \
    _Pragma("unroll") for (int i = 0; i < 16; ++i) { s0[i] = __builtin_amdgcn_exp2f(fmaf(s0[i], CS, -mc)); ps += s0[i]; } \
    _Pragma("unroll") for (int i = 0; i < 16; ++i) { s1[i] = __builtin_amdgcn_exp2f(fmaf(s1[i], CS, -mc)); ps += s1[i]; } \
    l += ps;                                                                                                \
    asm volatile("" : "+v"(l));                                                                             \
    _Pragma("unroll") for (int ss = 0; ss < 2; ++ss) {                                                      \
      u32x4 u0, u1;                                                                                         \
      _Pragma("unroll") for (int e = 0; e < 4; ++e) {                                                       \
        u0[e] = pk2(s0[8 * ss + 2 * e], s0[8 * ss + 2 * e + 1]);                                            \
        u1[e] = pk2(s1[8 * ss + 2 * e], s1[8 * ss + 2 * e + 1]);                                            \
      }                                                                                                     \
      pf[ss] = __builtin_bit_cast(bf16x8, u0);                                                              \
      pf[2 + ss] = __builtin_bit_cast(bf16x8, u1);                                                          \
    }                                                                                                       \
  } while (0)
#define DMA_A(S)                                                                                            \
  do {                                                                                                      \
    if ((S) == 0) issue_tile8(8);                                                                           \
    if ((S) == 1) issue_tile8(10);                                                                          \
    if ((S) == 2) { issue_tile8(12); issue_tile8(13); }                                                     \
    if ((S) == 3) {                                                                                         \
                    \
      const char* wb = (const char*)wo + (size_t)(kvh * 512 + 16 * b + 2 * wid) * 1024;                     \
      glds16(wb, lane * 16, lds0 + 6 * 16384 + 2 * wid * 1024);                                             \
      glds16(wb + 1024, lane * 16, lds0 + 6 * 16384 + (2 * wid + 1) * 1024);                                \
    }                                                                                                       \
  } while (0)
#define DMA_B(S)                                                                                            \
  do {                                                                                                      \
    if ((S) == 0) issue_tile8(9);                                                                           \
    if ((S) == 1) issue_tile8(11);                                                                          \
    if ((S) == 2) { issue_tile8(14); issue_tile8(15); }                                                     \
  } while (0)
#define STEP_WAIT(S)                                                                                        \
  do {                                                                                                      \
    if ((S) == 0) asm volatile("s_waitcnt vmcnt(12)" ::: "memory");                                         \
    if ((S) == 1 || (S) == 2) asm volatile("s_waitcnt vmcnt(4)" ::: "memory");                              \
    if ((S) == 3) asm volatile("s_waitcnt vmcnt(2)" ::: "memory");                                          \
  } while (0)

  char* mlp = smem + 7 * 16384;
  if (u == 1) {
    wg_barrier();
    if (grp == 0) {
#pragma unroll
      for (int t = NPRE_A; t < 8; ++t) issue_tile(t);
    } else {
#pragma unroll
      for (int t = NPRE_B; t < 8; ++t) issue_tile(t);
    }
  } else {
    wg_barrier();
  }
  STAMP(2);
  K_READ(0, 0);
  K_READ(0, 1);
  DMA_A(0);
  DMA_B(0);
  SBAR();
  QK_MMA();
  SBAR();
  if (u == 1) {
    STEP_WAIT(0);
    wg_barrier();
    DMA_A(1);
    DMA_B(1);
  }
  SOFTMAX(0);
  SBAR();
  if (u == 0) STEP_WAIT(0);
#pragma unroll 1
  for (int i = 1; i < 4; ++i) {
    if (u == 0) wg_barrier();
    V_READ(i - 1, 0);
    V_READ(i - 1, 1);
    V_READ(i - 1, 2);
    V_READ(i - 1, 3);
    if (u == 0) { DMA_A(i); DMA_B(i); }
    SBAR();
    PV_MMA(0);
    SBAR();
    K_READ(i, 0);
    SBAR();
    PV_MMA(2);
    SBAR();
    K_READ(i, 1);
    SBAR();
    QK_MMA();
    SBAR();
    if (u == 1) {
      STEP_WAIT(i);
      wg_barrier();
      DMA_A(i + 1);
      DMA_B(i + 1);
    }
    SOFTMAX(1);
    SBAR();
    if (u == 0) STEP_WAIT(i);
  }
  l = swap_half_add(l);
  if (h == 0) *reinterpret_cast<f32x2*>(mlp + wid * 256 + r * 8) = f32x2{m, l};
  if (u == 0) wg_barrier();
  STAMP(9);
  V_READ(3, 0);
  V_READ(3, 1);
  V_READ(3, 2);
  V_READ(3, 3);
  SBAR();
  PV_MMA(0);
  PV_MMA(2);
  SBAR();
  const auto wo_rs = __builtin_amdgcn_make_buffer_rsrc((void*)wo, 0, HQ * DH * DM * 2, 0x00020000);
  const int wo_voff = (kvh * 512 + 8 * q) * 1024 + (hh * 128 + 8 * r16) * 2;
  u32x4 w[QD][8];
  if (u == 0) {
#pragma unroll
    for (int d = 0; d < QD; ++d)
#pragma unroll
      for (int jj = 0; jj < 8; ++jj)
        w[d][jj] = __builtin_amdgcn_raw_buffer_load_b128(wo_rs, wo_voff, (32 * ((d + rot) & 15) + jj) * 1024, 0);
  }
  SBAR();
  wg_barrier();
  STAMP(10);
  {
    char* xb_ = smem + wid * 8192;
#pragma unroll
    for (int c = 0; c < 2; ++c) {
#pragma unroll
      for (int g = 0; g < 4; ++g) {
        f32x4 v;
        if (u == 0) v = f32x4{o[2 + c][4 * g], o[2 + c][4 * g + 1], o[2 + c][4 * g + 2], o[2 + c][4 * g + 3]};
        else v = f32x4{o[c][4 * g], o[c][4 * g + 1], o[c][4 * g + 2], o[c][4 * g + 3]};
        *reinterpret_cast<f32x4*>(xb_ + (c * 4 + g) * 1024 + lane * 16) = v;
      }
    }
    const f32x2 ml2 = *reinterpret_cast<const f32x2*>(mlp + (wid ^ 4) * 256 + r * 8);
    const float mm = fmaxf(m, ml2[0]);
    const float e1 = __builtin_amdgcn_exp2f((m - mm) * CS), e2 = __builtin_amdgcn_exp2f((ml2[0] - mm) * CS);
    const float inv = 1.0f / (l * e1 + ml2[1] * e2);
    const float f1 = e1 * inv, f2 = e2 * inv;
    wg_barrier();
    const char* pb = smem + (wid ^ 4) * 8192;
    char* of = smem + QX_OFF + (r >> 4) * 1024 + (r & 15) * 16 + 8 * h;
    f32x4 pv[8];
#pragma unroll
    for (int c = 0; c < 8; ++c) pv[c] = *reinterpret_cast<const f32x4*>(pb + c * 1024 + lane * 16);
    SBAR();
#pragma unroll
    for (int c = 0; c < 2; ++c) {
      const int dt = 2 * u + c;
#pragma unroll
      for (int g = 0; g < 4; ++g) {
        f32x4 ov;
        if (u == 0) ov = f32x4{o[c][4 * g], o[c][4 * g + 1], o[c][4 * g + 2], o[c][4 * g + 3]};
        else ov = f32x4{o[2 + c][4 * g], o[2 + c][4 * g + 1], o[2 + c][4 * g + 2], o[2 + c][4 * g + 3]};
        const f32x4 p2 = pv[c * 4 + g];
        u32x2 v;
        v[0] = pk2(ov[0] * f1 + p2[0] * f2, ov[1] * f1 + p2[1] * f2);
        v[1] = pk2(ov[2] * f1 + p2[2] * f2, ov[3] * f1 + p2[3] * f2);
        *reinterpret_cast<u32x2*>(of + (hh * 4 + dt) * 2048 + g * 256) = v;
      }
    }
  }
  if (u == 1) {
    asm volatile("s_waitcnt vmcnt(0)" ::: "memory");
    wg_barrier();
    return;
  }
  {
    wg_barrier();
    STAMP(11);
    f32x4 acc[8][2];
#pragma unroll
    for (int i = 0; i < 8; ++i) { acc[i][0] = f32x4{}; acc[i][1] = f32x4{}; }
    const char* of = smem + QX_OFF + lane * 16;
#define O_STEP(S, BUF, LD)                                                                              \
  do {                                                                                                  \
    SBAR();                                                                                             \
    const bf16x8 b0 = *reinterpret_cast<const bf16x8*>(of + (((S) + rot) & 15) * 2048);                 \
    const bf16x8 b1 = *reinterpret_cast<const bf16x8*>(of + (((S) + rot) & 15) * 2048 + 1024);          \
    PROJ_STEP(w[BUF], b0, b1, if (LD) {                                                                 \
      _Pragma("unroll") for (int jj = 0; jj < 8; ++jj)                                                  \
        w[BUF][jj] = __builtin_amdgcn_raw_buffer_load_b128(wo_rs, wo_voff, (32 * (((S) + 2 + rot) & 15) + jj) * 1024, 0); \
    });                                                                                                 \
  } while (0)
#pragma unroll 1
    for (int it = 0; it < 7; ++it) {
      O_STEP(2 * it, 0, true);
      O_STEP(2 * it + 1, 1, true);
    }
    O_STEP(14, 0, false);
    O_STEP(15, 1, false);
    STAMP(12);
    char* pp = ows + ((size_t)((kvh * NB + b) * 4 + hh)) * 8192 + lane * 16;
    const auto p_rs = __builtin_amdgcn_make_buffer_rsrc((void*)ows, 0, 8 << 20, 0x00020000);
    const int p_voff = ((kvh * NB + b) * 4 + hh) * 8192 + lane * 16;
#pragma unroll
    for (int T = 0; T < 2; ++T)
#pragma unroll
      for (int g = 0; g < 4; ++g) {
        f32x4 v0 = {acc[0][T][g], acc[1][T][g], acc[2][T][g], acc[3][T][g]};
        f32x4 v1 = {acc[4][T][g], acc[5][T][g], acc[6][T][g], acc[7][T][g]};
        u32x4 pk = {pk2(v0[0], v0[1]), pk2(v0[2], v0[3]), pk2(v1[0], v1[1]), pk2(v1[2], v1[3])};
        __builtin_amdgcn_raw_buffer_store_b128(pk, p_rs, p_voff, (T * 4 + g) * 1024, ST_AUX);
      }
    STAMP(13);
  }
}

#define RT 256
__global__ __launch_bounds__(RT) void reduce_kernel(const char* __restrict__ ows, __hip_bfloat16* __restrict__ out) {
  const int gid = blockIdx.x * RT + threadIdx.x;
  const int lane = gid & 63, tg = (gid >> 6) & 7, bc = gid >> 9;
  const char* pp = ows + (size_t)bc * 8192 + tg * 1024 + lane * 16;
  u32x4 v[8];
#pragma unroll
  for (int k = 0; k < 8; ++k) v[k] = *reinterpret_cast<const u32x4*>(pp + (size_t)k * (NB * 4 * 8192));
  f32x4 a = {0.f, 0.f, 0.f, 0.f}, c = {0.f, 0.f, 0.f, 0.f};
#pragma unroll
  for (int k = 0; k < 8; ++k) {
    a[0] += __uint_as_float(v[k][0] << 16); a[1] += __uint_as_float(v[k][0] & 0xffff0000u);
    a[2] += __uint_as_float(v[k][1] << 16); a[3] += __uint_as_float(v[k][1] & 0xffff0000u);
    c[0] += __uint_as_float(v[k][2] << 16); c[1] += __uint_as_float(v[k][2] & 0xffff0000u);
    c[2] += __uint_as_float(v[k][3] << 16); c[3] += __uint_as_float(v[k][3] & 0xffff0000u);
  }
  const int b = bc >> 2, cg = bc & 3, T = tg >> 2, g = tg & 3, r16 = lane & 15, q = lane >> 4;
  u32x4 o = {pk2(a[0], a[1]), pk2(a[2], a[3]), pk2(c[0], c[1]), pk2(c[2], c[3])};
  *reinterpret_cast<u32x4*>(out + (size_t)(b * SQ + 16 * T + r16) * DM + 128 * cg + 32 * q + 8 * g) = o;
}

extern "C" void kernel_launch(void* const* d_in, const int* in_sizes, int n_in, void* d_out, int out_size,
                              void* d_ws, size_t ws_size, hipStream_t stream) {
  const __hip_bfloat16* x = (const __hip_bfloat16*)d_in[0];
  const __hip_bfloat16* kc = (const __hip_bfloat16*)d_in[1];
  const __hip_bfloat16* vc = (const __hip_bfloat16*)d_in[2];
  const __hip_bfloat16* wq = (const __hip_bfloat16*)d_in[3];
  const __hip_bfloat16* wo = (const __hip_bfloat16*)d_in[4];
  (void)hipFuncSetAttribute((const void*)attn_kernel, hipFuncAttributeMaxDynamicSharedMemorySize, LDS_BYTES);
  attn_kernel<<<256, 512, LDS_BYTES, stream>>>(x, kc, vc, wq, wo, (char*)d_ws);
  reduce_kernel<<<65536 / RT, RT, 0, stream>>>((const char*)d_ws, (__hip_bfloat16*)d_out);
}
```

```cpp
#include <hip/hip_runtime.h>
#include <hip/hip_bf16.h>

typedef __bf16 bf16x8 __attribute__((ext_vector_type(8)));
typedef __bf16 bf16x2 __attribute__((ext_vector_type(2)));
typedef short s16x4 __attribute__((ext_vector_type(4)));
typedef float f32x2 __attribute__((ext_vector_type(2)));
typedef float f32x4 __attribute__((ext_vector_type(4)));
typedef float f32x16 __attribute__((ext_vector_type(16)));
typedef unsigned u32x2 __attribute__((ext_vector_type(2)));
typedef unsigned u32x4 __attribute__((ext_vector_type(4)));
typedef __attribute__((address_space(3))) s16x4* lds_s16x4_ptr;

#define DI __device__ __forceinline__
#define MFMA32(a, b, c) __builtin_amdgcn_mfma_f32_32x32x16_bf16((a), (b), (c), 0, 0, 0)
#define MFMA16(a, b, c) __builtin_amdgcn_mfma_f32_16x16x32_bf16((a), (b), (c), 0, 0, 0)
#define SBAR() __builtin_amdgcn_sched_barrier(0)
#define STAMP(i)

constexpr int NB = 32, SQ = 32, DM = 512, HQ = 32, HKV = 8, SK = 512, DH = 128;
constexpr int LDS_BYTES = 163840;
constexpr int QX_OFF = 131072;

DI unsigned pk2(float a, float b) {
  f32x2 f = {a, b};
  return __builtin_bit_cast(unsigned, __builtin_convertvector(f, bf16x2));
}
DI float swap_half_max(float v) {
  auto rr = __builtin_amdgcn_permlane32_swap(__float_as_uint(v), __float_as_uint(v), false, false);
  return fmaxf(__uint_as_float(rr[0]), __uint_as_float(rr[1]));
}
DI float swap_half_add(float v) {
  auto rr = __builtin_amdgcn_permlane32_swap(__float_as_uint(v), __float_as_uint(v), false, false);
  return __uint_as_float(rr[0]) + __uint_as_float(rr[1]);
}
DI void wg_barrier() {
  asm volatile("s_waitcnt lgkmcnt(0)" ::: "memory");
  __builtin_amdgcn_s_barrier();
  asm volatile("" ::: "memory");
}
DI void glds16_nt(const void* sbase, unsigned voff, unsigned lds_dst) {
  unsigned keep;
  asm volatile("s_mov_b32 %0, m0\n\ts_mov_b32 m0, %3\n\ts_nop 0\n\tglobal_load_lds_dwordx4 %1, %2 nt\n\ts_mov_b32 m0, %0"
               : "=&s"(keep) : "v"(voff), "s"(sbase), "s"(lds_dst) : "memory");
}
DI void glds16(const void* sbase, unsigned voff, unsigned lds_dst) {
  unsigned keep;
  asm volatile("s_mov_b32 %0, m0\n\ts_mov_b32 m0, %3\n\ts_nop 0\n\tglobal_load_lds_dwordx4 %1, %2\n\ts_mov_b32 m0, %0"
               : "=&s"(keep) : "v"(voff), "s"(sbase), "s"(lds_dst) : "memory");
}
template <int I>
DI bf16x8 tr_frag(const u32x4* w) {
  u32x4 o;
  constexpr unsigned sel = (I & 1) ? 0x07060302u : 0x05040100u;
#pragma unroll
  for (int m = 0; m < 4; ++m) o[m] = __builtin_amdgcn_perm(w[2 * m + 1][I >> 1], w[2 * m][I >> 1], sel);
  return __builtin_bit_cast(bf16x8, o);
}
#define PROJ_STEP(W, B0, B1, LOADS)                           \
  do {                                                        \
    bf16x8 a_[8];                                             \
    a_[0] = tr_frag<0>(W); a_[1] = tr_frag<1>(W); a_[2] = tr_frag<2>(W); a_[3] = tr_frag<3>(W); \
    a_[4] = tr_frag<4>(W); a_[5] = tr_frag<5>(W); a_[6] = tr_frag<6>(W); a_[7] = tr_frag<7>(W); \
    SBAR();                                                   \
    _Pragma("unroll") for (int i_ = 0; i_ < 8; ++i_) {        \
      acc[i_][0] = MFMA16(a_[i_], B0, acc[i_][0]);            \
      acc[i_][1] = MFMA16(a_[i_], B1, acc[i_][1]);            \
    }                                                         \
    LOADS;                                                    \
    _Pragma("unroll") for (int i_ = 0; i_ < 8; ++i_) {        \
      __builtin_amdgcn_sched_group_barrier(0x8, 2, 0);        \
      __builtin_amdgcn_sched_group_barrier(0x20, 1, 0);       \
    }                                                         \
  } while (0)

#define NPRE_A 8
#define NPRE_B 4
#define BW_STEP 12
#define RESC_THR 6.0f
#define ST_AUX 16
#define ROLL
#define CODE_WARM 14
#define QDEPTH 2
constexpr int QD = QDEPTH;

__global__ __launch_bounds__(512, 2) void attn_kernel(const __hip_bfloat16* __restrict__ x,
                                                      const __hip_bfloat16* __restrict__ kc,
                                                      const __hip_bfloat16* __restrict__ vc,
                                                      const __hip_bfloat16* __restrict__ wq,
                                                      const __hip_bfloat16* __restrict__ wo,
                                                      char* __restrict__ ows) {
  extern __shared__ __attribute__((aligned(16))) char smem[];
  const int tid = threadIdx.x;
  const int wid = __builtin_amdgcn_readfirstlane(tid >> 6);
  const int lane = tid & 63;
  const int kvh = blockIdx.x & 7, b = blockIdx.x >> 3;
  const int hh = wid & 3, u = wid >> 2;
  const int r = lane & 31, h = lane >> 5;
  const int r16 = lane & 15, q = lane >> 4;
#define GRP_EXPR (b & 1)
#define ROT_EXPR ((b >> 1) & 15)
  const int grp = GRP_EXPR;
  const int rot = ROT_EXPR;
  STAMP(0);

  const unsigned lds0 = (unsigned)(size_t)smem;
  const size_t kvbase = ((size_t)(b * HKV + kvh)) * SK * DH;
  auto tile_isv = [](int t) { return t >= 14 ? 1 : (t < 2 ? 0 : (((t - 2) >> 1) & 1)); };
  auto tile_idx = [](int t) { return t >= 14 ? t - 8 : (t < 2 ? t : ((((t - 2) >> 1) & 1) ? 2 * ((t - 2) >> 2) + (t & 1) : 2 * ((t - 2) >> 2) + 2 + (t & 1))); };
  auto issue_tile = [&](int t) {
    const int slot = t < 10 ? t : t - 10;
    const int isv = tile_isv(t);
    const char* base = (const char*)((isv ? vc : kc) + kvbase + (size_t)tile_idx(t) * 64 * DH) + hh * 4096;
#pragma unroll
    for (int e = 0; e < 4; ++e) {
      const int chunk = isv ? (r16 ^ (q << 2)) : (r16 ^ (4 * e + q));
      glds16_nt(base + e * 1024, q * 256 + chunk * 16, lds0 + slot * 16384 + (4 * hh + e) * 1024);
    }
  };
  auto issue_tile8 = [&](int t) {
    const int slot = t < 10 ? t : t - 10;
    const int isv = tile_isv(t);
    const char* base = (const char*)((isv ? vc : kc) + kvbase + (size_t)tile_idx(t) * 64 * DH) + wid * 2048;
#pragma unroll
    for (int e = 0; e < 2; ++e) {
      const int chunk = isv ? (r16 ^ (q << 2)) : (r16 ^ (8 * (wid & 1) + 4 * e + q));
      glds16_nt(base + e * 1024, q * 256 + chunk * 16, lds0 + slot * 16384 + (2 * wid + e) * 1024);
    }
  };
  u32x4 cw = {0u, 0u, 0u, 0u};
  if (wid >= 6 && b + 32 * (7 - wid) < CODE_WARM) {
    unsigned long long pc;
    asm volatile("s_getpc_b64 %0" : "=s"(pc));
    const char* cp = (const char*)(pc & ~255ull) + (b + 32 * (7 - wid)) * 1024 + lane * 16;
    asm volatile("global_load_dwordx4 %0, %1, off" : "=v"(cw) : "v"(cp) : "memory");
  }
  {
    const __hip_bfloat16* xb = x + (size_t)b * SQ * DM;
#pragma unroll
    for (int i = 0; i < 4; ++i) {
      const int row = 4 * wid + i;
      glds16(xb + row * DM, (lane ^ (row & 15)) * 16, lds0 + QX_OFF + row * 1024);
    }
  }

  bf16x8 qf[8];
  if (u == 1) {
    if (grp == 0) {
#pragma unroll 1
      for (int t = 0; t < NPRE_A; ++t) issue_tile(t);
      asm volatile("s_waitcnt vmcnt(%0)" ::"i"(4 * NPRE_A) : "memory");
    } else {
      asm volatile("s_waitcnt vmcnt(0)" ::: "memory");
    }
    asm volatile("" ::"v"(cw));
    wg_barrier();
    wg_barrier();
    if (grp == 1) {
#pragma unroll 1
      for (int t = 0; t < NPRE_B; ++t) issue_tile(t);
    }
    wg_barrier();
    wg_barrier();
#pragma unroll
    for (int p = 0; p < 8; ++p)
      qf[p] = *reinterpret_cast<const bf16x8*>(smem + QX_OFF + (hh * 8 + p) * 1024 + lane * 16);
    if (grp == 0) asm volatile("s_waitcnt vmcnt(%0)" ::"i"(4 * NPRE_A - 8) : "memory");
    else asm volatile("s_waitcnt vmcnt(%0)" ::"i"(4 * NPRE_B - 8) : "memory");
  } else {
    const auto wq_rs = __builtin_amdgcn_make_buffer_rsrc((void*)wq, 0, DM * HQ * DH * 2, 0x00020000);
    const int w_voff = (8 * q) * 8192 + (kvh * 512 + hh * 128 + 8 * r16) * 2;
    u32x4 w[QD][8];
#define PRE_W 1
#pragma unroll
    for (int d = 0; d < PRE_W; ++d) {
      const int sr = (d + rot) & 15;
#pragma unroll
      for (int jj = 0; jj < 8; ++jj)
        w[d][jj] = __builtin_amdgcn_raw_buffer_load_b128(wq_rs, w_voff, (32 * sr + jj) * 8192, 0);
    }
    asm volatile("s_waitcnt vmcnt(%0)" ::"i"(8 * PRE_W) : "memory");
    wg_barrier();
#pragma unroll
    for (int d = PRE_W; d < QD; ++d) {
      const int sr = (d + rot) & 15;
#pragma unroll
      for (int jj = 0; jj < 8; ++jj)
        w[d][jj] = __builtin_amdgcn_raw_buffer_load_b128(wq_rs, w_voff, (32 * sr + jj) * 8192, 0);
    }
    STAMP(14);
    f32x4 acc[8][2];
#pragma unroll
    for (int i = 0; i < 8; ++i) { acc[i][0] = f32x4{}; acc[i][1] = f32x4{}; }
    const char* xs = smem + QX_OFF + r16 * 1024;
#define Q_STEP(S, BUF, LD)                                                                              \
  do {                                                                                                  \
    SBAR();                                                                                             \
    const int xo = ((4 * (((S) + rot) & 15) + q) ^ r16) << 4;                                           \
    const bf16x8 b0 = *reinterpret_cast<const bf16x8*>(xs + xo);                                        \
    const bf16x8 b1 = *reinterpret_cast<const bf16x8*>(xs + xo + 16384);                                \
    const int sr = ((S) + 2 + rot) & 15;                                                                \
    PROJ_STEP(w[BUF], b0, b1, if (LD) {                                                                 \
      _Pragma("unroll") for (int jj = 0; jj < 8; ++jj)                                                  \
        w[BUF][jj] = __builtin_amdgcn_raw_buffer_load_b128(rs_, w_voff, (32 * sr + jj) * 8192, 0);      \
    });                                                                                                 \
  } while (0)
#pragma unroll 1
    for (int it = 0; it < 8; ++it) {
      if (it == 7) __builtin_amdgcn_s_barrier();
      const auto rs_ = __builtin_amdgcn_make_buffer_rsrc((void*)wq, 0, it == 7 ? 0 : DM * HQ * DH * 2, 0x00020000);
      Q_STEP(2 * it, 0, true);
      Q_STEP(2 * it + 1, 1, true);
    }
#pragma unroll
    for (int g = 0; g < 4; ++g) {
      u32x4 f0, f1;
#pragma unroll
      for (int e = 0; e < 4; ++e) {
        const unsigned a0 = pk2(acc[2 * e][0][g], acc[2 * e + 1][0][g]);
        const unsigned a1 = pk2(acc[2 * e][1][g], acc[2 * e + 1][1][g]);
        auto rr = __builtin_amdgcn_permlane16_swap(a0, a1, false, false);
        f0[e] = rr[0];
        f1[e] = rr[1];
      }
      qf[g] = __builtin_bit_cast(bf16x8, f0);
      qf[4 + g] = __builtin_bit_cast(bf16x8, f1);
    }
    wg_barrier();
#pragma unroll
    for (int p = 0; p < 8; ++p)
      *reinterpret_cast<bf16x8*>(smem + QX_OFF + (hh * 8 + p) * 1024 + lane * 16) = qf[p];
    STAMP(1);
    wg_barrier();
  }

  constexpr float CS = 0.088388347648318447f * 1.4426950408889634f;
  float m = -__builtin_inff(), l = 0.f;
  f32x16 o[4];
#pragma unroll
  for (int i = 0; i < 4; ++i) o[i] = f32x16{};
  const int ky = (r & 15) ^ (8 * h);
  const int koff = r * 256;
  const int vq = (lane >> 2) & 3, vpp = lane & 3;
  const int voff = (4 * h + vq) * 256 + (2 * (q & 1) + (vpp >> 1)) * 16 + 8 * (vpp & 1);
  bf16x8 fb[16];
  bf16x8 pf[4];
  f32x16 s0, s1;
#define SLOT(T) (smem + (((T) < 10 ? (T) : (T) - 10) + u) * 16384)
#define KT(S) ((S) == 0 ? 0 : 4 * (S) - 2)
#define VT(S) ((S) == 3 ? 14 : 4 * (S) + 4)
#define K_READ(S, H)                                                                                        \
  do {                                                                                                      \
    const char* Ks = SLOT(KT(S));                                                                           \
    _Pragma("unroll") for (int p = 4 * (H); p < 4 * (H) + 4; ++p) {                                         \
      const char* a = Ks + koff + ((p ^ ky) << 4);                                                          \
      fb[2 * p] = *reinterpret_cast<const bf16x8*>(a);                                                      \
      fb[2 * p + 1] = *reinterpret_cast<const bf16x8*>(a + 8192);                                           \
    }                                                                                                       \
  } while (0)
#define QK_MMA()                                                                                            \
  do {                                                                                                      \
    s0 = f32x16{}; s1 = f32x16{};                                                                           \
    _Pragma("unroll") for (int p = 0; p < 8; ++p) {                                                         \
      s0 = MFMA32(fb[2 * p], qf[p], s0);                                                                    \
      s1 = MFMA32(fb[2 * p + 1], qf[p], s1);                                                                \
    }                                                                                                       \
  } while (0)
#define V_READ(S, DT)                                                                                       \
  do {                                                                                                      \
    const char* vb = SLOT(VT(S)) + voff + (((DT) ^ vq) << 6);                                         \
    _Pragma("unroll") for (int ks = 0; ks < 4; ++ks) {                                                      \
      const s16x4 lo = __builtin_amdgcn_ds_read_tr16_b64_v4i16((lds_s16x4_ptr)(vb + ks * 4096));            \
      const s16x4 hi = __builtin_amdgcn_ds_read_tr16_b64_v4i16((lds_s16x4_ptr)(vb + ks * 4096 + 2048));     \
      fb[(DT) * 4 + ks] = __builtin_bit_cast(bf16x8, __builtin_shufflevector(lo, hi, 0, 1, 2, 3, 4, 5, 6, 7)); \
    }                                                                                                       \
  } while (0)
#define PRIO(x)
#define PV_MMA(D0)                                                                                          \
  do {                                                                                                      \
    _Pragma("unroll") for (int ks = 0; ks < 4; ++ks)                                                        \
      _Pragma("unroll") for (int dt = (D0); dt < (D0) + 2; ++dt) o[dt] = MFMA32(fb[dt * 4 + ks], pf[ks], o[dt]); \
  } while (0)
#define SOFTMAX(S)                                                                                          \
  do {                                                                                                      \
    float tmax = s0[0];                                                                                     \
    _Pragma("unroll") for (int i = 1; i < 16; ++i) tmax = fmaxf(tmax, s0[i]);                               \
    _Pragma("unroll") for (int i = 0; i < 16; ++i) tmax = fmaxf(tmax, s1[i]);                               \
    tmax = swap_half_max(tmax);                                                                             \
      \
      \
    if ((S) == 0) {                                                                                         \
      m = tmax;                                                                                             \
    } else if (__builtin_amdgcn_ballot_w64((tmax - m) * CS > RESC_THR) != 0) {                              \
      const float mnew = fmaxf(m, tmax);                                                                    \
      const float alpha = __builtin_amdgcn_exp2f((m - mnew) * CS);                                          \
      m = mnew;                                                                                             \
      l *= alpha;                                                                                           \
      _Pragma("unroll") for (int dt = 0; dt < 4; ++dt)                                                      \
        _Pragma("unroll") for (int i = 0; i < 16; ++i) o[dt][i] *= alpha;                                   \
    }                                                                                                       \
    const float mc = m * CS;                                                                                \
    float ps = 0.f;                                                                                         \
    _Pragma("unroll") for (int i = 0; i < 16; ++i) { s0[i] = __builtin_amdgcn_exp2f(fmaf(s0[i], CS, -mc)); ps += s0[i]; } \
    _Pragma("unroll") for (int i = 0; i < 16; ++i) { s1[i] = __builtin_amdgcn_exp2f(fmaf(s1[i], CS, -mc)); ps += s1[i]; } \
    l += ps;                                                                                                \
    asm volatile("" : "+v"(l));                                                                             \
    _Pragma("unroll") for (int ss = 0; ss < 2; ++ss) {                                                      \
      u32x4 u0, u1;                                                                                         \
      _Pragma("unroll") for (int e = 0; e < 4; ++e) {                                                       \
        u0[e] = pk2(s0[8 * ss + 2 * e], s0[8 * ss + 2 * e + 1]);                                            \
        u1[e] = pk2(s1[8 * ss + 2 * e], s1[8 * ss + 2 * e + 1]);                                            \
      }                                                                                                     \
      pf[ss] = __builtin_bit_cast(bf16x8, u0);                                                              \
      pf[2 + ss] = __builtin_bit_cast(bf16x8, u1);                                                          \
    }                                                                                                       \
  } while (0)
#define DMA_STEP(S)                                                                                         \
  do {                                                                                                      \
    if ((S) < 3) {                                                                                          \
      const int t1_ = (S) == 2 ? 16 : 10 + 2 * (S);                                                         \
      _Pragma("unroll 1") for (int t_ = 8 + 2 * (S); t_ < t1_; ++t_) issue_tile8(t_);                       \
    } else if ((S) == 3) {                                                                                  \
                    \
      const char* wb = (const char*)wo + (size_t)(kvh * 512 + 16 * b + 2 * wid) * 1024;                     \
      glds16(wb, lane * 16, lds0 + 6 * 16384 + 2 * wid * 1024);                                             \
      glds16(wb + 1024, lane * 16, lds0 + 6 * 16384 + (2 * wid + 1) * 1024);                                \
    }                                                                                                       \
  } while (0)
#define STEP_WAIT(S)                                                                                        \
  do {                                                                                                      \
    if ((S) == 0) asm volatile("s_waitcnt vmcnt(12)" ::: "memory");                                         \
    if ((S) == 1 || (S) == 2) asm volatile("s_waitcnt vmcnt(4)" ::: "memory");                              \
    if ((S) == 3) asm volatile("s_waitcnt vmcnt(2)" ::: "memory");                                          \
  } while (0)

  char* mlp = smem + 7 * 16384;
  if (u == 1) {
    wg_barrier();
#pragma unroll 1
    for (int t = grp == 0 ? NPRE_A : NPRE_B; t < 8; ++t) issue_tile(t);
  } else {
    wg_barrier();
  }
#pragma unroll 1
  for (int i = 0; i < 5; ++i) {
    if (u == 0 && i > 0) wg_barrier();
    if (i > 0) {
      V_READ(i - 1, 0);
      V_READ(i - 1, 1);
      V_READ(i - 1, 2);
      V_READ(i - 1, 3);
    }
    if (u == 0 || i == 0) DMA_STEP(i);
    SBAR();
    if (i > 0) PV_MMA(0);
    SBAR();
    if (i < 4) K_READ(i, 0);
    SBAR();
    if (i > 0) PV_MMA(2);
    SBAR();
    if (i < 4) {
      K_READ(i, 1);
      SBAR();
      QK_MMA();
      SBAR();
      if (u == 1) {
        STEP_WAIT(i);
        wg_barrier();
        DMA_STEP(i + 1);
      }
      SOFTMAX(1);
      SBAR();
      if (u == 0) STEP_WAIT(i);
    }
  }
  l = swap_half_add(l);
  if (h == 0) *reinterpret_cast<f32x2*>(mlp + wid * 256 + r * 8) = f32x2{m, l};
  const auto wo_rs = __builtin_amdgcn_make_buffer_rsrc((void*)wo, 0, HQ * DH * DM * 2, 0x00020000);
  const int wo_voff = (kvh * 512 + 8 * q) * 1024 + (hh * 128 + 8 * r16) * 2;
  u32x4 w[QD][8];
  if (u == 0) {
#pragma unroll
    for (int d = 0; d < QD; ++d)
#pragma unroll
      for (int jj = 0; jj < 8; ++jj)
        w[d][jj] = __builtin_amdgcn_raw_buffer_load_b128(wo_rs, wo_voff, (32 * ((d + rot) & 15) + jj) * 1024, 0);
  }
  SBAR();
  wg_barrier();
  STAMP(10);
  {
    char* xb_ = smem + wid * 8192;
#pragma unroll
    for (int c = 0; c < 2; ++c) {
#pragma unroll
      for (int g = 0; g < 4; ++g) {
        f32x4 v;
        if (u == 0) v = f32x4{o[2 + c][4 * g], o[2 + c][4 * g + 1], o[2 + c][4 * g + 2], o[2 + c][4 * g + 3]};
        else v = f32x4{o[c][4 * g], o[c][4 * g + 1], o[c][4 * g + 2], o[c][4 * g + 3]};
        *reinterpret_cast<f32x4*>(xb_ + (c * 4 + g) * 1024 + lane * 16) = v;
      }
    }
    const f32x2 ml2 = *reinterpret_cast<const f32x2*>(mlp + (wid ^ 4) * 256 + r * 8);
    const float mm = fmaxf(m, ml2[0]);
    const float e1 = __builtin_amdgcn_exp2f((m - mm) * CS), e2 = __builtin_amdgcn_exp2f((ml2[0] - mm) * CS);
    const float inv = 1.0f / (l * e1 + ml2[1] * e2);
    const float f1 = e1 * inv, f2 = e2 * inv;
    wg_barrier();
    const char* pb = smem + (wid ^ 4) * 8192;
    char* of = smem + QX_OFF + (r >> 4) * 1024 + (r & 15) * 16 + 8 * h;
    f32x4 pv[8];
#pragma unroll
    for (int c = 0; c < 8; ++c) pv[c] = *reinterpret_cast<const f32x4*>(pb + c * 1024 + lane * 16);
    SBAR();
#pragma unroll
    for (int c = 0; c < 2; ++c) {
      const int dt = 2 * u + c;
#pragma unroll
      for (int g = 0; g < 4; ++g) {
        f32x4 ov;
        if (u == 0) ov = f32x4{o[c][4 * g], o[c][4 * g + 1], o[c][4 * g + 2], o[c][4 * g + 3]};
        else ov = f32x4{o[2 + c][4 * g], o[2 + c][4 * g + 1], o[2 + c][4 * g + 2], o[2 + c][4 * g + 3]};
        const f32x4 p2 = pv[c * 4 + g];
        u32x2 v;
        v[0] = pk2(ov[0] * f1 + p2[0] * f2, ov[1] * f1 + p2[1] * f2);
        v[1] = pk2(ov[2] * f1 + p2[2] * f2, ov[3] * f1 + p2[3] * f2);
        *reinterpret_cast<u32x2*>(of + (hh * 4 + dt) * 2048 + g * 256) = v;
      }
    }
  }
  if (u == 1) {
    asm volatile("s_waitcnt vmcnt(0)" ::: "memory");
    wg_barrier();
    return;
  }
  {
    wg_barrier();
    STAMP(11);
    f32x4 acc[8][2];
#pragma unroll
    for (int i = 0; i < 8; ++i) { acc[i][0] = f32x4{}; acc[i][1] = f32x4{}; }
    const char* of = smem + QX_OFF + lane * 16;
#define O_STEP(S, BUF, LD)                                                                              \
  do {                                                                                                  \
    SBAR();                                                                                             \
    const bf16x8 b0 = *reinterpret_cast<const bf16x8*>(of + (((S) + rot) & 15) * 2048);                 \
    const bf16x8 b1 = *reinterpret_cast<const bf16x8*>(of + (((S) + rot) & 15) * 2048 + 1024);          \
    PROJ_STEP(w[BUF], b0, b1, if (LD) {                                                                 \
      _Pragma("unroll") for (int jj = 0; jj < 8; ++jj)                                                  \
        w[BUF][jj] = __builtin_amdgcn_raw_buffer_load_b128(rs_, wo_voff, (32 * (((S) + 2 + rot) & 15) + jj) * 1024, 0); \
    });                                                                                                 \
  } while (0)
#pragma unroll 1
    for (int it = 0; it < 8; ++it) {
      const auto rs_ = __builtin_amdgcn_make_buffer_rsrc((void*)wo, 0, it == 7 ? 0 : HQ * DH * DM * 2, 0x00020000);
      O_STEP(2 * it, 0, true);
      O_STEP(2 * it + 1, 1, true);
    }
    STAMP(12);
    char* pp = ows + ((size_t)((kvh * NB + b) * 4 + hh)) * 8192 + lane * 16;
    const auto p_rs = __builtin_amdgcn_make_buffer_rsrc((void*)ows, 0, 8 << 20, 0x00020000);
    const int p_voff = ((kvh * NB + b) * 4 + hh) * 8192 + lane * 16;
#pragma unroll
    for (int T = 0; T < 2; ++T)
#pragma unroll
      for (int g = 0; g < 4; ++g) {
        f32x4 v0 = {acc[0][T][g], acc[1][T][g], acc[2][T][g], acc[3][T][g]};
        f32x4 v1 = {acc[4][T][g], acc[5][T][g], acc[6][T][g], acc[7][T][g]};
        u32x4 pk = {pk2(v0[0], v0[1]), pk2(v0[2], v0[3]), pk2(v1[0], v1[1]), pk2(v1[2], v1[3])};
        __builtin_amdgcn_raw_buffer_store_b128(pk, p_rs, p_voff, (T * 4 + g) * 1024, ST_AUX);
      }
    STAMP(13);
  }
}

#define RT 256
__global__ __launch_bounds__(RT) void reduce_kernel(const char* __restrict__ ows, __hip_bfloat16* __restrict__ out) {
  const int gid = blockIdx.x * RT + threadIdx.x;
  const int lane = gid & 63, tg = (gid >> 6) & 7, bc = gid >> 9;
  const char* pp = ows + (size_t)bc * 8192 + tg * 1024 + lane * 16;
  u32x4 v[8];
#pragma unroll
  for (int k = 0; k < 8; ++k) v[k] = *reinterpret_cast<const u32x4*>(pp + (size_t)k * (NB * 4 * 8192));
  f32x4 a = {0.f, 0.f, 0.f, 0.f}, c = {0.f, 0.f, 0.f, 0.f};
#pragma unroll
  for (int k = 0; k < 8; ++k) {
    a[0] += __uint_as_float(v[k][0] << 16); a[1] += __uint_as_float(v[k][0] & 0xffff0000u);
    a[2] += __uint_as_float(v[k][1] << 16); a[3] += __uint_as_float(v[k][1] & 0xffff0000u);
    c[0] += __uint_as_float(v[k][2] << 16); c[1] += __uint_as_float(v[k][2] & 0xffff0000u);
    c[2] += __uint_as_float(v[k][3] << 16); c[3] += __uint_as_float(v[k][3] & 0xffff0000u);
  }
  const int b = bc >> 2, cg = bc & 3, T = tg >> 2, g = tg & 3, r16 = lane & 15, q = lane >> 4;
  u32x4 o = {pk2(a[0], a[1]), pk2(a[2], a[3]), pk2(c[0], c[1]), pk2(c[2], c[3])};
  *reinterpret_cast<u32x4*>(out + (size_t)(b * SQ + 16 * T + r16) * DM + 128 * cg + 32 * q + 8 * g) = o;
}

extern "C" void kernel_launch(void* const* d_in, const int* in_sizes, int n_in, void* d_out, int out_size,
                              void* d_ws, size_t ws_size, hipStream_t stream) {
  const __hip_bfloat16* x = (const __hip_bfloat16*)d_in[0];
  const __hip_bfloat16* kc = (const __hip_bfloat16*)d_in[1];
  const __hip_bfloat16* vc = (const __hip_bfloat16*)d_in[2];
  const __hip_bfloat16* wq = (const __hip_bfloat16*)d_in[3];
  const __hip_bfloat16* wo = (const __hip_bfloat16*)d_in[4];
  (void)hipFuncSetAttribute((const void*)attn_kernel, hipFuncAttributeMaxDynamicSharedMemorySize, LDS_BYTES);
  attn_kernel<<<256, 512, LDS_BYTES, stream>>>(x, kc, vc, wq, wo, (char*)d_ws);
  reduce_kernel<<<65536 / RT, RT, 0, stream>>>((const char*)d_ws, (__hip_bfloat16*)d_out);
}
```

```cpp
#include <hip/hip_runtime.h>
#include <hip/hip_bf16.h>

typedef __bf16 bf16x8 __attribute__((ext_vector_type(8)));
typedef __bf16 bf16x2 __attribute__((ext_vector_type(2)));
typedef short s16x4 __attribute__((ext_vector_type(4)));
typedef float f32x2 __attribute__((ext_vector_type(2)));
typedef float f32x4 __attribute__((ext_vector_type(4)));
typedef float f32x16 __attribute__((ext_vector_type(16)));
typedef unsigned u32x2 __attribute__((ext_vector_type(2)));
typedef unsigned u32x4 __attribute__((ext_vector_type(4)));
typedef __attribute__((address_space(3))) s16x4* lds_s16x4_ptr;

#define DI __device__ __forceinline__
#define MFMA32(a, b, c) __builtin_amdgcn_mfma_f32_32x32x16_bf16((a), (b), (c), 0, 0, 0)
#define MFMA16(a, b, c) __builtin_amdgcn_mfma_f32_16x16x32_bf16((a), (b), (c), 0, 0, 0)
#define SBAR() __builtin_amdgcn_sched_barrier(0)
#define STAMP(i)

constexpr int NB = 32, SQ = 32, DM = 512, HQ = 32, HKV = 8, SK = 512, DH = 128;
constexpr int LDS_BYTES = 163840;
constexpr int QX_OFF = 131072;

DI unsigned pk2(float a, float b) {
  f32x2 f = {a, b};
  return __builtin_bit_cast(unsigned, __builtin_convertvector(f, bf16x2));
}
DI float swap_half_max(float v) {
  auto rr = __builtin_amdgcn_permlane32_swap(__float_as_uint(v), __float_as_uint(v), false, false);
  return fmaxf(__uint_as_float(rr[0]), __uint_as_float(rr[1]));
}
DI float swap_half_add(float v) {
  auto rr = __builtin_amdgcn_permlane32_swap(__float_as_uint(v), __float_as_uint(v), false, false);
  return __uint_as_float(rr[0]) + __uint_as_float(rr[1]);
}
DI void wg_barrier() {
  asm volatile("s_waitcnt lgkmcnt(0)" ::: "memory");
  __builtin_amdgcn_s_barrier();
  asm volatile("" ::: "memory");
}
DI void glds16_nt(const void* sbase, unsigned voff, unsigned lds_dst) {
  unsigned keep;
  asm volatile("s_mov_b32 %0, m0\n\ts_mov_b32 m0, %3\n\ts_nop 0\n\tglobal_load_lds_dwordx4 %1, %2 nt\n\ts_mov_b32 m0, %0"
               : "=&s"(keep) : "v"(voff), "s"(sbase), "s"(lds_dst) : "memory");
}
DI void glds16(const void* sbase, unsigned voff, unsigned lds_dst) {
  unsigned keep;
  asm volatile("s_mov_b32 %0, m0\n\ts_mov_b32 m0, %3\n\ts_nop 0\n\tglobal_load_lds_dwordx4 %1, %2\n\ts_mov_b32 m0, %0"
               : "=&s"(keep) : "v"(voff), "s"(sbase), "s"(lds_dst) : "memory");
}
template <int I>
DI bf16x8 tr_frag(const u32x4* w) {
  u32x4 o;
  constexpr unsigned sel = (I & 1) ? 0x07060302u : 0x05040100u;
#pragma unroll
  for (int m = 0; m < 4; ++m) o[m] = __builtin_amdgcn_perm(w[2 * m + 1][I >> 1], w[2 * m][I >> 1], sel);
  return __builtin_bit_cast(bf16x8, o);
}
#define PROJ_STEP(W, B0, B1, LOADS)                           \
  do {                                                        \
    bf16x8 a_[8];                                             \
    a_[0] = tr_frag<0>(W); a_[1] = tr_frag<1>(W); a_[2] = tr_frag<2>(W); a_[3] = tr_frag<3>(W); \
    a_[4] = tr_frag<4>(W); a_[5] = tr_frag<5>(W); a_[6] = tr_frag<6>(W); a_[7] = tr_frag<7>(W); \
    SBAR();                                                   \
    _Pragma("unroll") for (int i_ = 0; i_ < 8; ++i_) {        \
      acc[i_][0] = MFMA16(a_[i_], B0, acc[i_][0]);            \
      acc[i_][1] = MFMA16(a_[i_], B1, acc[i_][1]);            \
    }                                                         \
    LOADS;                                                    \
    _Pragma("unroll") for (int i_ = 0; i_ < 8; ++i_) {        \
      __builtin_amdgcn_sched_group_barrier(0x8, 2, 0);        \
      __builtin_amdgcn_sched_group_barrier(0x20, 1, 0);       \
    }                                                         \
  } while (0)

#define NPRE_A 8
#define NPRE_B 4
#define BW_STEP 12
#define RESC_THR 6.0f
#define ST_AUX 16
#define ROLL
#define CODE_WARM 18
#define QDEPTH 2
constexpr int QD = QDEPTH;

__global__ __launch_bounds__(512, 2) void attn_kernel(const __hip_bfloat16* __restrict__ x,
                                                      const __hip_bfloat16* __restrict__ kc,
                                                      const __hip_bfloat16* __restrict__ vc,
                                                      const __hip_bfloat16* __restrict__ wq,
                                                      const __hip_bfloat16* __restrict__ wo,
                                                      char* __restrict__ ows) {
  extern __shared__ __attribute__((aligned(16))) char smem[];
  const int tid = threadIdx.x;
  const int wid = __builtin_amdgcn_readfirstlane(tid >> 6);
  const int lane = tid & 63;
  const int kvh = blockIdx.x & 7, b = blockIdx.x >> 3;
  const int hh = wid & 3, u = wid >> 2;
  const int r = lane & 31, h = lane >> 5;
  const int r16 = lane & 15, q = lane >> 4;
#define GRP_EXPR (b & 1)
#define ROT_EXPR ((b >> 1) & 15)
  const int grp = GRP_EXPR;
  const int rot = ROT_EXPR;
  STAMP(0);

  const unsigned lds0 = (unsigned)(size_t)smem;
  const size_t kvbase = ((size_t)(b * HKV + kvh)) * SK * DH;
  auto tile_isv = [](int t) { return t >= 14 ? 1 : (t < 2 ? 0 : (((t - 2) >> 1) & 1)); };
  auto tile_idx = [](int t) { return t >= 14 ? t - 8 : (t < 2 ? t : ((((t - 2) >> 1) & 1) ? 2 * ((t - 2) >> 2) + (t & 1) : 2 * ((t - 2) >> 2) + 2 + (t & 1))); };
  auto issue_tile = [&](int t) {
    const int slot = t < 10 ? t : t - 10;
    const int isv = tile_isv(t);
    const char* base = (const char*)((isv ? vc : kc) + kvbase + (size_t)tile_idx(t) * 64 * DH) + hh * 4096;
#pragma unroll
    for (int e = 0; e < 4; ++e) {
      const int chunk = isv ? (r16 ^ (q << 2)) : (r16 ^ (4 * e + q));
      glds16_nt(base + e * 1024, q * 256 + chunk * 16, lds0 + slot * 16384 + (4 * hh + e) * 1024);
    }
  };
  auto issue_tile8 = [&](int t) {
    const int slot = t < 10 ? t : t - 10;
    const int isv = tile_isv(t);
    const char* base = (const char*)((isv ? vc : kc) + kvbase + (size_t)tile_idx(t) * 64 * DH) + wid * 2048;
#pragma unroll
    for (int e = 0; e < 2; ++e) {
      const int chunk = isv ? (r16 ^ (q << 2)) : (r16 ^ (8 * (wid & 1) + 4 * e + q));
      glds16_nt(base + e * 1024, q * 256 + chunk * 16, lds0 + slot * 16384 + (2 * wid + e) * 1024);
    }
  };
  u32x4 cw = {0u, 0u, 0u, 0u};
  if (wid >= 6 && b + 32 * (7 - wid) < CODE_WARM) {
    unsigned long long pc;
    asm volatile("s_getpc_b64 %0" : "=s"(pc));
    const char* cp = (const char*)(pc & ~255ull) + (b + 32 * (7 - wid)) * 1024 + lane * 16;
    asm volatile("global_load_dwordx4 %0, %1, off" : "=v"(cw) : "v"(cp) : "memory");
  }
  {
    const __hip_bfloat16* xb = x + (size_t)b * SQ * DM;
#pragma unroll
    for (int i = 0; i < 4; ++i) {
      const int row = 4 * wid + i;
      glds16(xb + row * DM, (lane ^ (row & 15)) * 16, lds0 + QX_OFF + row * 1024);
    }
  }

  bf16x8 qf[8];
  if (u == 1) {
    if (grp == 0) {
#pragma unroll 1
      for (int t = 0; t < NPRE_A; ++t) issue_tile(t);
      asm volatile("s_waitcnt vmcnt(%0)" ::"i"(4 * NPRE_A) : "memory");
    } else {
      asm volatile("s_waitcnt vmcnt(0)" ::: "memory");
    }
    asm volatile("" ::"v"(cw));
    wg_barrier();
    wg_barrier();
    if (grp == 1) {
#pragma unroll 1
      for (int t = 0; t < NPRE_B; ++t) issue_tile(t);
    }
    wg_barrier();
    wg_barrier();
#pragma unroll
    for (int p = 0; p < 8; ++p)
      qf[p] = *reinterpret_cast<const bf16x8*>(smem + QX_OFF + (hh * 8 + p) * 1024 + lane * 16);
    if (grp == 0) asm volatile("s_waitcnt vmcnt(%0)" ::"i"(4 * NPRE_A - 8) : "memory");
    else asm volatile("s_waitcnt vmcnt(%0)" ::"i"(4 * NPRE_B - 8) : "memory");
  } else {
    const auto wq_rs = __builtin_amdgcn_make_buffer_rsrc((void*)wq, 0, DM * HQ * DH * 2, 0x00020000);
    const int w_voff = (8 * q) * 8192 + (kvh * 512 + hh * 128 + 8 * r16) * 2;
    u32x4 w[QD][8];
#define PRE_W 1
#pragma unroll
    for (int d = 0; d < PRE_W; ++d) {
      const int sr = (d + rot) & 15;
#pragma unroll
      for (int jj = 0; jj < 8; ++jj)
        w[d][jj] = __builtin_amdgcn_raw_buffer_load_b128(wq_rs, w_voff, (32 * sr + jj) * 8192, 0);
    }
    asm volatile("s_waitcnt vmcnt(%0)" ::"i"(8 * PRE_W) : "memory");
    wg_barrier();
#pragma unroll
    for (int d = PRE_W; d < QD; ++d) {
      const int sr = (d + rot) & 15;
#pragma unroll
      for (int jj = 0; jj < 8; ++jj)
        w[d][jj] = __builtin_amdgcn_raw_buffer_load_b128(wq_rs, w_voff, (32 * sr + jj) * 8192, 0);
    }
    STAMP(14);
    f32x4 acc[8][2];
#pragma unroll
    for (int i = 0; i < 8; ++i) { acc[i][0] = f32x4{}; acc[i][1] = f32x4{}; }
    const char* xs = smem + QX_OFF + r16 * 1024;
#define Q_STEP(S, BUF, LD)                                                                              \
  do {                                                                                                  \
    SBAR();                                                                                             \
    const int xo = ((4 * (((S) + rot) & 15) + q) ^ r16) << 4;                                           \
    const bf16x8 b0 = *reinterpret_cast<const bf16x8*>(xs + xo);                                        \
    const bf16x8 b1 = *reinterpret_cast<const bf16x8*>(xs + xo + 16384);                                \
    const int sr = ((S) + 2 + rot) & 15;                                                                \
    PROJ_STEP(w[BUF], b0, b1, if (LD) {                                                                 \
      _Pragma("unroll") for (int jj = 0; jj < 8; ++jj)                                                  \
        w[BUF][jj] = __builtin_amdgcn_raw_buffer_load_b128(wq_rs, w_voff, (32 * sr + jj) * 8192, 0);    \
    });                                                                                                 \
  } while (0)
#pragma unroll 1
    for (int it = 0; it < 7; ++it) {
      Q_STEP(2 * it, 0, true);
      Q_STEP(2 * it + 1, 1, true);
    }
    SBAR();
    __builtin_amdgcn_s_barrier();
    Q_STEP(14, 0, false);
    Q_STEP(15, 1, false);
#pragma unroll
    for (int g = 0; g < 4; ++g) {
      u32x4 f0, f1;
#pragma unroll
      for (int e = 0; e < 4; ++e) {
        const unsigned a0 = pk2(acc[2 * e][0][g], acc[2 * e + 1][0][g]);
        const unsigned a1 = pk2(acc[2 * e][1][g], acc[2 * e + 1][1][g]);
        auto rr = __builtin_amdgcn_permlane16_swap(a0, a1, false, false);
        f0[e] = rr[0];
        f1[e] = rr[1];
      }
      qf[g] = __builtin_bit_cast(bf16x8, f0);
      qf[4 + g] = __builtin_bit_cast(bf16x8, f1);
    }
    wg_barrier();
#pragma unroll
    for (int p = 0; p < 8; ++p)
      *reinterpret_cast<bf16x8*>(smem + QX_OFF + (hh * 8 + p) * 1024 + lane * 16) = qf[p];
    STAMP(1);
    wg_barrier();
  }

  constexpr float CS = 0.088388347648318447f * 1.4426950408889634f;
  float m = -__builtin_inff(), l = 0.f;
  f32x16 o[4];
#pragma unroll
  for (int i = 0; i < 4; ++i) o[i] = f32x16{};
  const int ky = (r & 15) ^ (8 * h);
  const int koff = r * 256;
  const int vq = (lane >> 2) & 3, vpp = lane & 3;
  const int voff = (4 * h + vq) * 256 + (2 * (q & 1) + (vpp >> 1)) * 16 + 8 * (vpp & 1);
  bf16x8 fb[16];
  bf16x8 pf[4];
  f32x16 s0, s1;
#define SLOT(T) (smem + (((T) < 10 ? (T) : (T) - 10) + u) * 16384)
#define KT(S) ((S) == 0 ? 0 : 4 * (S) - 2)
#define VT(S) ((S) == 3 ? 14 : 4 * (S) + 4)
#define K_READ(S, H)                                                                                        \
  do {                                                                                                      \
    const char* Ks = SLOT(KT(S));                                                                           \
    _Pragma("unroll") for (int p = 4 * (H); p < 4 * (H) + 4; ++p) {                                         \
      const char* a = Ks + koff + ((p ^ ky) << 4);                                                          \
      fb[2 * p] = *reinterpret_cast<const bf16x8*>(a);                                                      \
      fb[2 * p + 1] = *reinterpret_cast<const bf16x8*>(a + 8192);                                           \
    }                                                                                                       \
  } while (0)
#define QK_MMA()                                                                                            \
  do {                                                                                                      \
    s0 = f32x16{}; s1 = f32x16{};                                                                           \
    _Pragma("unroll") for (int p = 0; p < 8; ++p) {                                                         \
      s0 = MFMA32(fb[2 * p], qf[p], s0);                                                                    \
      s1 = MFMA32(fb[2 * p + 1], qf[p], s1);                                                                \
    }                                                                                                       \
  } while (0)
#define V_READ(S, DT)                                                                                       \
  do {                                                                                                      \
    const char* vb = SLOT(VT(S)) + voff + (((DT) ^ vq) << 6);                                         \
    _Pragma("unroll") for (int ks = 0; ks < 4; ++ks) {                                                      \
      const s16x4 lo = __builtin_amdgcn_ds_read_tr16_b64_v4i16((lds_s16x4_ptr)(vb + ks * 4096));            \
      const s16x4 hi = __builtin_amdgcn_ds_read_tr16_b64_v4i16((lds_s16x4_ptr)(vb + ks * 4096 + 2048));     \
      fb[(DT) * 4 + ks] = __builtin_bit_cast(bf16x8, __builtin_shufflevector(lo, hi, 0, 1, 2, 3, 4, 5, 6, 7)); \
    }                                                                                                       \
  } while (0)
#define PRIO(x)
#define PV_MMA(D0)                                                                                          \
  do {                                                                                                      \
    _Pragma("unroll") for (int ks = 0; ks < 4; ++ks)                                                        \
      _Pragma("unroll") for (int dt = (D0); dt < (D0) + 2; ++dt) o[dt] = MFMA32(fb[dt * 4 + ks], pf[ks], o[dt]); \
  } while (0)
#define SOFTMAX(S)                                                                                          \
  do {                                                                                                      \
    float tmax = s0[0];                                                                                     \
    _Pragma("unroll") for (int i = 1; i < 16; ++i) tmax = fmaxf(tmax, s0[i]);                               \
    _Pragma("unroll") for (int i = 0; i < 16; ++i) tmax = fmaxf(tmax, s1[i]);                               \
    tmax = swap_half_max(tmax);                                                                             \
      \
      \
    if ((S) == 0) {                                                                                         \
      m = tmax;                                                                                             \
    } else if (__builtin_amdgcn_ballot_w64((tmax - m) * CS > RESC_THR) != 0) {                              \
      const float mnew = fmaxf(m, tmax);                                                                    \
      const float alpha = __builtin_amdgcn_exp2f((m - mnew) * CS);                                          \
      m = mnew;                                                                                             \
      l *= alpha;                                                                                           \
      _Pragma("unroll") for (int dt = 0; dt < 4; ++dt)                                                      \
        _Pragma("unroll") for (int i = 0; i < 16; ++i) o[dt][i] *= alpha;                                   \
    }                                                                                                       \
    const float mc = m * CS;                                                                                \
    float ps = 0.f;                                                                                         \
    _Pragma("unroll") for (int i = 0; i < 16; ++i) { s0[i] = __builtin_amdgcn_exp2f(fmaf(s0[i], CS, -mc)); ps += s0[i]; } \
    _Pragma("unroll") for (int i = 0; i < 16; ++i) { s1[i] = __builtin_amdgcn_exp2f(fmaf(s1[i], CS, -mc)); ps += s1[i]; } \
    l += ps;                                                                                                \
    asm volatile("" : "+v"(l));                                                                             \
    _Pragma("unroll") for (int ss = 0; ss < 2; ++ss) {                                                      \
      u32x4 u0, u1;                                                                                         \
      _Pragma("unroll") for (int e = 0; e < 4; ++e) {                                                       \
        u0[e] = pk2(s0[8 * ss + 2 * e], s0[8 * ss + 2 * e + 1]);                                            \
        u1[e] = pk2(s1[8 * ss + 2 * e], s1[8 * ss + 2 * e + 1]);                                            \
      }                                                                                                     \
      pf[ss] = __builtin_bit_cast(bf16x8, u0);                                                              \
      pf[2 + ss] = __builtin_bit_cast(bf16x8, u1);                                                          \
    }                                                                                                       \
  } while (0)
#define DMA_A(S)                                                                                            \
  do {                                                                                                      \
    if ((S) == 0) issue_tile8(8);                                                                           \
    if ((S) == 1) issue_tile8(10);                                                                          \
    if ((S) == 2) { issue_tile8(12); issue_tile8(13); }                                                     \
    if ((S) == 3) {                                                                                         \
                    \
      const char* wb = (const char*)wo + (size_t)(kvh * 512 + 16 * b + 2 * wid) * 1024;                     \
      glds16(wb, lane * 16, lds0 + 6 * 16384 + 2 * wid * 1024);                                             \
      glds16(wb + 1024, lane * 16, lds0 + 6 * 16384 + (2 * wid + 1) * 1024);                                \
    }                                                                                                       \
  } while (0)
#define DMA_B(S)                                                                                            \
  do {                                                                                                      \
    if ((S) == 0) issue_tile8(9);                                                                           \
    if ((S) == 1) issue_tile8(11);                                                                          \
    if ((S) == 2) { issue_tile8(14); issue_tile8(15); }                                                     \
  } while (0)
#define STEP_WAIT(S)                                                                                        \
  do {                                                                                                      \
    if ((S) == 0) asm volatile("s_waitcnt vmcnt(12)" ::: "memory");                                         \
    if ((S) == 1 || (S) == 2) asm volatile("s_waitcnt vmcnt(4)" ::: "memory");                              \
    if ((S) == 3) asm volatile("s_waitcnt vmcnt(2)" ::: "memory");                                          \
  } while (0)

  char* mlp = smem + 7 * 16384;
  if (u == 1) {
    wg_barrier();
#pragma unroll 1
    for (int t = grp == 0 ? NPRE_A : NPRE_B; t < 8; ++t) issue_tile(t);
  } else {
    wg_barrier();
  }
  STAMP(2);
  K_READ(0, 0);
  K_READ(0, 1);
  DMA_A(0);
  DMA_B(0);
  SBAR();
  QK_MMA();
  SBAR();
  if (u == 1) {
    STEP_WAIT(0);
    wg_barrier();
    DMA_A(1);
    DMA_B(1);
  }
  SOFTMAX(0);
  SBAR();
  if (u == 0) STEP_WAIT(0);
#pragma unroll 1
  for (int i = 1; i < 4; ++i) {
    if (u == 0) wg_barrier();
    V_READ(i - 1, 0);
    V_READ(i - 1, 1);
    V_READ(i - 1, 2);
    V_READ(i - 1, 3);
    if (u == 0) { DMA_A(i); DMA_B(i); }
    SBAR();
    PV_MMA(0);
    SBAR();
    K_READ(i, 0);
    SBAR();
    PV_MMA(2);
    SBAR();
    K_READ(i, 1);
    SBAR();
    QK_MMA();
    SBAR();
    if (u == 1) {
      STEP_WAIT(i);
      wg_barrier();
      DMA_A(i + 1);
      DMA_B(i + 1);
    }
    SOFTMAX(1);
    SBAR();
    if (u == 0) STEP_WAIT(i);
  }
  l = swap_half_add(l);
  if (h == 0) *reinterpret_cast<f32x2*>(mlp + wid * 256 + r * 8) = f32x2{m, l};
  if (u == 0) wg_barrier();
  STAMP(9);
  V_READ(3, 0);
  V_READ(3, 1);
  V_READ(3, 2);
  V_READ(3, 3);
  SBAR();
  PV_MMA(0);
  PV_MMA(2);
  SBAR();
  const auto wo_rs = __builtin_amdgcn_make_buffer_rsrc((void*)wo, 0, HQ * DH * DM * 2, 0x00020000);
  const int wo_voff = (kvh * 512 + 8 * q) * 1024 + (hh * 128 + 8 * r16) * 2;
  u32x4 w[QD][8];
  if (u == 0) {
#pragma unroll
    for (int d = 0; d < QD; ++d)
#pragma unroll
      for (int jj = 0; jj < 8; ++jj)
        w[d][jj] = __builtin_amdgcn_raw_buffer_load_b128(wo_rs, wo_voff, (32 * ((d + rot) & 15) + jj) * 1024, 0);
  }
  SBAR();
  wg_barrier();
  STAMP(10);
  {
    char* xb_ = smem + wid * 8192;
#pragma unroll
    for (int c = 0; c < 2; ++c) {
#pragma unroll
      for (int g = 0; g < 4; ++g) {
        f32x4 v;
        if (u == 0) v = f32x4{o[2 + c][4 * g], o[2 + c][4 * g + 1], o[2 + c][4 * g + 2], o[2 + c][4 * g + 3]};
        else v = f32x4{o[c][4 * g], o[c][4 * g + 1], o[c][4 * g + 2], o[c][4 * g + 3]};
        *reinterpret_cast<f32x4*>(xb_ + (c * 4 + g) * 1024 + lane * 16) = v;
      }
    }
    const f32x2 ml2 = *reinterpret_cast<const f32x2*>(mlp + (wid ^ 4) * 256 + r * 8);
    const float mm = fmaxf(m, ml2[0]);
    const float e1 = __builtin_amdgcn_exp2f((m - mm) * CS), e2 = __builtin_amdgcn_exp2f((ml2[0] - mm) * CS);
    const float inv = 1.0f / (l * e1 + ml2[1] * e2);
    const float f1 = e1 * inv, f2 = e2 * inv;
    wg_barrier();
    const char* pb = smem + (wid ^ 4) * 8192;
    char* of = smem + QX_OFF + (r >> 4) * 1024 + (r & 15) * 16 + 8 * h;
    f32x4 pv[8];
#pragma unroll
    for (int c = 0; c < 8; ++c) pv[c] = *reinterpret_cast<const f32x4*>(pb + c * 1024 + lane * 16);
    SBAR();
#pragma unroll
    for (int c = 0; c < 2; ++c) {
      const int dt = 2 * u + c;
#pragma unroll
      for (int g = 0; g < 4; ++g) {
        f32x4 ov;
        if (u == 0) ov = f32x4{o[c][4 * g], o[c][4 * g + 1], o[c][4 * g + 2], o[c][4 * g + 3]};
        else ov = f32x4{o[2 + c][4 * g], o[2 + c][4 * g + 1], o[2 + c][4 * g + 2], o[2 + c][4 * g + 3]};
        const f32x4 p2 = pv[c * 4 + g];
        u32x2 v;
        v[0] = pk2(ov[0] * f1 + p2[0] * f2, ov[1] * f1 + p2[1] * f2);
        v[1] = pk2(ov[2] * f1 + p2[2] * f2, ov[3] * f1 + p2[3] * f2);
        *reinterpret_cast<u32x2*>(of + (hh * 4 + dt) * 2048 + g * 256) = v;
      }
    }
  }
  if (u == 1) {
    asm volatile("s_waitcnt vmcnt(0)" ::: "memory");
    wg_barrier();
    return;
  }
  {
    wg_barrier();
    STAMP(11);
    f32x4 acc[8][2];
#pragma unroll
    for (int i = 0; i < 8; ++i) { acc[i][0] = f32x4{}; acc[i][1] = f32x4{}; }
    const char* of = smem + QX_OFF + lane * 16;
#define O_STEP(S, BUF, LD)                                                                              \
  do {                                                                                                  \
    SBAR();                                                                                             \
    const bf16x8 b0 = *reinterpret_cast<const bf16x8*>(of + (((S) + rot) & 15) * 2048);                 \
    const bf16x8 b1 = *reinterpret_cast<const bf16x8*>(of + (((S) + rot) & 15) * 2048 + 1024);          \
    PROJ_STEP(w[BUF], b0, b1, if (LD) {                                                                 \
      _Pragma("unroll") for (int jj = 0; jj < 8; ++jj)                                                  \
        w[BUF][jj] = __builtin_amdgcn_raw_buffer_load_b128(wo_rs, wo_voff, (32 * (((S) + 2 + rot) & 15) + jj) * 1024, 0); \
    });                                                                                                 \
  } while (0)
#pragma unroll 1
    for (int it = 0; it < 7; ++it) {
      O_STEP(2 * it, 0, true);
      O_STEP(2 * it + 1, 1, true);
    }
    O_STEP(14, 0, false);
    O_STEP(15, 1, false);
    STAMP(12);
    char* pp = ows + ((size_t)((kvh * NB + b) * 4 + hh)) * 8192 + lane * 16;
    const auto p_rs = __builtin_amdgcn_make_buffer_rsrc((void*)ows, 0, 8 << 20, 0x00020000);
    const int p_voff = ((kvh * NB + b) * 4 + hh) * 8192 + lane * 16;
#pragma unroll
    for (int T = 0; T < 2; ++T)
#pragma unroll
      for (int g = 0; g < 4; ++g) {
        f32x4 v0 = {acc[0][T][g], acc[1][T][g], acc[2][T][g], acc[3][T][g]};
        f32x4 v1 = {acc[4][T][g], acc[5][T][g], acc[6][T][g], acc[7][T][g]};
        u32x4 pk = {pk2(v0[0], v0[1]), pk2(v0[2], v0[3]), pk2(v1[0], v1[1]), pk2(v1[2], v1[3])};
        __builtin_amdgcn_raw_buffer_store_b128(pk, p_rs, p_voff, (T * 4 + g) * 1024, ST_AUX);
      }
    STAMP(13);
  }
}

#define RT 256
__global__ __launch_bounds__(RT) void reduce_kernel(const char* __restrict__ ows, __hip_bfloat16* __restrict__ out) {
  const int gid = blockIdx.x * RT + threadIdx.x;
  const int lane = gid & 63, tg = (gid >> 6) & 7, bc = gid >> 9;
  const char* pp = ows + (size_t)bc * 8192 + tg * 1024 + lane * 16;
  u32x4 v[8];
#pragma unroll
  for (int k = 0; k < 8; ++k) v[k] = *reinterpret_cast<const u32x4*>(pp + (size_t)k * (NB * 4 * 8192));
  f32x4 a = {0.f, 0.f, 0.f, 0.f}, c = {0.f, 0.f, 0.f, 0.f};
#pragma unroll
  for (int k = 0; k < 8; ++k) {
    a[0] += __uint_as_float(v[k][0] << 16); a[1] += __uint_as_float(v[k][0] & 0xffff0000u);
    a[2] += __uint_as_float(v[k][1] << 16); a[3] += __uint_as_float(v[k][1] & 0xffff0000u);
    c[0] += __uint_as_float(v[k][2] << 16); c[1] += __uint_as_float(v[k][2] & 0xffff0000u);
    c[2] += __uint_as_float(v[k][3] << 16); c[3] += __uint_as_float(v[k][3] & 0xffff0000u);
  }
  const int b = bc >> 2, cg = bc & 3, T = tg >> 2, g = tg & 3, r16 = lane & 15, q = lane >> 4;
  u32x4 o = {pk2(a[0], a[1]), pk2(a[2], a[3]), pk2(c[0], c[1]), pk2(c[2], c[3])};
  *reinterpret_cast<u32x4*>(out + (size_t)(b * SQ + 16 * T + r16) * DM + 128 * cg + 32 * q + 8 * g) = o;
}

extern "C" void kernel_launch(void* const* d_in, const int* in_sizes, int n_in, void* d_out, int out_size,
                              void* d_ws, size_t ws_size, hipStream_t stream) {
  const __hip_bfloat16* x = (const __hip_bfloat16*)d_in[0];
  const __hip_bfloat16* kc = (const __hip_bfloat16*)d_in[1];
  const __hip_bfloat16* vc = (const __hip_bfloat16*)d_in[2];
  const __hip_bfloat16* wq = (const __hip_bfloat16*)d_in[3];
  const __hip_bfloat16* wo = (const __hip_bfloat16*)d_in[4];
  (void)hipFuncSetAttribute((const void*)attn_kernel, hipFuncAttributeMaxDynamicSharedMemorySize, LDS_BYTES);
  attn_kernel<<<256, 512, LDS_BYTES, stream>>>(x, kc, vc, wq, wo, (char*)d_ws);
  reduce_kernel<<<65536 / RT, RT, 0, stream>>>((const char*)d_ws, (__hip_bfloat16*)d_out);
}
```

```cpp
#include <hip/hip_runtime.h>
#include <hip/hip_bf16.h>

typedef __bf16 bf16x8 __attribute__((ext_vector_type(8)));
typedef __bf16 bf16x2 __attribute__((ext_vector_type(2)));
typedef short s16x4 __attribute__((ext_vector_type(4)));
typedef float f32x2 __attribute__((ext_vector_type(2)));
typedef float f32x4 __attribute__((ext_vector_type(4)));
typedef float f32x16 __attribute__((ext_vector_type(16)));
typedef unsigned u32x2 __attribute__((ext_vector_type(2)));
typedef unsigned u32x4 __attribute__((ext_vector_type(4)));
typedef __attribute__((address_space(3))) s16x4* lds_s16x4_ptr;

#define DI __device__ __forceinline__
#define MFMA32(a, b, c) __builtin_amdgcn_mfma_f32_32x32x16_bf16((a), (b), (c), 0, 0, 0)
#define MFMA16(a, b, c) __builtin_amdgcn_mfma_f32_16x16x32_bf16((a), (b), (c), 0, 0, 0)
#define SBAR() __builtin_amdgcn_sched_barrier(0)
#define STAMP(i)

constexpr int NB = 32, SQ = 32, DM = 512, HQ = 32, HKV = 8, SK = 512, DH = 128;
constexpr int LDS_BYTES = 163840;
constexpr int QX_OFF = 131072;

DI unsigned pk2(float a, float b) {
  f32x2 f = {a, b};
  return __builtin_bit_cast(unsigned, __builtin_convertvector(f, bf16x2));
}
DI float swap_half_max(float v) {
  auto rr = __builtin_amdgcn_permlane32_swap(__float_as_uint(v), __float_as_uint(v), false, false);
  return fmaxf(__uint_as_float(rr[0]), __uint_as_float(rr[1]));
}
DI float swap_half_add(float v) {
  auto rr = __builtin_amdgcn_permlane32_swap(__float_as_uint(v), __float_as_uint(v), false, false);
  return __uint_as_float(rr[0]) + __uint_as_float(rr[1]);
}
DI void wg_barrier() {
  asm volatile("s_waitcnt lgkmcnt(0)" ::: "memory");
  __builtin_amdgcn_s_barrier();
  asm volatile("" ::: "memory");
}
DI void glds16_nt(const void* sbase, unsigned voff, unsigned lds_dst) {
  unsigned keep;
  asm volatile("s_mov_b32 %0, m0\n\ts_mov_b32 m0, %3\n\ts_nop 0\n\tglobal_load_lds_dwordx4 %1, %2 nt\n\ts_mov_b32 m0, %0"
               : "=&s"(keep) : "v"(voff), "s"(sbase), "s"(lds_dst) : "memory");
}
DI void glds16(const void* sbase, unsigned voff, unsigned lds_dst) {
  unsigned keep;
  asm volatile("s_mov_b32 %0, m0\n\ts_mov_b32 m0, %3\n\ts_nop 0\n\tglobal_load_lds_dwordx4 %1, %2\n\ts_mov_b32 m0, %0"
               : "=&s"(keep) : "v"(voff), "s"(sbase), "s"(lds_dst) : "memory");
}
template <int I>
DI bf16x8 tr_frag(const u32x4* w) {
  u32x4 o;
  constexpr unsigned sel = (I & 1) ? 0x07060302u : 0x05040100u;
#pragma unroll
  for (int m = 0; m < 4; ++m) o[m] = __builtin_amdgcn_perm(w[2 * m + 1][I >> 1], w[2 * m][I >> 1], sel);
  return __builtin_bit_cast(bf16x8, o);
}
#define PROJ_STEP(W, B0, B1, LOADS)                           \
  do {                                                        \
    bf16x8 a_[8];                                             \
    a_[0] = tr_frag<0>(W); a_[1] = tr_frag<1>(W); a_[2] = tr_frag<2>(W); a_[3] = tr_frag<3>(W); \
    a_[4] = tr_frag<4>(W); a_[5] = tr_frag<5>(W); a_[6] = tr_frag<6>(W); a_[7] = tr_frag<7>(W); \
    SBAR();                                                   \
    _Pragma("unroll") for (int i_ = 0; i_ < 8; ++i_) {        \
      acc[i_][0] = MFMA16(a_[i_], B0, acc[i_][0]);            \
      acc[i_][1] = MFMA16(a_[i_], B1, acc[i_][1]);            \
    }                                                         \
    LOADS;                                                    \
    _Pragma("unroll") for (int i_ = 0; i_ < 8; ++i_) {        \
      __builtin_amdgcn_sched_group_barrier(0x8, 2, 0);        \
      __builtin_amdgcn_sched_group_barrier(0x20, 1, 0);       \
    }                                                         \
  } while (0)

#define NPRE_A 8
#define NPRE_B 4
#define BW_STEP 12
#define RESC_THR 6.0f
#define ST_AUX 16
#define ROLL
#define CODE_WARM 23
#define QDEPTH 2
constexpr int QD = QDEPTH;

__global__ __launch_bounds__(512, 2) void attn_kernel(const __hip_bfloat16* __restrict__ x,
                                                      const __hip_bfloat16* __restrict__ kc,
                                                      const __hip_bfloat16* __restrict__ vc,
                                                      const __hip_bfloat16* __restrict__ wq,
                                                      const __hip_bfloat16* __restrict__ wo,
                                                      char* __restrict__ ows) {
  extern __shared__ __attribute__((aligned(16))) char smem[];
  const int tid = threadIdx.x;
  const int wid = __builtin_amdgcn_readfirstlane(tid >> 6);
  const int lane = tid & 63;
  const int kvh = blockIdx.x & 7, b = blockIdx.x >> 3;
  const int hh = wid & 3, u = wid >> 2;
  const int r = lane & 31, h = lane >> 5;
  const int r16 = lane & 15, q = lane >> 4;
#define GRP_EXPR (b & 1)
#define ROT_EXPR ((b >> 1) & 15)
  const int grp = GRP_EXPR;
  const int rot = ROT_EXPR;
  STAMP(0);

  const unsigned lds0 = (unsigned)(size_t)smem;
  const size_t kvbase = ((size_t)(b * HKV + kvh)) * SK * DH;
  auto tile_isv = [](int t) { return t >= 14 ? 1 : (t < 2 ? 0 : (((t - 2) >> 1) & 1)); };
  auto tile_idx = [](int t) { return t >= 14 ? t - 8 : (t < 2 ? t : ((((t - 2) >> 1) & 1) ? 2 * ((t - 2) >> 2) + (t & 1) : 2 * ((t - 2) >> 2) + 2 + (t & 1))); };
  auto issue_tile = [&](int t) {
    const int slot = t < 10 ? t : t - 10;
    const int isv = tile_isv(t);
    const char* base = (const char*)((isv ? vc : kc) + kvbase + (size_t)tile_idx(t) * 64 * DH) + hh * 4096;
#pragma unroll
    for (int e = 0; e < 4; ++e) {
      const int chunk = isv ? (r16 ^ (q << 2)) : (r16 ^ (4 * e + q));
      glds16_nt(base + e * 1024, q * 256 + chunk * 16, lds0 + slot * 16384 + (4 * hh + e) * 1024);
    }
  };
  auto issue_tile8 = [&](int t) {
    const int slot = t < 10 ? t : t - 10;
    const int isv = tile_isv(t);
    const char* base = (const char*)((isv ? vc : kc) + kvbase + (size_t)tile_idx(t) * 64 * DH) + wid * 2048;
#pragma unroll
    for (int e = 0; e < 2; ++e) {
      const int chunk = isv ? (r16 ^ (q << 2)) : (r16 ^ (8 * (wid & 1) + 4 * e + q));
      glds16_nt(base + e * 1024, q * 256 + chunk * 16, lds0 + slot * 16384 + (2 * wid + e) * 1024);
    }
  };
  u32x4 cw = {0u, 0u, 0u, 0u};
  if (wid >= 6 && b + 32 * (7 - wid) < CODE_WARM) {
    unsigned long long pc;
    asm volatile("s_getpc_b64 %0" : "=s"(pc));
    const char* cp = (const char*)(pc & ~255ull) + (b + 32 * (7 - wid)) * 1024 + lane * 16;
    asm volatile("global_load_dwordx4 %0, %1, off" : "=v"(cw) : "v"(cp) : "memory");
  }
  {
    const __hip_bfloat16* xb = x + (size_t)b * SQ * DM;
#pragma unroll
    for (int i = 0; i < 4; ++i) {
      const int row = 4 * wid + i;
      glds16(xb + row * DM, (lane ^ (row & 15)) * 16, lds0 + QX_OFF + row * 1024);
    }
  }

  bf16x8 qf[8];
  if (u == 1) {
    if (grp == 0) {
#pragma unroll
      for (int t = 0; t < NPRE_A; ++t) issue_tile(t);
      asm volatile("s_waitcnt vmcnt(%0)" ::"i"(4 * NPRE_A) : "memory");
    } else {
      asm volatile("s_waitcnt vmcnt(0)" ::: "memory");
    }
    asm volatile("" ::"v"(cw));
    wg_barrier();
    {
      u32x4 ww[4];
      const char* wb = (const char*)wo + (size_t)(kvh * 512 + 16 * b + 4 * hh) * 1024 + lane * 16;
      asm volatile("global_load_dwordx4 %0, %4, off\n\tglobal_load_dwordx4 %1, %4, off offset:1024\n\t"
                   "global_load_dwordx4 %2, %4, off offset:2048\n\tglobal_load_dwordx4 %3, %4, off offset:3072\n\t"
                   "s_waitcnt vmcnt(0)"
                   : "=&v"(ww[0]), "=&v"(ww[1]), "=&v"(ww[2]), "=&v"(ww[3]) : "v"(wb) : "memory");
    }
    wg_barrier();
    if (grp == 1) {
#pragma unroll
      for (int t = 0; t < NPRE_B; ++t) issue_tile(t);
    }
    wg_barrier();
    wg_barrier();
#pragma unroll
    for (int p = 0; p < 8; ++p)
      qf[p] = *reinterpret_cast<const bf16x8*>(smem + QX_OFF + (hh * 8 + p) * 1024 + lane * 16);
    if (grp == 0) asm volatile("s_waitcnt vmcnt(%0)" ::"i"(4 * NPRE_A - 8) : "memory");
    else asm volatile("s_waitcnt vmcnt(%0)" ::"i"(4 * NPRE_B - 8) : "memory");
  } else {
    const auto wq_rs = __builtin_amdgcn_make_buffer_rsrc((void*)wq, 0, DM * HQ * DH * 2, 0x00020000);
    const int w_voff = (8 * q) * 8192 + (kvh * 512 + hh * 128 + 8 * r16) * 2;
    u32x4 w[QD][8];
#define PRE_W 1
#pragma unroll
    for (int d = 0; d < PRE_W; ++d) {
      const int sr = (d + rot) & 15;
#pragma unroll
      for (int jj = 0; jj < 8; ++jj)
        w[d][jj] = __builtin_amdgcn_raw_buffer_load_b128(wq_rs, w_voff, (32 * sr + jj) * 8192, 0);
    }
    asm volatile("s_waitcnt vmcnt(%0)" ::"i"(8 * PRE_W) : "memory");
    wg_barrier();
#pragma unroll
    for (int d = PRE_W; d < QD; ++d) {
      const int sr = (d + rot) & 15;
#pragma unroll
      for (int jj = 0; jj < 8; ++jj)
        w[d][jj] = __builtin_amdgcn_raw_buffer_load_b128(wq_rs, w_voff, (32 * sr + jj) * 8192, 0);
    }
    STAMP(14);
    f32x4 acc[8][2];
#pragma unroll
    for (int i = 0; i < 8; ++i) { acc[i][0] = f32x4{}; acc[i][1] = f32x4{}; }
    const char* xs = smem + QX_OFF + r16 * 1024;
#define Q_STEP(S, BUF, LD)                                                                              \
  do {                                                                                                  \
    SBAR();                                                                                             \
    const int xo = ((4 * (((S) + rot) & 15) + q) ^ r16) << 4;                                           \
    const bf16x8 b0 = *reinterpret_cast<const bf16x8*>(xs + xo);                                        \
    const bf16x8 b1 = *reinterpret_cast<const bf16x8*>(xs + xo + 16384);                                \
    const int sr = ((S) + 2 + rot) & 15;                                                                \
    PROJ_STEP(w[BUF], b0, b1, if (LD) {                                                                 \
      _Pragma("unroll") for (int jj = 0; jj < 8; ++jj)                                                  \
        w[BUF][jj] = __builtin_amdgcn_raw_buffer_load_b128(wq_rs, w_voff, (32 * sr + jj) * 8192, 0);    \
    });                                                                                                 \
  } while (0)
#pragma unroll 1
    for (int it = 0; it < 7; ++it) {
      Q_STEP(2 * it, 0, true);
      Q_STEP(2 * it + 1, 1, true);
    }
    SBAR();
    __builtin_amdgcn_s_barrier();
    Q_STEP(14, 0, false);
    Q_STEP(15, 1, false);
#pragma unroll
    for (int g = 0; g < 4; ++g) {
      u32x4 f0, f1;
#pragma unroll
      for (int e = 0; e < 4; ++e) {
        const unsigned a0 = pk2(acc[2 * e][0][g], acc[2 * e + 1][0][g]);
        const unsigned a1 = pk2(acc[2 * e][1][g], acc[2 * e + 1][1][g]);
        auto rr = __builtin_amdgcn_permlane16_swap(a0, a1, false, false);
        f0[e] = rr[0];
        f1[e] = rr[1];
      }
      qf[g] = __builtin_bit_cast(bf16x8, f0);
      qf[4 + g] = __builtin_bit_cast(bf16x8, f1);
    }
    wg_barrier();
#pragma unroll
    for (int p = 0; p < 8; ++p)
      *reinterpret_cast<bf16x8*>(smem + QX_OFF + (hh * 8 + p) * 1024 + lane * 16) = qf[p];
    STAMP(1);
    wg_barrier();
  }

  constexpr float CS = 0.088388347648318447f * 1.4426950408889634f;
  float m = -__builtin_inff(), l = 0.f;
  f32x16 o[4];
#pragma unroll
  for (int i = 0; i < 4; ++i) o[i] = f32x16{};
  const int ky = (r & 15) ^ (8 * h);
  const int koff = r * 256;
  const int vq = (lane >> 2) & 3, vpp = lane & 3;
  const int voff = (4 * h + vq) * 256 + (2 * (q & 1) + (vpp >> 1)) * 16 + 8 * (vpp & 1);
  bf16x8 fb[16];
  bf16x8 pf[4];
  f32x16 s0, s1;
#define SLOT(T) (smem + (((T) < 10 ? (T) : (T) - 10) + u) * 16384)
#define KT(S) ((S) == 0 ? 0 : 4 * (S) - 2)
#define VT(S) ((S) == 3 ? 14 : 4 * (S) + 4)
#define K_READ(S, H)                                                                                        \
  do {                                                                                                      \
    const char* Ks = SLOT(KT(S));                                                                           \
    _Pragma("unroll") for (int p = 4 * (H); p < 4 * (H) + 4; ++p) {                                         \
      const char* a = Ks + koff + ((p ^ ky) << 4);                                                          \
      fb[2 * p] = *reinterpret_cast<const bf16x8*>(a);                                                      \
      fb[2 * p + 1] = *reinterpret_cast<const bf16x8*>(a + 8192);                                           \
    }                                                                                                       \
  } while (0)
#define QK_MMA()                                                                                            \
  do {                                                                                                      \
    s0 = f32x16{}; s1 = f32x16{};                                                                           \
    _Pragma("unroll") for (int p = 0; p < 8; ++p) {                                                         \
      s0 = MFMA32(fb[2 * p], qf[p], s0);                                                                    \
      s1 = MFMA32(fb[2 * p + 1], qf[p], s1);                                                                \
    }                                                                                                       \
  } while (0)
#define V_READ(S, DT)                                                                                       \
  do {                                                                                                      \
    const char* vb = SLOT(VT(S)) + voff + (((DT) ^ vq) << 6);                                         \
    _Pragma("unroll") for (int ks = 0; ks < 4; ++ks) {                                                      \
      const s16x4 lo = __builtin_amdgcn_ds_read_tr16_b64_v4i16((lds_s16x4_ptr)(vb + ks * 4096));            \
      const s16x4 hi = __builtin_amdgcn_ds_read_tr16_b64_v4i16((lds_s16x4_ptr)(vb + ks * 4096 + 2048));     \
      fb[(DT) * 4 + ks] = __builtin_bit_cast(bf16x8, __builtin_shufflevector(lo, hi, 0, 1, 2, 3, 4, 5, 6, 7)); \
    }                                                                                                       \
  } while (0)
#define PRIO(x)
#define PV_MMA(D0)                                                                                          \
  do {                                                                                                      \
    _Pragma("unroll") for (int ks = 0; ks < 4; ++ks)                                                        \
      _Pragma("unroll") for (int dt = (D0); dt < (D0) + 2; ++dt) o[dt] = MFMA32(fb[dt * 4 + ks], pf[ks], o[dt]); \
  } while (0)
#define SOFTMAX(S)                                                                                          \
  do {                                                                                                      \
    float tmax = s0[0];                                                                                     \
    _Pragma("unroll") for (int i = 1; i < 16; ++i) tmax = fmaxf(tmax, s0[i]);                               \
    _Pragma("unroll") for (int i = 0; i < 16; ++i) tmax = fmaxf(tmax, s1[i]);                               \
    tmax = swap_half_max(tmax);                                                                             \
      \
      \
    if ((S) == 0) {                                                                                         \
      m = tmax;                                                                                             \
    } else if (__builtin_amdgcn_ballot_w64((tmax - m) * CS > RESC_THR) != 0) {                              \
      const float mnew = fmaxf(m, tmax);                                                                    \
      const float alpha = __builtin_amdgcn_exp2f((m - mnew) * CS);                                          \
      m = mnew;                                                                                             \
      l *= alpha;                                                                                           \
      _Pragma("unroll") for (int dt = 0; dt < 4; ++dt)                                                      \
        _Pragma("unroll") for (int i = 0; i < 16; ++i) o[dt][i] *= alpha;                                   \
    }                                                                                                       \
    const float mc = m * CS;                                                                                \
    float ps = 0.f;                                                                                         \
    _Pragma("unroll") for (int i = 0; i < 16; ++i) { s0[i] = __builtin_amdgcn_exp2f(fmaf(s0[i], CS, -mc)); ps += s0[i]; } \
    _Pragma("unroll") for (int i = 0; i < 16; ++i) { s1[i] = __builtin_amdgcn_exp2f(fmaf(s1[i], CS, -mc)); ps += s1[i]; } \
    l += ps;                                                                                                \
    asm volatile("" : "+v"(l));                                                                             \
    _Pragma("unroll") for (int ss = 0; ss < 2; ++ss) {                                                      \
      u32x4 u0, u1;                                                                                         \
      _Pragma("unroll") for (int e = 0; e < 4; ++e) {                                                       \
        u0[e] = pk2(s0[8 * ss + 2 * e], s0[8 * ss + 2 * e + 1]);                                            \
        u1[e] = pk2(s1[8 * ss + 2 * e], s1[8 * ss + 2 * e + 1]);                                            \
      }                                                                                                     \
      pf[ss] = __builtin_bit_cast(bf16x8, u0);                                                              \
      pf[2 + ss] = __builtin_bit_cast(bf16x8, u1);                                                          \
    }                                                                                                       \
  } while (0)
#define DMA_A(S)                                                                                            \
  do {                                                                                                      \
    if ((S) == 0) issue_tile8(8);                                                                           \
    if ((S) == 1) issue_tile8(10);                                                                          \
    if ((S) == 2) { issue_tile8(12); issue_tile8(13); }                                                     \
    if ((S) == 3) {                                                                                         \
                    \
      const char* wb = (const char*)wo + (size_t)(kvh * 512 + 16 * b + 2 * wid) * 1024;                     \
      glds16(wb, lane * 16, lds0 + 6 * 16384 + 2 * wid * 1024);                                             \
      glds16(wb + 1024, lane * 16, lds0 + 6 * 16384 + (2 * wid + 1) * 1024);                                \
    }                                                                                                       \
  } while (0)
#define DMA_B(S)                                                                                            \
  do {                                                                                                      \
    if ((S) == 0) issue_tile8(9);                                                                           \
    if ((S) == 1) issue_tile8(11);                                                                          \
    if ((S) == 2) { issue_tile8(14); issue_tile8(15); }                                                     \
  } while (0)
#define STEP_WAIT(S)                                                                                        \
  do {                                                                                                      \
    if ((S) == 0) asm volatile("s_waitcnt vmcnt(12)" ::: "memory");                                         \
    if ((S) == 1 || (S) == 2) asm volatile("s_waitcnt vmcnt(4)" ::: "memory");                              \
    if ((S) == 3) asm volatile("s_waitcnt vmcnt(2)" ::: "memory");                                          \
  } while (0)

  char* mlp = smem + 7 * 16384;
  if (u == 1) {
    wg_barrier();
    if (grp == 0) {
#pragma unroll
      for (int t = NPRE_A; t < 8; ++t) issue_tile(t);
    } else {
#pragma unroll
      for (int t = NPRE_B; t < 8; ++t) issue_tile(t);
    }
  } else {
    wg_barrier();
  }
  STAMP(2);
  K_READ(0, 0);
  K_READ(0, 1);
  DMA_A(0);
  DMA_B(0);
  SBAR();
  QK_MMA();
  SBAR();
  if (u == 1) {
    STEP_WAIT(0);
    wg_barrier();
    DMA_A(1);
    DMA_B(1);
  }
  SOFTMAX(0);
  SBAR();
  if (u == 0) STEP_WAIT(0);
#pragma unroll 1
  for (int i = 1; i < 4; ++i) {
    if (u == 0) wg_barrier();
    V_READ(i - 1, 0);
    V_READ(i - 1, 1);
    V_READ(i - 1, 2);
    V_READ(i - 1, 3);
    if (u == 0) { DMA_A(i); DMA_B(i); }
    SBAR();
    PV_MMA(0);
    SBAR();
    K_READ(i, 0);
    SBAR();
    PV_MMA(2);
    SBAR();
    K_READ(i, 1);
    SBAR();
    QK_MMA();
    SBAR();
    if (u == 1) {
      STEP_WAIT(i);
      wg_barrier();
      DMA_A(i + 1);
      DMA_B(i + 1);
    }
    SOFTMAX(1);
    SBAR();
    if (u == 0) STEP_WAIT(i);
  }
  l = swap_half_add(l);
  if (h == 0) *reinterpret_cast<f32x2*>(mlp + wid * 256 + r * 8) = f32x2{m, l};
  if (u == 0) wg_barrier();
  STAMP(9);
  V_READ(3, 0);
  V_READ(3, 1);
  V_READ(3, 2);
  V_READ(3, 3);
  SBAR();
  PV_MMA(0);
  PV_MMA(2);
  SBAR();
  const auto wo_rs = __builtin_amdgcn_make_buffer_rsrc((void*)wo, 0, HQ * DH * DM * 2, 0x00020000);
  const int wo_voff = (kvh * 512 + 8 * q) * 1024 + (hh * 128 + 8 * r16) * 2;
  u32x4 w[QD][8];
  if (u == 0) {
#pragma unroll
    for (int d = 0; d < QD; ++d)
#pragma unroll
      for (int jj = 0; jj < 8; ++jj)
        w[d][jj] = __builtin_amdgcn_raw_buffer_load_b128(wo_rs, wo_voff, (32 * ((d + rot) & 15) + jj) * 1024, 0);
  }
  SBAR();
  wg_barrier();
  STAMP(10);
  {
    char* xb_ = smem + wid * 8192;
#pragma unroll
    for (int c = 0; c < 2; ++c) {
#pragma unroll
      for (int g = 0; g < 4; ++g) {
        f32x4 v;
        if (u == 0) v = f32x4{o[2 + c][4 * g], o[2 + c][4 * g + 1], o[2 + c][4 * g + 2], o[2 + c][4 * g + 3]};
        else v = f32x4{o[c][4 * g], o[c][4 * g + 1], o[c][4 * g + 2], o[c][4 * g + 3]};
        *reinterpret_cast<f32x4*>(xb_ + (c * 4 + g) * 1024 + lane * 16) = v;
      }
    }
    const f32x2 ml2 = *reinterpret_cast<const f32x2*>(mlp + (wid ^ 4) * 256 + r * 8);
    const float mm = fmaxf(m, ml2[0]);
    const float e1 = __builtin_amdgcn_exp2f((m - mm) * CS), e2 = __builtin_amdgcn_exp2f((ml2[0] - mm) * CS);
    const float inv = 1.0f / (l * e1 + ml2[1] * e2);
    const float f1 = e1 * inv, f2 = e2 * inv;
    wg_barrier();
    const char* pb = smem + (wid ^ 4) * 8192;
    char* of = smem + QX_OFF + (r >> 4) * 1024 + (r & 15) * 16 + 8 * h;
    f32x4 pv[8];
#pragma unroll
    for (int c = 0; c < 8; ++c) pv[c] = *reinterpret_cast<const f32x4*>(pb + c * 1024 + lane * 16);
    SBAR();
#pragma unroll
    for (int c = 0; c < 2; ++c) {
      const int dt = 2 * u + c;
#pragma unroll
      for (int g = 0; g < 4; ++g) {
        f32x4 ov;
        if (u == 0) ov = f32x4{o[c][4 * g], o[c][4 * g + 1], o[c][4 * g + 2], o[c][4 * g + 3]};
        else ov = f32x4{o[2 + c][4 * g], o[2 + c][4 * g + 1], o[2 + c][4 * g + 2], o[2 + c][4 * g + 3]};
        const f32x4 p2 = pv[c * 4 + g];
        u32x2 v;
        v[0] = pk2(ov[0] * f1 + p2[0] * f2, ov[1] * f1 + p2[1] * f2);
        v[1] = pk2(ov[2] * f1 + p2[2] * f2, ov[3] * f1 + p2[3] * f2);
        *reinterpret_cast<u32x2*>(of + (hh * 4 + dt) * 2048 + g * 256) = v;
      }
    }
  }
  if (u == 1) {
    asm volatile("s_waitcnt vmcnt(0)" ::: "memory");
    wg_barrier();
    return;
  }
  {
    wg_barrier();
    STAMP(11);
    f32x4 acc[8][2];
#pragma unroll
    for (int i = 0; i < 8; ++i) { acc[i][0] = f32x4{}; acc[i][1] = f32x4{}; }
    const char* of = smem + QX_OFF + lane * 16;
#define O_STEP(S, BUF, LD)                                                                              \
  do {                                                                                                  \
    SBAR();                                                                                             \
    const bf16x8 b0 = *reinterpret_cast<const bf16x8*>(of + (((S) + rot) & 15) * 2048);                 \
    const bf16x8 b1 = *reinterpret_cast<const bf16x8*>(of + (((S) + rot) & 15) * 2048 + 1024);          \
    PROJ_STEP(w[BUF], b0, b1, if (LD) {                                                                 \
      _Pragma("unroll") for (int jj = 0; jj < 8; ++jj)                                                  \
        w[BUF][jj] = __builtin_amdgcn_raw_buffer_load_b128(wo_rs, wo_voff, (32 * (((S) + 2 + rot) & 15) + jj) * 1024, 0); \
    });                                                                                                 \
  } while (0)
#pragma unroll 1
    for (int it = 0; it < 7; ++it) {
      O_STEP(2 * it, 0, true);
      O_STEP(2 * it + 1, 1, true);
    }
    O_STEP(14, 0, false);
    O_STEP(15, 1, false);
    STAMP(12);
    char* pp = ows + ((size_t)((kvh * NB + b) * 4 + hh)) * 8192 + lane * 16;
    const auto p_rs = __builtin_amdgcn_make_buffer_rsrc((void*)ows, 0, 8 << 20, 0x00020000);
    const int p_voff = ((kvh * NB + b) * 4 + hh) * 8192 + lane * 16;
#pragma unroll
    for (int T = 0; T < 2; ++T)
#pragma unroll
      for (int g = 0; g < 4; ++g) {
        f32x4 v0 = {acc[0][T][g], acc[1][T][g], acc[2][T][g], acc[3][T][g]};
        f32x4 v1 = {acc[4][T][g], acc[5][T][g], acc[6][T][g], acc[7][T][g]};
        u32x4 pk = {pk2(v0[0], v0[1]), pk2(v0[2], v0[3]), pk2(v1[0], v1[1]), pk2(v1[2], v1[3])};
        __builtin_amdgcn_raw_buffer_store_b128(pk, p_rs, p_voff, (T * 4 + g) * 1024, ST_AUX);
      }
    STAMP(13);
  }
}

#define RT 256
__global__ __launch_bounds__(RT) void reduce_kernel(const char* __restrict__ ows, __hip_bfloat16* __restrict__ out) {
  const int gid = blockIdx.x * RT + threadIdx.x;
  const int lane = gid & 63, tg = (gid >> 6) & 7, bc = gid >> 9;
  const char* pp = ows + (size_t)bc * 8192 + tg * 1024 + lane * 16;
  u32x4 v[8];
#pragma unroll
  for (int k = 0; k < 8; ++k) v[k] = *reinterpret_cast<const u32x4*>(pp + (size_t)k * (NB * 4 * 8192));
  f32x4 a = {0.f, 0.f, 0.f, 0.f}, c = {0.f, 0.f, 0.f, 0.f};
#pragma unroll
  for (int k = 0; k < 8; ++k) {
    a[0] += __uint_as_float(v[k][0] << 16); a[1] += __uint_as_float(v[k][0] & 0xffff0000u);
    a[2] += __uint_as_float(v[k][1] << 16); a[3] += __uint_as_float(v[k][1] & 0xffff0000u);
    c[0] += __uint_as_float(v[k][2] << 16); c[1] += __uint_as_float(v[k][2] & 0xffff0000u);
    c[2] += __uint_as_float(v[k][3] << 16); c[3] += __uint_as_float(v[k][3] & 0xffff0000u);
  }
  const int b = bc >> 2, cg = bc & 3, T = tg >> 2, g = tg & 3, r16 = lane & 15, q = lane >> 4;
  u32x4 o = {pk2(a[0], a[1]), pk2(a[2], a[3]), pk2(c[0], c[1]), pk2(c[2], c[3])};
  *reinterpret_cast<u32x4*>(out + (size_t)(b * SQ + 16 * T + r16) * DM + 128 * cg + 32 * q + 8 * g) = o;
}

extern "C" void kernel_launch(void* const* d_in, const int* in_sizes, int n_in, void* d_out, int out_size,
                              void* d_ws, size_t ws_size, hipStream_t stream) {
  const __hip_bfloat16* x = (const __hip_bfloat16*)d_in[0];
  const __hip_bfloat16* kc = (const __hip_bfloat16*)d_in[1];
  const __hip_bfloat16* vc = (const __hip_bfloat16*)d_in[2];
  const __hip_bfloat16* wq = (const __hip_bfloat16*)d_in[3];
  const __hip_bfloat16* wo = (const __hip_bfloat16*)d_in[4];
  (void)hipFuncSetAttribute((const void*)attn_kernel, hipFuncAttributeMaxDynamicSharedMemorySize, LDS_BYTES);
  attn_kernel<<<256, 512, LDS_BYTES, stream>>>(x, kc, vc, wq, wo, (char*)d_ws);
  reduce_kernel<<<65536 / RT, RT, 0, stream>>>((const char*)d_ws, (__hip_bfloat16*)d_out);
}
```

```cpp
#include <hip/hip_runtime.h>
#include <hip/hip_bf16.h>

typedef __bf16 bf16x8 __attribute__((ext_vector_type(8)));
typedef __bf16 bf16x2 __attribute__((ext_vector_type(2)));
typedef short s16x4 __attribute__((ext_vector_type(4)));
typedef float f32x2 __attribute__((ext_vector_type(2)));
typedef float f32x4 __attribute__((ext_vector_type(4)));
typedef float f32x16 __attribute__((ext_vector_type(16)));
typedef unsigned u32x2 __attribute__((ext_vector_type(2)));
typedef unsigned u32x4 __attribute__((ext_vector_type(4)));
typedef __attribute__((address_space(3))) s16x4* lds_s16x4_ptr;

#define DI __device__ __forceinline__
#define MFMA32(a, b, c) __builtin_amdgcn_mfma_f32_32x32x16_bf16((a), (b), (c), 0, 0, 0)
#define MFMA16(a, b, c) __builtin_amdgcn_mfma_f32_16x16x32_bf16((a), (b), (c), 0, 0, 0)
#define SBAR() __builtin_amdgcn_sched_barrier(0)
#define STAMP(i)

constexpr int NB = 32, SQ = 32, DM = 512, HQ = 32, HKV = 8, SK = 512, DH = 128;
constexpr int LDS_BYTES = 163840;
constexpr int QX_OFF = 131072;

DI unsigned pk2(float a, float b) {
  f32x2 f = {a, b};
  return __builtin_bit_cast(unsigned, __builtin_convertvector(f, bf16x2));
}
DI float swap_half_max(float v) {
  auto rr = __builtin_amdgcn_permlane32_swap(__float_as_uint(v), __float_as_uint(v), false, false);
  return fmaxf(__uint_as_float(rr[0]), __uint_as_float(rr[1]));
}
DI float swap_half_add(float v) {
  auto rr = __builtin_amdgcn_permlane32_swap(__float_as_uint(v), __float_as_uint(v), false, false);
  return __uint_as_float(rr[0]) + __uint_as_float(rr[1]);
}
DI void wg_barrier() {
  asm volatile("s_waitcnt lgkmcnt(0)" ::: "memory");
  __builtin_amdgcn_s_barrier();
  asm volatile("" ::: "memory");
}
DI void glds16_nt(const void* sbase, unsigned voff, unsigned lds_dst) {
  unsigned keep;
  asm volatile("s_mov_b32 %0, m0\n\ts_mov_b32 m0, %3\n\ts_nop 0\n\tglobal_load_lds_dwordx4 %1, %2 nt\n\ts_mov_b32 m0, %0"
               : "=&s"(keep) : "v"(voff), "s"(sbase), "s"(lds_dst) : "memory");
}
DI void glds16(const void* sbase, unsigned voff, unsigned lds_dst) {
  unsigned keep;
  asm volatile("s_mov_b32 %0, m0\n\ts_mov_b32 m0, %3\n\ts_nop 0\n\tglobal_load_lds_dwordx4 %1, %2\n\ts_mov_b32 m0, %0"
               : "=&s"(keep) : "v"(voff), "s"(sbase), "s"(lds_dst) : "memory");
}
template <int I>
DI bf16x8 tr_frag(const u32x4* w) {
  u32x4 o;
  constexpr unsigned sel = (I & 1) ? 0x07060302u : 0x05040100u;
#pragma unroll
  for (int m = 0; m < 4; ++m) o[m] = __builtin_amdgcn_perm(w[2 * m + 1][I >> 1], w[2 * m][I >> 1], sel);
  return __builtin_bit_cast(bf16x8, o);
}
#define PROJ_STEP(W, B0, B1, LOADS)                           \
  do {                                                        \
    bf16x8 a_[8];                                             \
    a_[0] = tr_frag<0>(W); a_[1] = tr_frag<1>(W); a_[2] = tr_frag<2>(W); a_[3] = tr_frag<3>(W); \
    a_[4] = tr_frag<4>(W); a_[5] = tr_frag<5>(W); a_[6] = tr_frag<6>(W); a_[7] = tr_frag<7>(W); \
    SBAR();                                                   \
    _Pragma("unroll") for (int i_ = 0; i_ < 8; ++i_) {        \
      acc[i_][0] = MFMA16(a_[i_], B0, acc[i_][0]);            \
      acc[i_][1] = MFMA16(a_[i_], B1, acc[i_][1]);            \
    }                                                         \
    LOADS;                                                    \
    _Pragma("unroll") for (int i_ = 0; i_ < 8; ++i_) {        \
      __builtin_amdgcn_sched_group_barrier(0x8, 2, 0);        \
      __builtin_amdgcn_sched_group_barrier(0x20, 1, 0);       \
    }                                                         \
  } while (0)

#define NPRE_A 8
#define NPRE_B 4
#define RESC_THR 6.0f
#define ST_AUX 16
#define ROLL
#define CODE_WARM 18
#define QDEPTH 2
constexpr int QD = QDEPTH;

__global__ __launch_bounds__(512, 2) void attn_kernel(const __hip_bfloat16* __restrict__ x,
                                                      const __hip_bfloat16* __restrict__ kc,
                                                      const __hip_bfloat16* __restrict__ vc,
                                                      const __hip_bfloat16* __restrict__ wq,
                                                      const __hip_bfloat16* __restrict__ wo,
                                                      char* __restrict__ ows) {
  extern __shared__ __attribute__((aligned(16))) char smem[];
  const int tid = threadIdx.x;
  const int wid = __builtin_amdgcn_readfirstlane(tid >> 6);
  const int lane = tid & 63;
  const int kvh = blockIdx.x & 7, b = blockIdx.x >> 3;
  const int hh = wid & 3, u = wid >> 2;
  const int r = lane & 31, h = lane >> 5;
  const int r16 = lane & 15, q = lane >> 4;
#define GRP_EXPR (b & 1)
#define ROT_EXPR ((b >> 1) & 15)
  const int grp = GRP_EXPR;
  const int rot = ROT_EXPR;
  STAMP(0);

  const unsigned lds0 = (unsigned)(size_t)smem;
  const size_t kvbase = ((size_t)(b * HKV + kvh)) * SK * DH;
  auto tile_isv = [](int t) { return t >= 14 ? 1 : (t < 2 ? 0 : (((t - 2) >> 1) & 1)); };
  auto tile_idx = [](int t) { return t >= 14 ? t - 8 : (t < 2 ? t : ((((t - 2) >> 1) & 1) ? 2 * ((t - 2) >> 2) + (t & 1) : 2 * ((t - 2) >> 2) + 2 + (t & 1))); };
  auto issue_tile = [&](int t) {
    const int slot = t < 10 ? t : t - 10;
    const int isv = tile_isv(t);
    const char* base = (const char*)((isv ? vc : kc) + kvbase + (size_t)tile_idx(t) * 64 * DH) + hh * 4096;
#pragma unroll
    for (int e = 0; e < 4; ++e) {
      const int chunk = isv ? (r16 ^ (q << 2)) : (r16 ^ (4 * e + q));
      glds16_nt(base + e * 1024, q * 256 + chunk * 16, lds0 + slot * 16384 + (4 * hh + e) * 1024);
    }
  };
  auto issue_tile8 = [&](int t) {
    const int slot = t < 10 ? t : t - 10;
    const int isv = tile_isv(t);
    const char* base = (const char*)((isv ? vc : kc) + kvbase + (size_t)tile_idx(t) * 64 * DH) + wid * 2048;
#pragma unroll
    for (int e = 0; e < 2; ++e) {
      const int chunk = isv ? (r16 ^ (q << 2)) : (r16 ^ (8 * (wid & 1) + 4 * e + q));
      glds16_nt(base + e * 1024, q * 256 + chunk * 16, lds0 + slot * 16384 + (2 * wid + e) * 1024);
    }
  };
  u32x4 cw = {0u, 0u, 0u, 0u};
  if (wid >= 6 && b + 32 * (7 - wid) < CODE_WARM) {
    unsigned long long pc;
    asm volatile("s_getpc_b64 %0" : "=s"(pc));
    const char* cp = (const char*)(pc & ~255ull) + (b + 32 * (7 - wid)) * 1024 + lane * 16;
    asm volatile("global_load_dwordx4 %0, %1, off" : "=v"(cw) : "v"(cp) : "memory");
  }
  {
    const __hip_bfloat16* xb = x + (size_t)b * SQ * DM;
#pragma unroll
    for (int i = 0; i < 4; ++i) {
      const int row = 4 * wid + i;
      glds16(xb + row * DM, (lane ^ (row & 15)) * 16, lds0 + QX_OFF + row * 1024);
    }
  }

  bf16x8 qf[8];
  if (u == 1) {
    if (grp == 0) {
#pragma unroll
      for (int t = 0; t < NPRE_A; ++t) issue_tile(t);
      asm volatile("s_waitcnt vmcnt(%0)" ::"i"(4 * NPRE_A) : "memory");
    } else {
      asm volatile("s_waitcnt vmcnt(0)" ::: "memory");
    }
    asm volatile("" ::"v"(cw));
    wg_barrier();
    wg_barrier();
    if (grp == 1) {
#pragma unroll
      for (int t = 0; t < NPRE_B; ++t) issue_tile(t);
    }
    wg_barrier();
    wg_barrier();
#pragma unroll
    for (int p = 0; p < 8; ++p)
      qf[p] = *reinterpret_cast<const bf16x8*>(smem + QX_OFF + (hh * 8 + p) * 1024 + lane * 16);
    if (grp == 0) asm volatile("s_waitcnt vmcnt(%0)" ::"i"(4 * NPRE_A - 8) : "memory");
    else asm volatile("s_waitcnt vmcnt(%0)" ::"i"(4 * NPRE_B - 8) : "memory");
  } else {
    const auto wq_rs = __builtin_amdgcn_make_buffer_rsrc((void*)wq, 0, DM * HQ * DH * 2, 0x00020000);
    const int w_voff = (8 * q) * 8192 + (kvh * 512 + hh * 128 + 8 * r16) * 2;
    u32x4 w[QD][8];
#define PRE_W 1
#pragma unroll
    for (int d = 0; d < PRE_W; ++d) {
      const int sr = (d + rot) & 15;
#pragma unroll
      for (int jj = 0; jj < 8; ++jj)
        w[d][jj] = __builtin_amdgcn_raw_buffer_load_b128(wq_rs, w_voff, (32 * sr + jj) * 8192, 0);
    }
    asm volatile("s_waitcnt vmcnt(%0)" ::"i"(8 * PRE_W) : "memory");
    wg_barrier();
#pragma unroll
    for (int d = PRE_W; d < QD; ++d) {
      const int sr = (d + rot) & 15;
#pragma unroll
      for (int jj = 0; jj < 8; ++jj)
        w[d][jj] = __builtin_amdgcn_raw_buffer_load_b128(wq_rs, w_voff, (32 * sr + jj) * 8192, 0);
    }
    STAMP(14);
    f32x4 acc[8][2];
#pragma unroll
    for (int i = 0; i < 8; ++i) { acc[i][0] = f32x4{}; acc[i][1] = f32x4{}; }
    const char* xs = smem + QX_OFF + r16 * 1024;
#define Q_STEP(S, BUF, LD)                                                                              \
  do {                                                                                                  \
    SBAR();                                                                                             \
    const int xo = ((4 * (((S) + rot) & 15) + q) ^ r16) << 4;                                           \
    const bf16x8 b0 = *reinterpret_cast<const bf16x8*>(xs + xo);                                        \
    const bf16x8 b1 = *reinterpret_cast<const bf16x8*>(xs + xo + 16384);                                \
    const int sr = ((S) + 2 + rot) & 15;                                                                \
    PROJ_STEP(w[BUF], b0, b1, if (LD) {                                                                 \
      _Pragma("unroll") for (int jj = 0; jj < 8; ++jj)                                                  \
        w[BUF][jj] = __builtin_amdgcn_raw_buffer_load_b128(wq_rs, w_voff, (32 * sr + jj) * 8192, 0);    \
    });                                                                                                 \
  } while (0)
#pragma unroll 1
    for (int it = 0; it < 7; ++it) {
      Q_STEP(2 * it, 0, true);
      Q_STEP(2 * it + 1, 1, true);
    }
    SBAR();
    __builtin_amdgcn_s_barrier();
    Q_STEP(14, 0, false);
    Q_STEP(15, 1, false);
#pragma unroll
    for (int g = 0; g < 4; ++g) {
      u32x4 f0, f1;
#pragma unroll
      for (int e = 0; e < 4; ++e) {
        const unsigned a0 = pk2(acc[2 * e][0][g], acc[2 * e + 1][0][g]);
        const unsigned a1 = pk2(acc[2 * e][1][g], acc[2 * e + 1][1][g]);
        auto rr = __builtin_amdgcn_permlane16_swap(a0, a1, false, false);
        f0[e] = rr[0];
        f1[e] = rr[1];
      }
      qf[g] = __builtin_bit_cast(bf16x8, f0);
      qf[4 + g] = __builtin_bit_cast(bf16x8, f1);
    }
    wg_barrier();
#pragma unroll
    for (int p = 0; p < 8; ++p)
      *reinterpret_cast<bf16x8*>(smem + QX_OFF + (hh * 8 + p) * 1024 + lane * 16) = qf[p];
    STAMP(1);
    wg_barrier();
  }

  constexpr float CS = 0.088388347648318447f * 1.4426950408889634f;
  float m = -__builtin_inff(), l = 0.f;
  f32x16 o[4];
#pragma unroll
  for (int i = 0; i < 4; ++i) o[i] = f32x16{};
  const int ky = (r & 15) ^ (8 * h);
  const int koff = r * 256;
  const int vq = (lane >> 2) & 3, vpp = lane & 3;
  const int voff = (4 * h + vq) * 256 + (2 * (q & 1) + (vpp >> 1)) * 16 + 8 * (vpp & 1);
  bf16x8 fb[16];
  bf16x8 pf[4];
  f32x16 s0, s1;
#define SLOT(T) (smem + (((T) < 10 ? (T) : (T) - 10) + u) * 16384)
#define KT(S) ((S) == 0 ? 0 : 4 * (S) - 2)
#define VT(S) ((S) == 3 ? 14 : 4 * (S) + 4)
#define K_READ(S, H)                                                                                        \
  do {                                                                                                      \
    const char* Ks = SLOT(KT(S));                                                                           \
    _Pragma("unroll") for (int p = 4 * (H); p < 4 * (H) + 4; ++p) {                                         \
      const char* a = Ks + koff + ((p ^ ky) << 4);                                                          \
      fb[2 * p] = *reinterpret_cast<const bf16x8*>(a);                                                      \
      fb[2 * p + 1] = *reinterpret_cast<const bf16x8*>(a + 8192);                                           \
    }                                                                                                       \
  } while (0)
#define QK_MMA()                                                                                            \
  do {                                                                                                      \
    s0 = f32x16{}; s1 = f32x16{};                                                                           \
    _Pragma("unroll") for (int p = 0; p < 8; ++p) {                                                         \
      s0 = MFMA32(fb[2 * p], qf[p], s0);                                                                    \
      s1 = MFMA32(fb[2 * p + 1], qf[p], s1);                                                                \
    }                                                                                                       \
  } while (0)
#define V_READ(S, DT)                                                                                       \
  do {                                                                                                      \
    const char* vb = SLOT(VT(S)) + voff + (((DT) ^ vq) << 6);                                         \
    _Pragma("unroll") for (int ks = 0; ks < 4; ++ks) {                                                      \
      const s16x4 lo = __builtin_amdgcn_ds_read_tr16_b64_v4i16((lds_s16x4_ptr)(vb + ks * 4096));            \
      const s16x4 hi = __builtin_amdgcn_ds_read_tr16_b64_v4i16((lds_s16x4_ptr)(vb + ks * 4096 + 2048));     \
      fb[(DT) * 4 + ks] = __builtin_bit_cast(bf16x8, __builtin_shufflevector(lo, hi, 0, 1, 2, 3, 4, 5, 6, 7)); \
    }                                                                                                       \
  } while (0)
#define PRIO(x)
#define PV_MMA(D0)                                                                                          \
  do {                                                                                                      \
    _Pragma("unroll") for (int ks = 0; ks < 4; ++ks)                                                        \
      _Pragma("unroll") for (int dt = (D0); dt < (D0) + 2; ++dt) o[dt] = MFMA32(fb[dt * 4 + ks], pf[ks], o[dt]); \
  } while (0)
#define SOFTMAX(S)                                                                                          \
  do {                                                                                                      \
    float tmax = s0[0];                                                                                     \
    _Pragma("unroll") for (int i = 1; i < 16; ++i) tmax = fmaxf(tmax, s0[i]);                               \
    _Pragma("unroll") for (int i = 0; i < 16; ++i) tmax = fmaxf(tmax, s1[i]);                               \
    tmax = swap_half_max(tmax);                                                                             \
      \
      \
    if ((S) == 0) {                                                                                         \
      m = tmax;                                                                                             \
    } else if (__builtin_amdgcn_ballot_w64((tmax - m) * CS > RESC_THR) != 0) {                              \
      const float mnew = fmaxf(m, tmax);                                                                    \
      const float alpha = __builtin_amdgcn_exp2f((m - mnew) * CS);                                          \
      m = mnew;                                                                                             \
      l *= alpha;                                                                                           \
      _Pragma("unroll") for (int dt = 0; dt < 4; ++dt)                                                      \
        _Pragma("unroll") for (int i = 0; i < 16; ++i) o[dt][i] *= alpha;                                   \
    }                                                                                                       \
    const float mc = m * CS;                                                                                \
    float ps = 0.f;                                                                                         \
    _Pragma("unroll") for (int i = 0; i < 16; ++i) { s0[i] = __builtin_amdgcn_exp2f(fmaf(s0[i], CS, -mc)); ps += s0[i]; } \
    _Pragma("unroll") for (int i = 0; i < 16; ++i) { s1[i] = __builtin_amdgcn_exp2f(fmaf(s1[i], CS, -mc)); ps += s1[i]; } \
    l += ps;                                                                                                \
    asm volatile("" : "+v"(l));                                                                             \
    _Pragma("unroll") for (int ss = 0; ss < 2; ++ss) {                                                      \
      u32x4 u0, u1;                                                                                         \
      _Pragma("unroll") for (int e = 0; e < 4; ++e) {                                                       \
        u0[e] = pk2(s0[8 * ss + 2 * e], s0[8 * ss + 2 * e + 1]);                                            \
        u1[e] = pk2(s1[8 * ss + 2 * e], s1[8 * ss + 2 * e + 1]);                                            \
      }                                                                                                     \
      pf[ss] = __builtin_bit_cast(bf16x8, u0);                                                              \
      pf[2 + ss] = __builtin_bit_cast(bf16x8, u1);                                                          \
    }                                                                                                       \
  } while (0)
#define DMA_A(S)                                                                                            \
  do {                                                                                                      \
    if ((S) == 0) issue_tile8(8);                                                                           \
    if ((S) == 1) issue_tile8(10);                                                                          \
    if ((S) == 2) { issue_tile8(12); issue_tile8(13); }                                                     \
    if ((S) == 3) {                                                                                         \
                    \
      const char* wb = (const char*)wo + (size_t)(kvh * 512 + 16 * b + 2 * wid) * 1024;                     \
      glds16(wb, lane * 16, lds0 + 6 * 16384 + 2 * wid * 1024);                                             \
      glds16(wb + 1024, lane * 16, lds0 + 6 * 16384 + (2 * wid + 1) * 1024);                                \
    }                                                                                                       \
  } while (0)
#define DMA_B(S)                                                                                            \
  do {                                                                                                      \
    if ((S) == 0) issue_tile8(9);                                                                           \
    if ((S) == 1) issue_tile8(11);                                                                          \
    if ((S) == 2) { issue_tile8(14); issue_tile8(15); }                                                     \
  } while (0)
#define STEP_WAIT(S)                                                                                        \
  do {                                                                                                      \
    if ((S) == 0) asm volatile("s_waitcnt vmcnt(12)" ::: "memory");                                         \
    if ((S) == 1 || (S) == 2) asm volatile("s_waitcnt vmcnt(4)" ::: "memory");                              \
    if ((S) == 3) asm volatile("s_waitcnt vmcnt(2)" ::: "memory");                                          \
  } while (0)

  char* mlp = smem + 7 * 16384;
  if (u == 1) {
    wg_barrier();
    if (grp == 0) {
#pragma unroll
      for (int t = NPRE_A; t < 8; ++t) issue_tile(t);
    } else {
#pragma unroll
      for (int t = NPRE_B; t < 8; ++t) issue_tile(t);
    }
  } else {
    wg_barrier();
  }
  STAMP(2);
  K_READ(0, 0);
  K_READ(0, 1);
  DMA_A(0);
  DMA_B(0);
  SBAR();
  QK_MMA();
  SBAR();
  if (u == 1) {
    STEP_WAIT(0);
    wg_barrier();
    DMA_A(1);
    DMA_B(1);
  }
  SOFTMAX(0);
  SBAR();
  if (u == 0) STEP_WAIT(0);
#pragma unroll 1
  for (int i = 1; i < 4; ++i) {
    if (u == 0) wg_barrier();
    V_READ(i - 1, 0);
    V_READ(i - 1, 1);
    V_READ(i - 1, 2);
    V_READ(i - 1, 3);
    if (u == 0) { DMA_A(i); DMA_B(i); }
    SBAR();
    PV_MMA(0);
    SBAR();
    K_READ(i, 0);
    SBAR();
    PV_MMA(2);
    SBAR();
    K_READ(i, 1);
    SBAR();
    QK_MMA();
    SBAR();
    if (u == 1) {
      STEP_WAIT(i);
      wg_barrier();
      DMA_A(i + 1);
      DMA_B(i + 1);
    }
    SOFTMAX(1);
    SBAR();
    if (u == 0) STEP_WAIT(i);
  }
  l = swap_half_add(l);
  if (h == 0) *reinterpret_cast<f32x2*>(mlp + wid * 256 + r * 8) = f32x2{m, l};
  if (u == 0) wg_barrier();
  STAMP(9);
  V_READ(3, 0);
  V_READ(3, 1);
  V_READ(3, 2);
  V_READ(3, 3);
  SBAR();
  PV_MMA(0);
  PV_MMA(2);
  SBAR();
  const auto wo_rs = __builtin_amdgcn_make_buffer_rsrc((void*)wo, 0, HQ * DH * DM * 2, 0x00020000);
  const int wo_voff = (kvh * 512 + 8 * q) * 1024 + (hh * 128 + 8 * r16) * 2;
  u32x4 w[QD][8];
  if (u == 0) {
#pragma unroll
    for (int d = 0; d < QD; ++d)
#pragma unroll
      for (int jj = 0; jj < 8; ++jj)
        w[d][jj] = __builtin_amdgcn_raw_buffer_load_b128(wo_rs, wo_voff, (32 * ((d + rot) & 15) + jj) * 1024, 0);
  }
  SBAR();
  wg_barrier();
  STAMP(10);
  {
    char* xb_ = smem + wid * 8192;
#pragma unroll
    for (int c = 0; c < 2; ++c) {
#pragma unroll
      for (int g = 0; g < 4; ++g) {
        f32x4 v;
        if (u == 0) v = f32x4{o[2 + c][4 * g], o[2 + c][4 * g + 1], o[2 + c][4 * g + 2], o[2 + c][4 * g + 3]};
        else v = f32x4{o[c][4 * g], o[c][4 * g + 1], o[c][4 * g + 2], o[c][4 * g + 3]};
        *reinterpret_cast<f32x4*>(xb_ + (c * 4 + g) * 1024 + lane * 16) = v;
      }
    }
    const f32x2 ml2 = *reinterpret_cast<const f32x2*>(mlp + (wid ^ 4) * 256 + r * 8);
    const float mm = fmaxf(m, ml2[0]);
    const float e1 = __builtin_amdgcn_exp2f((m - mm) * CS), e2 = __builtin_amdgcn_exp2f((ml2[0] - mm) * CS);
    const float inv = 1.0f / (l * e1 + ml2[1] * e2);
    const float f1 = e1 * inv, f2 = e2 * inv;
    wg_barrier();
    const char* pb = smem + (wid ^ 4) * 8192;
    char* of = smem + QX_OFF + (r >> 4) * 1024 + (r & 15) * 16 + 8 * h;
    f32x4 pv[8];
#pragma unroll
    for (int c = 0; c < 8; ++c) pv[c] = *reinterpret_cast<const f32x4*>(pb + c * 1024 + lane * 16);
    SBAR();
#pragma unroll
    for (int c = 0; c < 2; ++c) {
      const int dt = 2 * u + c;
#pragma unroll
      for (int g = 0; g < 4; ++g) {
        f32x4 ov;
        if (u == 0) ov = f32x4{o[c][4 * g], o[c][4 * g + 1], o[c][4 * g + 2], o[c][4 * g + 3]};
        else ov = f32x4{o[2 + c][4 * g], o[2 + c][4 * g + 1], o[2 + c][4 * g + 2], o[2 + c][4 * g + 3]};
        const f32x4 p2 = pv[c * 4 + g];
        u32x2 v;
        v[0] = pk2(ov[0] * f1 + p2[0] * f2, ov[1] * f1 + p2[1] * f2);
        v[1] = pk2(ov[2] * f1 + p2[2] * f2, ov[3] * f1 + p2[3] * f2);
        *reinterpret_cast<u32x2*>(of + (hh * 4 + dt) * 2048 + g * 256) = v;
      }
    }
  }
  if (u == 1) {
    asm volatile("s_waitcnt vmcnt(0)" ::: "memory");
    wg_barrier();
    return;
  }
  {
    wg_barrier();
    STAMP(11);
    f32x4 acc[8][2];
#pragma unroll
    for (int i = 0; i < 8; ++i) { acc[i][0] = f32x4{}; acc[i][1] = f32x4{}; }
    const char* of = smem + QX_OFF + lane * 16;
#define O_STEP(S, BUF, LD)                                                                              \
  do {                                                                                                  \
    SBAR();                                                                                             \
    const bf16x8 b0 = *reinterpret_cast<const bf16x8*>(of + (((S) + rot) & 15) * 2048);                 \
    const bf16x8 b1 = *reinterpret_cast<const bf16x8*>(of + (((S) + rot) & 15) * 2048 + 1024);          \
    PROJ_STEP(w[BUF], b0, b1, if (LD) {                                                                 \
      _Pragma("unroll") for (int jj = 0; jj < 8; ++jj)                                                  \
        w[BUF][jj] = __builtin_amdgcn_raw_buffer_load_b128(wo_rs, wo_voff, (32 * (((S) + 2 + rot) & 15) + jj) * 1024, 0); \
    });                                                                                                 \
  } while (0)
#pragma unroll 1
    for (int it = 0; it < 7; ++it) {
      O_STEP(2 * it, 0, true);
      O_STEP(2 * it + 1, 1, true);
    }
    O_STEP(14, 0, false);
    O_STEP(15, 1, false);
    STAMP(12);
    char* pp = ows + ((size_t)((kvh * NB + b) * 4 + hh)) * 8192 + lane * 16;
    const auto p_rs = __builtin_amdgcn_make_buffer_rsrc((void*)ows, 0, 8 << 20, 0x00020000);
    const int p_voff = ((kvh * NB + b) * 4 + hh) * 8192 + lane * 16;
#pragma unroll
    for (int T = 0; T < 2; ++T)
#pragma unroll
      for (int g = 0; g < 4; ++g) {
        f32x4 v0 = {acc[0][T][g], acc[1][T][g], acc[2][T][g], acc[3][T][g]};
        f32x4 v1 = {acc[4][T][g], acc[5][T][g], acc[6][T][g], acc[7][T][g]};
        u32x4 pk = {pk2(v0[0], v0[1]), pk2(v0[2], v0[3]), pk2(v1[0], v1[1]), pk2(v1[2], v1[3])};
        __builtin_amdgcn_raw_buffer_store_b128(pk, p_rs, p_voff, (T * 4 + g) * 1024, ST_AUX);
      }
    STAMP(13);
  }
}

#define RT 256
__global__ __launch_bounds__(RT) void reduce_kernel(const char* __restrict__ ows, __hip_bfloat16* __restrict__ out) {
  const int gid = blockIdx.x * RT + threadIdx.x;
  const int lane = gid & 63, tg = (gid >> 6) & 7, bc = gid >> 9;
  const char* pp = ows + (size_t)bc * 8192 + tg * 1024 + lane * 16;
  u32x4 v[8];
#pragma unroll
  for (int k = 0; k < 8; ++k) v[k] = *reinterpret_cast<const u32x4*>(pp + (size_t)k * (NB * 4 * 8192));
  f32x4 a = {0.f, 0.f, 0.f, 0.f}, c = {0.f, 0.f, 0.f, 0.f};
#pragma unroll
  for (int k = 0; k < 8; ++k) {
    a[0] += __uint_as_float(v[k][0] << 16); a[1] += __uint_as_float(v[k][0] & 0xffff0000u);
    a[2] += __uint_as_float(v[k][1] << 16); a[3] += __uint_as_float(v[k][1] & 0xffff0000u);
    c[0] += __uint_as_float(v[k][2] << 16); c[1] += __uint_as_float(v[k][2] & 0xffff0000u);
    c[2] += __uint_as_float(v[k][3] << 16); c[3] += __uint_as_float(v[k][3] & 0xffff0000u);
  }
  const int b = bc >> 2, cg = bc & 3, T = tg >> 2, g = tg & 3, r16 = lane & 15, q = lane >> 4;
  u32x4 o = {pk2(a[0], a[1]), pk2(a[2], a[3]), pk2(c[0], c[1]), pk2(c[2], c[3])};
  *reinterpret_cast<u32x4*>(out + (size_t)(b * SQ + 16 * T + r16) * DM + 128 * cg + 32 * q + 8 * g) = o;
}

extern "C" void kernel_launch(void* const* d_in, const int* in_sizes, int n_in, void* d_out, int out_size,
                              void* d_ws, size_t ws_size, hipStream_t stream) {
  const __hip_bfloat16* x = (const __hip_bfloat16*)d_in[0];
  const __hip_bfloat16* kc = (const __hip_bfloat16*)d_in[1];
  const __hip_bfloat16* vc = (const __hip_bfloat16*)d_in[2];
  const __hip_bfloat16* wq = (const __hip_bfloat16*)d_in[3];
  const __hip_bfloat16* wo = (const __hip_bfloat16*)d_in[4];
  (void)hipFuncSetAttribute((const void*)attn_kernel, hipFuncAttributeMaxDynamicSharedMemorySize, LDS_BYTES);
  attn_kernel<<<256, 512, LDS_BYTES, stream>>>(x, kc, vc, wq, wo, (char*)d_ws);
  reduce_kernel<<<65536 / RT, RT, 0, stream>>>((const char*)d_ws, (__hip_bfloat16*)d_out);
}
```
